```python
import torch

def _softmax(x, axis=-1):
    return torch.softmax(x, dim=axis)
def _gelu(x):
    return torch.nn.functional.gelu(x, approximate="tanh")
def _silu(x):
    return torch.nn.functional.silu(x)
def _sigmoid(x):
    return torch.sigmoid(x)
def _layernorm(x, axis=-1, eps=1e-5):
    m = x.mean(dim=axis, keepdim=True)
    v = x.var(dim=axis, keepdim=True, unbiased=False)
    return (x - m) / torch.sqrt(v + eps)
def _scatter_add(tgt, idx, src, axis=0):
    return tgt.clone().index_add_(axis, idx, src)
def _segment_sum(src, seg, n):
    out = torch.zeros((n,) + src.shape[1:], dtype=src.dtype)
    return out.index_add_(0, seg, src)
def _pad(x, pw):
    flat = tuple(v for pair in reversed(pw) for v in pair)
    return torch.nn.functional.pad(x, flat)

kernel_name = 'interleave_layers__mha_block__glu_gated_reglu__prompt'

def setup_inputs():

    _w = (lambda a: (lambda t: t.to(torch.bfloat16) if t.is_floating_point() else t)(torch.as_tensor(a)))
    def _A_setup():
        B, S, D, H = 1, 2048, 640, 32
        return {
            "x": _w((lambda *s: torch.randn(*s, dtype=torch.bfloat16) * 0.05)(B, S, D)),
            "wqkv": _w((lambda *s: torch.randn(*s, dtype=torch.bfloat16) * 0.05)(D, 3 * D)),
            "wo": _w((lambda *s: torch.randn(*s, dtype=torch.bfloat16) * 0.05)(D, D)),
        }
    _A_in = _A_setup()

    def _B_setup():
        B, S, D, F = 1, 2048, 640, 1536
        return {
            "x": _w((lambda *s: torch.randn(*s, dtype=torch.bfloat16) * 0.05)(B, S, D)),
            "W_gate": _w((lambda *s: torch.randn(*s, dtype=torch.bfloat16) * 0.8)(D, F) * (2.0 / D) ** 0.5),
            "W_up": _w((lambda *s: torch.randn(*s, dtype=torch.bfloat16) * 0.8)(D, F) * (2.0 / D) ** 0.5),
            "W_down": _w((lambda *s: torch.randn(*s, dtype=torch.bfloat16) * 0.8)(F, D) * (1.0 / F) ** 0.5),
        }
    _B_in = _B_setup()

    return {
        "A_x": _A_in["x"],
        "A_wqkv": _A_in["wqkv"],
        "A_wo": _A_in["wo"],
        "B_x": _B_in["x"],
        "B_W_gate": _B_in["W_gate"],
        "B_W_up": _B_in["W_up"],
        "B_W_down": _B_in["W_down"],
    }

def reference(A_x, A_wqkv, A_wo, B_x, B_W_gate, B_W_up, B_W_down):
    def _A_ref(x, wqkv, wo):
        B, S, D = x.shape
        H = 32
        Dh = D // H
        qkv = (lambda x, *d: x.permute(*d))((x @ wqkv).reshape(B, S, 3, H, Dh), 2, 0, 3, 1, 4)
        q, k, v = qkv[0], qkv[1], qkv[2]
        scores = torch.einsum("bhqd,bhkd->bhqk", q, k) * (Dh ** -0.5)
        p = _softmax(scores, axis=-1)
        o = torch.einsum("bhqk,bhkd->bhqd", p, v)
        return (lambda x, *d: x.permute(*d))(o, 0, 2, 1, 3).reshape(B, S, D) @ wo

    def _B_ref(x, W_gate, W_up, W_down):
        gate = torch.einsum("bsd,df->bsf", x, W_gate)
        up = torch.einsum("bsd,df->bsf", x, W_up)
        gate = gate * (lambda x, dt: x.to(dt))(gate > 0, gate.dtype)
        h = gate * up
        return torch.einsum("bsf,fd->bsd", h, W_down)

    x = A_x
    x = x + _A_ref(_layernorm(x, axis=-1), A_wqkv, A_wo)
    x = x + _B_ref(_layernorm(x, axis=-1), B_W_gate, B_W_up, B_W_down)
    x = x + _A_ref(_layernorm(x, axis=-1), A_wqkv, A_wo)
    x = x + _B_ref(_layernorm(x, axis=-1), B_W_gate, B_W_up, B_W_down)
    return x
```

```cpp
#include <hip/hip_runtime.h>
#include <hip/hip_bf16.h>
#include <stdint.h>

typedef short bf16x8 __attribute__((ext_vector_type(8)));
typedef short s16x4 __attribute__((ext_vector_type(4)));
typedef float f32x16 __attribute__((ext_vector_type(16)));
typedef float f32x4 __attribute__((ext_vector_type(4)));
typedef float f32x2 __attribute__((ext_vector_type(2)));
typedef unsigned u32x4 __attribute__((ext_vector_type(4)));
typedef unsigned u32x2 __attribute__((ext_vector_type(2)));
typedef unsigned short u16;
typedef const __attribute__((address_space(1))) void* gptr_t;
typedef __attribute__((address_space(3))) void* lptr_t;
typedef __attribute__((address_space(3))) char* lds_t;

constexpr int S_ = 2048, D_ = 640, H_ = 32, DH = 20, F_ = 1536;
constexpr float LN_EPS = 1e-5f;
constexpr float QSCALE = 0.22360679774997896f * 1.4426950408889634f;

constexpr size_t XT_BYTES = (size_t)S_ * D_ * 2;
constexpr size_t HT_BYTES = (size_t)S_ * F_ * 2;
constexpr size_t Q_BYTES = (size_t)H_ * 64 * 1536;
constexpr size_t KV_BYTES = (size_t)H_ * 64 * 3072;
constexpr size_t ST_BYTES = (size_t)8 * S_ * 8;
constexpr size_t WS_WOT = 0;
constexpr size_t WS_WDT = WS_WOT + (size_t)D_ * D_ * 2;
constexpr size_t WS_CSQ = WS_WDT + (size_t)F_ * D_ * 2;
constexpr size_t WS_CSG = WS_CSQ + 16384;
constexpr size_t WS_ST = WS_CSG + 16384;
constexpr size_t WS_L0 = WS_ST + 4 * ST_BYTES;
constexpr size_t L_Q = 0, L_KV = L_Q + Q_BYTES, L_O = L_KV + KV_BYTES, L_X1 = L_O + XT_BYTES, L_H = L_X1 + XT_BYTES,
                 L_X2 = L_H + HT_BYTES, L_BYTES = L_X2 + XT_BYTES;

#define WAIT_V(n) asm volatile("s_waitcnt vmcnt(%0)" ::"i"(n) : "memory")
#define BAR_L() asm volatile("s_waitcnt lgkmcnt(0)\n\ts_barrier" ::: "memory")

__device__ __forceinline__ void glds16(const void* g, unsigned lds_off, char* smem) {
    __builtin_amdgcn_global_load_lds((gptr_t)g, (lptr_t)(smem + lds_off), 16, 0, 0);
}
__device__ __forceinline__ unsigned cvtpk(float lo, float hi) {
    typedef __bf16 bf2 __attribute__((ext_vector_type(2)));
    f32x2 v = {lo, hi};
    bf2 r = __builtin_convertvector(v, bf2);
    return __builtin_bit_cast(unsigned, r);
}
__device__ __forceinline__ float bflo(unsigned u) { return __uint_as_float(u << 16); }
__device__ __forceinline__ float bfhi(unsigned u) { return __uint_as_float(u & 0xffff0000u); }
__device__ __forceinline__ bf16x8 ldsr128(const char* p) { return *(const bf16x8*)p; }
__device__ __forceinline__ s16x4 ldstr(const char* p) {
    return __builtin_bit_cast(s16x4, __builtin_amdgcn_ds_read_tr16_b64_v4i16((__attribute__((address_space(3))) s16x4*)p));
}
__device__ __forceinline__ bf16x8 cat8(s16x4 a, s16x4 b) { return __builtin_shufflevector(a, b, 0, 1, 2, 3, 4, 5, 6, 7); }
#define MFMA32(a, b, c) __builtin_amdgcn_mfma_f32_32x32x16_bf16((a), (b), (c), 0, 0, 0)
#define MFMA16(a, b, c) __builtin_amdgcn_mfma_f32_16x16x32_bf16((a), (b), (c), 0, 0, 0)

__device__ __forceinline__ float xsum16_32(float x) {
    auto s = __builtin_amdgcn_permlane16_swap(__float_as_uint(x), __float_as_uint(x), false, false);
    x = __uint_as_float(s[0]) + __uint_as_float(s[1]);
    auto t = __builtin_amdgcn_permlane32_swap(__float_as_uint(x), __float_as_uint(x), false, false);
    return __uint_as_float(t[0]) + __uint_as_float(t[1]);
}

__device__ __forceinline__ f32x2 ln_combine(const char* p, int tok, int ntok) {
    f32x2 v[8];
    float sum = 0.f;
#pragma unroll
    for (int c = 0; c < 8; ++c) { v[c] = *(const f32x2*)(p + (c * ntok + tok) * 8); sum += v[c][0]; }
    const float mean = sum * (1.f / 640.f);
    float m2 = 0.f;
#pragma unroll
    for (int c = 0; c < 8; ++c) { const float d = v[c][0] * (1.f / 80.f) - mean; m2 += v[c][1] + 80.f * d * d; }
    const float rstd = __builtin_amdgcn_rsqf(m2 * (1.f / 640.f) + LN_EPS);
    f32x2 r = {rstd, -mean * rstd};
    return r;
}

constexpr int PREP_TILE_BLKS = (640 + 1536) * 640 / 8 / 256;
constexpr int PREP_CS_BLKS = 30 + 48;
constexpr int PREP_ST_BLKS = 2048 * 8 / 256;
__global__ __launch_bounds__(256) void prep_kernel(const u16* __restrict__ x0, const u16* __restrict__ wqkv, const u16* __restrict__ wo,
                                                   const u16* __restrict__ wg, const u16* __restrict__ wu, const u16* __restrict__ wd,
                                                   char* __restrict__ ws) {
    __shared__ float red[32 * 64];
    int b = blockIdx.x;
    const int t = threadIdx.x;
    if (b < PREP_CS_BLKS) {
        const u16* w; int ld, c0;
        if (b < 30) { w = wqkv; ld = 1920; c0 = b * 64; }
        else if (b < 54) { w = wg; ld = 1536; c0 = (b - 30) * 64; }
        else { w = wu; ld = 1536; c0 = (b - 54) * 64; }
        const int cg = t & 7, kr = t >> 3;
        float a[8] = {0, 0, 0, 0, 0, 0, 0, 0};
#pragma unroll 4
        for (int i = 0; i < 20; ++i) {
            const u32x4 v = *(const u32x4*)(w + (size_t)(kr * 20 + i) * ld + c0 + cg * 8);
#pragma unroll
            for (int j = 0; j < 4; ++j) { a[2 * j] += bflo(v[j]); a[2 * j + 1] += bfhi(v[j]); }
        }
#pragma unroll
        for (int j = 0; j < 8; ++j) red[kr * 64 + cg * 8 + j] = a[j];
        __syncthreads();
        if (t < 64) {
            float s = 0.f;
            for (int i = 0; i < 32; ++i) s += red[i * 64 + t];
            const int c = c0 + t;
            if (b < 30) ((float*)(ws + WS_CSQ))[c] = s;
            else ((float*)(ws + WS_CSG))[(c >> 4) * 32 + (b < 54 ? 0 : 16) + (c & 15)] = s;
        }
        return;
    }
    b -= PREP_CS_BLKS;
    if (b < PREP_ST_BLKS) {
        const int id = b * 256 + t, row = id >> 3, ct = id & 7;
        float v[80];
        float s = 0.f;
#pragma unroll
        for (int i = 0; i < 10; ++i) {
            const u32x4 u = *(const u32x4*)(x0 + (size_t)row * 640 + ct * 80 + i * 8);
#pragma unroll
            for (int j = 0; j < 4; ++j) { v[i * 8 + 2 * j] = bflo(u[j]); v[i * 8 + 2 * j + 1] = bfhi(u[j]); }
        }
#pragma unroll
        for (int i = 0; i < 80; ++i) s += v[i];
        const float mp = s * (1.f / 80.f);
        float m2 = 0.f;
#pragma unroll
        for (int i = 0; i < 80; ++i) { const float d = v[i] - mp; m2 += d * d; }
        f32x2 r = {s, m2};
        *(f32x2*)(ws + WS_ST + ((size_t)ct * S_ + row) * 8) = r;
        return;
    }
    b -= PREP_ST_BLKS;
    {
        int id = b * 256 + t;
        const int lane = id & 63;
        int fr = id >> 6;
        const int g = lane >> 4, c = lane & 15;
        u16 e[8];
        char* dst;
        if (fr < 8 * 20 * 5) {
            const int nt = fr % 5, kk = (fr / 5) % 20, ct = fr / 100;
            const int n = ct * 80 + nt * 16 + c;
#pragma unroll
            for (int j = 0; j < 8; ++j) e[j] = wo[(size_t)(kk * 32 + g * 8 + j) * 640 + n];
            dst = ws + WS_WOT + (size_t)id * 16;
        } else {
            fr -= 800;
            const int nt = fr % 5, kk = (fr / 5) % 48, ct = fr / 240;
            const int n = ct * 80 + nt * 16 + c;
#pragma unroll
            for (int j = 0; j < 8; ++j) e[j] = wd[(size_t)(kk * 32 + (g >> 1) * 16 + (j >> 2) * 8 + (g & 1) * 4 + (j & 3)) * 640 + n];
            dst = ws + WS_WDT + ((size_t)fr * 64 + lane) * 16;
        }
        u32x4 o;
#pragma unroll
        for (int j = 0; j < 4; ++j) o[j] = (unsigned)e[2 * j] | ((unsigned)e[2 * j + 1] << 16);
        *(u32x4*)dst = o;
    }
}

constexpr int QKV_SLOT = 32768, QKV_NS = 4, QKV_NST = 10;
constexpr int QKV_ST_OFF = QKV_NS * QKV_SLOT, QKV_CS_OFF = QKV_ST_OFF + 8192, QKV_S2_OFF = QKV_CS_OFF + 1024, QKV_LDS = QKV_S2_OFF + 1024;

template <bool XROW, bool ISV>
__device__ __forceinline__ void qkv_body(char* smem, const u16* __restrict__ X, const u16* __restrict__ W, const char* __restrict__ cs,
                                         const char* __restrict__ stats, char* __restrict__ Qb, char* __restrict__ KVb, int tm, int tn) {
    const int tid = threadIdx.x, lane = tid & 63;
    const int w = __builtin_amdgcn_readfirstlane(tid >> 6);
    const int wm = w & 1, wn = w >> 1;
    const int r32 = lane & 31, hh = lane >> 5;
    const char* xsrc;
    if (XROW) xsrc = (const char*)X + ((size_t)(tm * 128 + w * 32 + (lane >> 2)) * 640 + (((lane & 3) ^ ((lane >> 4) & 3)) * 8)) * 2;
    else xsrc = (const char*)X + ((size_t)(tm * 4 + w) * 40 * 64 + lane) * 16;
    const char* wsrc = (const char*)W + ((size_t)(w * 16 + ((lane >> 2) & 3)) * 1920 + tn * 128 + (lane >> 4) * 32 + (lane & 3) * 8) * 2;
    auto stage = [&](int s) {
        const unsigned slot = (s % QKV_NS) * QKV_SLOT;
#pragma unroll
        for (int i = 0; i < 4; ++i) {
            if (XROW) glds16(xsrc + s * 128 + (i >> 1) * 64 + (i & 1) * (16 * 1280), slot + (i >> 1) * 8192 + w * 2048 + (i & 1) * 1024, smem);
            else glds16(xsrc + (s * 4 + i) * 1024, slot + (w * 4 + i) * 1024, smem);
            glds16(wsrc + (size_t)(s * 64 + i * 4) * 3840, slot + 16384 + (w * 4 + i) * 1024, smem);
        }
    };
#pragma unroll
    for (int i = 0; i < 2; ++i) glds16(stats + ((size_t)(w * 2 + i) * S_ + tm * 128) * 8 + lane * 16, QKV_ST_OFF + (w * 2 + i) * 1024, smem);
    if (w == 0) glds16(cs + tn * 512 + lane * 16, QKV_CS_OFF, smem);
    stage(0); stage(1); stage(2);

    int xoff0, xoff1;
    if (XROW) {
        const int sw = (r32 >> 2) & 3;
        xoff0 = wm * 4096 + r32 * 64 + ((hh ^ sw) * 16);
        xoff1 = wm * 4096 + r32 * 64 + (((2 + hh) ^ sw) * 16);
    } else {
        xoff0 = wm * 8192 + lane * 16;
        xoff1 = xoff0 + 1024;
    }
    const int woff = 16384 + (lane >> 5) * 2048 + wn * 512 + ((lane >> 2) & 3) * 64 + ((lane >> 4) & 1) * 32 + (lane & 3) * 8;

    f32x16 acc[2][2];
#pragma unroll
    for (int i = 0; i < 2; ++i)
#pragma unroll
        for (int j = 0; j < 2; ++j)
#pragma unroll
            for (int r = 0; r < 16; ++r) acc[i][j][r] = 0.f;

#pragma unroll
    for (int s = 0; s < QKV_NST; ++s) {
        constexpr int D = 8;
        const int rem = QKV_NST - 1 - s;
        if (rem >= 2) WAIT_V(2 * D); else if (rem == 1) WAIT_V(D); else WAIT_V(0);
        BAR_L();
        if (s + 3 < QKV_NST) stage(s + 3);
        if (s == 1 && tid < 128) *(f32x2*)(smem + QKV_S2_OFF + tid * 8) = ln_combine(smem + QKV_ST_OFF, tid, 128);
        const char* sl = smem + (s % QKV_NS) * QKV_SLOT;
#pragma unroll
        for (int ks = 0; ks < 4; ++ks) {
            bf16x8 xf[2], wf[2];
#pragma unroll
            for (int j = 0; j < 2; ++j) {
                if (XROW) xf[j] = ldsr128(sl + ((ks & 1) ? xoff1 : xoff0) + (ks >> 1) * 8192 + j * 2048);
                else xf[j] = ldsr128(sl + xoff0 + ks * 1024 + j * 4096);
            }
#pragma unroll
            for (int i = 0; i < 2; ++i) wf[i] = cat8(ldstr(sl + woff + ks * 4096 + i * 256), ldstr(sl + woff + ks * 4096 + i * 256 + 1024));
#pragma unroll
            for (int i = 0; i < 2; ++i)
#pragma unroll
                for (int j = 0; j < 2; ++j) acc[i][j] = ISV ? MFMA32(xf[j], wf[i], acc[i][j]) : MFMA32(wf[i], xf[j], acc[i][j]);
        }
    }
    const float* csl = (const float*)(smem + QKV_CS_OFF);
    if (!ISV) {
        const bool isq = tn < 5;
        const float sc = isq ? QSCALE : 1.f;
        const int cbase = (isq ? tn : tn - 5) * 128;
        char* base = isq ? Qb : KVb;
        const int tstride = isq ? 1536 : 3072;
#pragma unroll
        for (int j = 0; j < 2; ++j) {
            f32x2 st = *(const f32x2*)(smem + QKV_S2_OFF + (wm * 64 + j * 32 + r32) * 8);
            st[0] *= sc; st[1] *= sc;
            const int tile = tm * 4 + wm * 2 + j;
#pragma unroll
            for (int i = 0; i < 2; ++i)
#pragma unroll
                for (int g = 0; g < 4; ++g) {
                    const int nl = (wn * 2 + i) * 32 + g * 8 + hh * 4;
                    const f32x4 c4 = *(const f32x4*)(csl + nl);
                    const int c = cbase + nl, head = c / 20, d0 = c - head * 20;
                    float v[4];
#pragma unroll
                    for (int e = 0; e < 4; ++e) v[e] = acc[i][j][g * 4 + e] * st[0] + c4[e] * st[1];
                    u32x2 o = {cvtpk(v[0], v[1]), cvtpk(v[2], v[3])};
                    char* p = base + (size_t)(head * 64 + tile) * tstride + (d0 >> 3) * 512 + r32 * 16 + (d0 & 7) * 2;
                    *(u32x2*)p = o;
                    if (!isq && d0 == 16) { u32x2 one = {0x3f80u, 0u}; *(u32x2*)(p + 8) = one; }
                }
        }
    } else {
#pragma unroll
        for (int i = 0; i < 2; ++i) {
            const int nl = (wn * 2 + i) * 32 + r32;
            const float c1 = csl[nl];
            const int c = (tn - 10) * 128 + nl, head = c / 20, d = c - head * 20;
#pragma unroll
            for (int j = 0; j < 2; ++j) {
                const int tile = tm * 4 + wm * 2 + j;
                char* p = KVb + (size_t)(head * 64 + tile) * 3072 + 1536 + hh * 384 + d * 16;
#pragma unroll
                for (int s2 = 0; s2 < 2; ++s2) {
                    float v[8];
#pragma unroll
                    for (int q = 0; q < 2; ++q) {
                        const char* sp = smem + QKV_S2_OFF + (wm * 64 + j * 32 + (s2 * 2 + q) * 8 + hh * 4) * 8;
                        const f32x4 a = *(const f32x4*)sp, b = *(const f32x4*)(sp + 16);
                        const int r = (s2 * 2 + q) * 4;
                        v[q * 4 + 0] = acc[i][j][r + 0] * a[0] + c1 * a[1];
                        v[q * 4 + 1] = acc[i][j][r + 1] * a[2] + c1 * a[3];
                        v[q * 4 + 2] = acc[i][j][r + 2] * b[0] + c1 * b[1];
                        v[q * 4 + 3] = acc[i][j][r + 3] * b[2] + c1 * b[3];
                    }
                    u32x4 o = {cvtpk(v[0], v[1]), cvtpk(v[2], v[3]), cvtpk(v[4], v[5]), cvtpk(v[6], v[7])};
                    *(u32x4*)(p + s2 * 768) = o;
                    if (d == 19) { u32x4 one = {0x3f803f80u, 0x3f803f80u, 0x3f803f80u, 0x3f803f80u}; *(u32x4*)(p + s2 * 768 + 16) = one; }
                }
            }
        }
    }
}

template <bool XROW>
__global__ __launch_bounds__(256, 1) void qkv_kernel(const u16* __restrict__ X, const u16* __restrict__ W, const char* __restrict__ cs,
                                                     const char* __restrict__ stats, char* __restrict__ Qb, char* __restrict__ KVb) {
    __shared__ __attribute__((aligned(1024))) char smem[QKV_LDS];
    const int b = blockIdx.x, xcd = b & 7, j = b >> 3;
    const int id = (xcd & 3) * 30 + j;
    const int tm = (xcd >> 2) * 8 + (id & 7), tn = id >> 3;
    if (tn >= 10) qkv_body<XROW, true>(smem, X, W, cs, stats, Qb, KVb, tm, tn);
    else qkv_body<XROW, false>(smem, X, W, cs, stats, Qb, KVb, tm, tn);
}

constexpr int AT_CH = 8, AT_SLOT = AT_CH * 3072, AT_NS = 4, AT_NCH = 64 / AT_CH;

__device__ __forceinline__ float rowmax16(const f32x16& s) {
    float a = fmaxf(fmaxf(s[0], s[1]), s[2]), b = fmaxf(fmaxf(s[3], s[4]), s[5]);
    a = fmaxf(fmaxf(a, s[6]), s[7]); b = fmaxf(fmaxf(b, s[8]), s[9]);
    a = fmaxf(fmaxf(a, s[10]), s[11]); b = fmaxf(fmaxf(b, s[12]), s[13]);
    a = fmaxf(fmaxf(a, s[14]), s[15]);
    a = fmaxf(a, b);
    auto rr = __builtin_amdgcn_permlane32_swap(__float_as_uint(a), __float_as_uint(a), false, false);
    return fmaxf(__uint_as_float(rr[0]), __uint_as_float(rr[1]));
}
__device__ __forceinline__ void exp_pack(const f32x16& s, bf16x8& p0, bf16x8& p1) {
    u32x4 a, b;
#pragma unroll
    for (int i = 0; i < 4; ++i) {
        a[i] = cvtpk(__builtin_amdgcn_exp2f(s[2 * i]), __builtin_amdgcn_exp2f(s[2 * i + 1]));
        b[i] = cvtpk(__builtin_amdgcn_exp2f(s[8 + 2 * i]), __builtin_amdgcn_exp2f(s[8 + 2 * i + 1]));
    }
    p0 = __builtin_bit_cast(bf16x8, a); p1 = __builtin_bit_cast(bf16x8, b);
}

__global__ __launch_bounds__(512, 2) void attn_kernel(const char* __restrict__ Qb, const char* __restrict__ KVb, char* __restrict__ O) {
    __shared__ __attribute__((aligned(1024))) char smem[AT_NS * AT_SLOT];
    const int tid = threadIdx.x, lane = tid & 63;
    const int w = __builtin_amdgcn_readfirstlane(tid >> 6);
    const int b = blockIdx.x, xcd = b & 7, j = b >> 3;
    const int head = xcd * 4 + (j & 3), qblk = j >> 2;
    const int r32 = lane & 31, hh = lane >> 5;
    const int qtile = qblk * 8 + w;
    const char* kv = KVb + (size_t)head * 64 * 3072;
    auto chunk = [&](int c) {
#pragma unroll
        for (int i = 0; i < 3; ++i) glds16(kv + (size_t)c * AT_SLOT + (w * 3 + i) * 1024 + lane * 16, (c % AT_NS) * AT_SLOT + (w * 3 + i) * 1024, smem);
    };
    const char* qp = Qb + (size_t)(head * 64 + qtile) * 1536;
    const bf16x8 q0 = *(const bf16x8*)(qp + lane * 16);
    u32x2 q1lo = *(const u32x2*)(qp + 1024 + r32 * 16);
    asm volatile("s_waitcnt vmcnt(0)" : "+v"(q1lo) :: "memory");
    chunk(0); chunk(1);
    u32x4 q1u = {hh ? 0u : q1lo[0], hh ? 0u : q1lo[1], 0u, 0u};
    const int koff0 = lane * 16, koff1 = 1024 + r32 * 16;
    const int vrow = r32 <= 20 ? r32 : (r32 < 28 ? 20 : 16);
    const int voff = 1536 + hh * 384 + vrow * 16;
    f32x16 zero;
#pragma unroll
    for (int r = 0; r < 16; ++r) zero[r] = 0.f;
    f32x16 o = zero;

    WAIT_V(3);
    BAR_L();
    chunk(2);
    f32x16 sn;
    {
        const bf16x8 k0 = ldsr128(smem + koff0), k1 = ldsr128(smem + koff1);
        sn = MFMA32(k0, q0, zero);
        sn = MFMA32(k1, __builtin_bit_cast(bf16x8, q1u), sn);
        const float m = rowmax16(sn);
        const unsigned mb = cvtpk(-m, 0.f) & 0xffffu;
        q1u[2] = hh ? 0u : mb;
        sn = MFMA32(k0, q0, zero);
        sn = MFMA32(k1, __builtin_bit_cast(bf16x8, q1u), sn);
    }
    const bf16x8 q1 = __builtin_bit_cast(bf16x8, q1u);
#pragma unroll 1
    for (int c = 0; c < AT_NCH; ++c) {
        const char* sl = smem + (c % AT_NS) * AT_SLOT;
        const char* nsl = smem + ((c + 1) % AT_NS) * AT_SLOT;
#pragma unroll
        for (int t = 0; t < AT_CH; ++t) {
            const f32x16 s = sn;
            if (t == AT_CH - 1) {
                if (c + 1 < AT_NCH) {
                    if (c + 2 < AT_NCH) WAIT_V(3); else WAIT_V(0);
                    BAR_L();
                    if (c + 3 < AT_NCH) chunk(c + 3);
                    const bf16x8 k0 = ldsr128(nsl + koff0), k1 = ldsr128(nsl + koff1);
                    sn = MFMA32(k0, q0, zero);
                    sn = MFMA32(k1, q1, sn);
                }
            } else {
                const bf16x8 k0 = ldsr128(sl + (t + 1) * 3072 + koff0), k1 = ldsr128(sl + (t + 1) * 3072 + koff1);
                sn = MFMA32(k0, q0, zero);
                sn = MFMA32(k1, q1, sn);
            }
            const bf16x8 v0 = ldsr128(sl + t * 3072 + voff), v1 = ldsr128(sl + t * 3072 + voff + 768);
            bf16x8 p0, p1;
            exp_pack(s, p0, p1);
            o = MFMA32(v0, p0, o);
            o = MFMA32(v1, p1, o);
        }
    }
    float l;
    {
        auto rr = __builtin_amdgcn_permlane32_swap(__float_as_uint(o[8]), __float_as_uint(o[8]), false, false);
        l = __uint_as_float(rr[1]);
    }
    bool bad = !(l < 1.8446744073709552e19f);
#pragma unroll
    for (int r = 0; r < 12; ++r) if (r < 8 || !hh) bad |= !(fabsf(o[r]) < 3.0e38f);
    if (__any(bad)) {
        u32x4 qz = q1u; qz[2] = 0u;
        const bf16x8 q1z = __builtin_bit_cast(bf16x8, qz);
        float m = -INFINITY;
        o = zero;
#pragma unroll 1
        for (int t = 0; t < 64; ++t) {
            const char* tp = kv + (size_t)t * 3072;
            const bf16x8 k0 = *(const bf16x8*)(tp + koff0), k1 = *(const bf16x8*)(tp + koff1);
            const bf16x8 v0 = *(const bf16x8*)(tp + voff), v1 = *(const bf16x8*)(tp + voff + 768);
            f32x16 s = MFMA32(k0, q0, zero);
            s = MFMA32(k1, q1z, s);
            const float mn = fmaxf(m, rowmax16(s));
            const float alpha = __builtin_amdgcn_exp2f(m - mn);
            m = mn;
#pragma unroll
            for (int r = 0; r < 16; ++r) { s[r] -= mn; o[r] *= alpha; }
            bf16x8 p0, p1;
            exp_pack(s, p0, p1);
            o = MFMA32(v0, p0, o);
            o = MFMA32(v1, p1, o);
        }
        auto rr = __builtin_amdgcn_permlane32_swap(__float_as_uint(o[8]), __float_as_uint(o[8]), false, false);
        l = __uint_as_float(rr[1]);
    }
    const float inv = 1.f / l;
    char* ob = O + (size_t)qtile * 40 * 1024 + r32 * 16;
#pragma unroll
    for (int g = 0; g < 3; ++g) {
        if (g < 2 || !hh) {
            const int col = head * 20 + g * 8 + hh * 4;
            u32x2 v = {cvtpk(o[g * 4] * inv, o[g * 4 + 1] * inv), cvtpk(o[g * 4 + 2] * inv, o[g * 4 + 3] * inv)};
            *(u32x2*)(ob + (col >> 3) * 512 + (col & 4) * 2) = v;
        }
    }
}

constexpr int GU_SLOT = 40960, GU_NS = 3, GU_NST = 10;
constexpr int GU_ST_OFF = GU_NS * GU_SLOT, GU_CS_OFF = GU_ST_OFF + 8192, GU_S2_OFF = GU_CS_OFF + 1024, GU_LDS = GU_S2_OFF + 1024;

__global__ __launch_bounds__(256, 1) void gu_kernel(const char* __restrict__ X, const u16* __restrict__ Wg, const u16* __restrict__ Wu,
                                                    const char* __restrict__ cs, const char* __restrict__ stats, char* __restrict__ Hh) {
    __shared__ __attribute__((aligned(1024))) char smem[GU_LDS];
    const int tid = threadIdx.x, lane = tid & 63;
    const int w = __builtin_amdgcn_readfirstlane(tid >> 6);
    const int wm = w & 1, wn = w >> 1;
    const int r32 = lane & 31, hh = lane >> 5;
    const int b = blockIdx.x, xcd = b & 7, j0 = b >> 3;
    const int tm = (xcd >> 2) * 8 + (j0 & 7), tn = (xcd & 3) * 4 + (j0 >> 3);
    const char* xsrc = X + ((size_t)(tm * 4 + w) * 40 * 64 + lane) * 16;
    const char* wsrc[3];
#pragma unroll
    for (int i = 0; i < 3; ++i) {
        const int idx = i * 64 + lane, kq = idx / 96, rem = idx - kq * 96, sub = rem >> 4, kr = (rem >> 2) & 3, c = rem & 3;
        const bool up = sub >= 3;
        const u16* base = up ? Wu : Wg;
        wsrc[i] = (const char*)(base + (size_t)(w * 16 + kq * 4 + kr) * 1536 + tn * 96 + (up ? sub - 3 : sub) * 32 + (up ? (c ^ 2) : c) * 8);
    }
    auto stage = [&](int s) {
        const unsigned slot = (s % GU_NS) * GU_SLOT;
#pragma unroll
        for (int i = 0; i < 6; ++i) {
            if (i < 4) glds16(xsrc + (s * 4 + i) * 1024, slot + (w * 4 + i) * 1024, smem);
            glds16(wsrc[i % 3] + (size_t)(s * 64 + (i / 3) * 8) * 3072, slot + 16384 + (w * 6 + i) * 1024, smem);
        }
    };
#pragma unroll
    for (int i = 0; i < 2; ++i) glds16(stats + ((size_t)(w * 2 + i) * S_ + tm * 128) * 8 + lane * 16, GU_ST_OFF + (w * 2 + i) * 1024, smem);
    if (w == 0) glds16(cs + tn * 768 + lane * 16, GU_CS_OFF, smem);
    stage(0); stage(1);

    const int xoff = wm * 8192 + lane * 16;
    const int upl = (lane >> 4) & 1;
    const int wbase = 16384 + (lane >> 5) * 3072 + upl * 768 + ((lane >> 2) & 3) * 64 + (lane & 3) * 8;
    int woff[3];
#pragma unroll
    for (int i = 0; i < 3; ++i) { const int t = wn * 3 + i; woff[i] = wbase + (t >> 1) * 256 + (((t & 1) ^ upl) * 32); }

    f32x16 acc[3][2];
#pragma unroll
    for (int i = 0; i < 3; ++i)
#pragma unroll
        for (int j = 0; j < 2; ++j)
#pragma unroll
            for (int r = 0; r < 16; ++r) acc[i][j][r] = 0.f;
#pragma unroll
    for (int s = 0; s < GU_NST; ++s) {
        constexpr int D = 10;
        if (s + 1 < GU_NST) WAIT_V(D); else WAIT_V(0);
        BAR_L();
        if (s + 2 < GU_NST) stage(s + 2);
        if (s == 1 && tid < 128) *(f32x2*)(smem + GU_S2_OFF + tid * 8) = ln_combine(smem + GU_ST_OFF, tid, 128);
        const char* sl = smem + (s % GU_NS) * GU_SLOT;
#pragma unroll
        for (int ks = 0; ks < 4; ++ks) {
            bf16x8 xf[2], wf[3];
#pragma unroll
            for (int j = 0; j < 2; ++j) xf[j] = ldsr128(sl + xoff + ks * 1024 + j * 4096);
#pragma unroll
            for (int i = 0; i < 3; ++i) wf[i] = cat8(ldstr(sl + woff[i] + ks * 6144), ldstr(sl + woff[i] + ks * 6144 + 1536));
#pragma unroll
            for (int i = 0; i < 3; ++i)
#pragma unroll
                for (int j = 0; j < 2; ++j) acc[i][j] = MFMA32(wf[i], xf[j], acc[i][j]);
        }
    }
    const float* csl = (const float*)(smem + GU_CS_OFF);
#pragma unroll
    for (int j = 0; j < 2; ++j) {
        const f32x2 st = *(const f32x2*)(smem + GU_S2_OFF + (wm * 64 + j * 32 + r32) * 8);
        const int tt = tm * 4 + wm * 2 + j;
#pragma unroll
        for (int i = 0; i < 3; ++i) {
            const int t = wn * 3 + i;
            float hv[8];
#pragma unroll
            for (int g = 0; g < 2; ++g) {
                const f32x4 cg = *(const f32x4*)(csl + t * 32 + g * 8 + hh * 4), cu = *(const f32x4*)(csl + t * 32 + 16 + g * 8 + hh * 4);
#pragma unroll
                for (int e = 0; e < 4; ++e) {
                    const float ga = acc[i][j][g * 4 + e] * st[0] + cg[e] * st[1];
                    const float up = acc[i][j][8 + g * 4 + e] * st[0] + cu[e] * st[1];
                    hv[g * 4 + e] = (ga > 0.f ? ga : ga * 0.f) * up;
                }
            }
            u32x4 o = {cvtpk(hv[0], hv[1]), cvtpk(hv[2], hv[3]), cvtpk(hv[4], hv[5]), cvtpk(hv[6], hv[7])};
            *(u32x4*)(Hh + ((size_t)(tt * 96 + tn * 6 + t) * 64 + lane) * 16) = o;
        }
    }
}

constexpr int PR_SLOT = 36864, PR_NS = 4, PR_RES_OFF = PR_NS * PR_SLOT, PR_LDS = PR_RES_OFF + 10240;

template <int KTOT, bool RESROW, bool OUTROW>
__global__ __launch_bounds__(256, 1) void proj_kernel(const char* __restrict__ A, const char* __restrict__ WT, const char* __restrict__ Xold,
                                                      char* __restrict__ Xnew, char* __restrict__ stats) {
    __shared__ __attribute__((aligned(1024))) char smem[PR_LDS];
    constexpr int NST = KTOT / 128, KS16 = KTOT / 16;
    const int tid = threadIdx.x, lane = tid & 63;
    const int w = __builtin_amdgcn_readfirstlane(tid >> 6);
    const int b = blockIdx.x, xcd = b & 7, j0 = b >> 3;
    const int tm = (xcd >> 1) * 8 + (j0 & 7), tn = (xcd & 1) * 4 + (j0 >> 3);
    const char* asrc = A + ((size_t)((tm * 2 + (w >> 1)) * KS16 + (w & 1) * 4) * 64 + lane) * 16;
    const char* wsrc = WT + ((size_t)(tn * (KTOT / 32) + w) * 5 * 64 + lane) * 16;
    auto stage = [&](int s) {
        const unsigned slot = (s % PR_NS) * PR_SLOT;
#pragma unroll
        for (int i = 0; i < 5; ++i) {
            if (i < 4) glds16(asrc + (s * 8 + i) * 1024, slot + (w * 4 + i) * 1024, smem);
            glds16(wsrc + (s * 20 + i) * 1024, slot + 16384 + (w * 5 + i) * 1024, smem);
        }
    };
#pragma unroll
    for (int i = 0; i < 3; ++i) {
        const int idx = w * 3 + i;
        if (idx < 10) {
            const int tt = idx / 5, ks = idx - tt * 5;
            const char* src;
            if (RESROW) src = Xold + ((size_t)(tm * 64 + tt * 32 + (lane & 31)) * 640 + tn * 80 + ks * 16 + (lane >> 5) * 8) * 2;
            else src = Xold + ((size_t)((tm * 2 + tt) * 40 + tn * 5 + ks) * 64 + lane) * 16;
            glds16(src, PR_RES_OFF + idx * 1024, smem);
        }
    }
    stage(0); stage(1); stage(2);
    const int g = lane >> 4, c16 = lane & 15;
    const int aoff = (w >> 1) * 8192 + g * 512 + ((w & 1) * 16 + c16) * 16;
    const int woff = 16384 + lane * 16;
    f32x4 acc[5];
#pragma unroll
    for (int i = 0; i < 5; ++i) acc[i] = f32x4{0.f, 0.f, 0.f, 0.f};
#pragma unroll
    for (int s = 0; s < NST; ++s) {
        constexpr int D = 9;
        const int rem = NST - 1 - s;
        if (rem >= 2) WAIT_V(2 * D); else if (rem == 1) WAIT_V(D); else WAIT_V(0);
        BAR_L();
        if (s + 3 < NST) stage(s + 3);
        const char* sl = smem + (s % PR_NS) * PR_SLOT;
#pragma unroll
        for (int kk = 0; kk < 4; ++kk) {
            const bf16x8 af = ldsr128(sl + aoff + kk * 2048);
#pragma unroll
            for (int i = 0; i < 5; ++i) acc[i] = MFMA16(ldsr128(sl + woff + (kk * 5 + i) * 1024), af, acc[i]);
        }
    }
    const int r32 = (w & 1) * 16 + c16;
    float v[20];
    float sum = 0.f;
    u32x2 ov[5];
#pragma unroll
    for (int i = 0; i < 5; ++i) {
        const u32x2 xo = *(const u32x2*)(smem + PR_RES_OFF + ((w >> 1) * 5 + i) * 1024 + (g >> 1) * 512 + r32 * 16 + (g & 1) * 8);
        ov[i][0] = cvtpk(acc[i][0] + bflo(xo[0]), acc[i][1] + bfhi(xo[0]));
        ov[i][1] = cvtpk(acc[i][2] + bflo(xo[1]), acc[i][3] + bfhi(xo[1]));
        if (OUTROW) {
            *(u32x2*)(Xnew + ((size_t)(tm * 64 + w * 16 + c16) * 640 + tn * 80 + i * 16 + g * 4) * 2) = ov[i];
        } else {
            *(u32x2*)(Xnew + ((size_t)((tm * 2 + (w >> 1)) * 40 + tn * 5 + i) * 64 + (g >> 1) * 32 + r32) * 16 + (g & 1) * 8) = ov[i];
            v[i * 4 + 0] = bflo(ov[i][0]); v[i * 4 + 1] = bfhi(ov[i][0]); v[i * 4 + 2] = bflo(ov[i][1]); v[i * 4 + 3] = bfhi(ov[i][1]);
            sum += (v[i * 4] + v[i * 4 + 1]) + (v[i * 4 + 2] + v[i * 4 + 3]);
        }
    }
    if (!OUTROW) {
        sum = xsum16_32(sum);
        const float mp = sum * (1.f / 80.f);
        float m2 = 0.f;
#pragma unroll
        for (int i = 0; i < 20; ++i) { const float d = v[i] - mp; m2 += d * d; }
        m2 = xsum16_32(m2);
        if (g == 0) { f32x2 r = {sum, m2}; *(f32x2*)(stats + ((size_t)tn * S_ + tm * 64 + w * 16 + c16) * 8) = r; }
    }
}

extern "C" void kernel_launch(void* const* d_in, const int* in_sizes, int n_in, void* d_out, int out_size, void* d_ws, size_t ws_size,
                              hipStream_t stream) {
    const u16* x0 = (const u16*)d_in[0];
    const u16* wqkv = (const u16*)d_in[1];
    const u16* wo = (const u16*)d_in[2];
    const u16* wg = (const u16*)d_in[4];
    const u16* wu = (const u16*)d_in[5];
    const u16* wd = (const u16*)d_in[6];
    char* ws = (char*)d_ws;
    prep_kernel<<<PREP_CS_BLKS + PREP_ST_BLKS + PREP_TILE_BLKS, 256, 0, stream>>>(x0, wqkv, wo, wg, wu, wd, ws);
    for (int L = 0; L < 2; ++L) {
        char* lw = ws + WS_L0 + (size_t)L * L_BYTES;
        char* st_a = ws + WS_ST + (size_t)(2 * L) * ST_BYTES;
        char* st_b = ws + WS_ST + (size_t)(2 * L + 1) * ST_BYTES;
        const char* xin = L == 0 ? (const char*)x0 : ws + WS_L0 + L_X2;
        if (L == 0) qkv_kernel<true><<<240, 256, 0, stream>>>((const u16*)xin, wqkv, ws + WS_CSQ, st_a, lw + L_Q, lw + L_KV);
        else qkv_kernel<false><<<240, 256, 0, stream>>>((const u16*)xin, wqkv, ws + WS_CSQ, st_a, lw + L_Q, lw + L_KV);
        attn_kernel<<<256, 512, 0, stream>>>(lw + L_Q, lw + L_KV, lw + L_O);
        if (L == 0) proj_kernel<640, true, false><<<256, 256, 0, stream>>>(lw + L_O, ws + WS_WOT, xin, lw + L_X1, st_b);
        else proj_kernel<640, false, false><<<256, 256, 0, stream>>>(lw + L_O, ws + WS_WOT, xin, lw + L_X1, st_b);
        gu_kernel<<<256, 256, 0, stream>>>(lw + L_X1, wg, wu, ws + WS_CSG, st_b, lw + L_H);
        if (L == 0) proj_kernel<1536, false, false><<<256, 256, 0, stream>>>(lw + L_H, ws + WS_WDT, lw + L_X1, lw + L_X2, ws + WS_ST + 2 * ST_BYTES);
        else proj_kernel<1536, false, true><<<256, 256, 0, stream>>>(lw + L_H, ws + WS_WDT, lw + L_X1, (char*)d_out, nullptr);
    }
}
```

```cpp
#include <hip/hip_runtime.h>
#include <hip/hip_bf16.h>
#include <stdint.h>

typedef short bf16x8 __attribute__((ext_vector_type(8)));
typedef short s16x4 __attribute__((ext_vector_type(4)));
typedef float f32x32 __attribute__((ext_vector_type(32)));
typedef float f32x16 __attribute__((ext_vector_type(16)));
typedef float f32x4 __attribute__((ext_vector_type(4)));
typedef float f32x2 __attribute__((ext_vector_type(2)));
typedef unsigned u32x4 __attribute__((ext_vector_type(4)));
typedef unsigned u32x2 __attribute__((ext_vector_type(2)));
typedef unsigned short u16;
typedef __attribute__((address_space(3))) char* lds_t;
typedef __attribute__((address_space(1))) unsigned gu32;

constexpr int S_ = 2048, D_ = 640, H_ = 32, F_ = 1536;
constexpr float LN_EPS = 1e-5f;
constexpr float QSCALE = 0.22360679774997896f * 1.4426950408889634f;
constexpr unsigned ONES2 = 0x3f803f80u;

constexpr size_t XT_BYTES = (size_t)S_ * D_ * 2;
constexpr size_t HT_BYTES = (size_t)S_ * F_ * 2;
constexpr size_t Q_BYTES = (size_t)H_ * 64 * 1536;
constexpr size_t KV_BYTES = (size_t)H_ * 64 * 3072;
constexpr size_t ST_BYTES = (size_t)8 * S_ * 8;
constexpr size_t FL_GROUP = 64 * 64;
constexpr size_t FL_A = 0, FL_W = FL_A + 32 * FL_GROUP, FL_G = FL_W + 16 * FL_GROUP, FL_D = FL_G + 32 * FL_GROUP,
                 FL_P = FL_D + 16 * FL_GROUP, FL_BYTES = FL_P + 8 * FL_GROUP;
constexpr size_t WS_FLAGS = 0;
constexpr size_t WS_TMO = WS_FLAGS + 2 * FL_BYTES;
constexpr size_t WS_CSQ = WS_TMO + 4096;
constexpr size_t WS_CSG = WS_CSQ + 16384;
constexpr size_t WS_ST = WS_CSG + 16384;
constexpr size_t WS_WT = WS_ST + 4 * ST_BYTES;
constexpr size_t WS_L0 = WS_WT + (size_t)8 * 68 * 5120;
constexpr size_t L_Q = 0, L_KV = L_Q + Q_BYTES, L_O = L_KV + KV_BYTES, L_X1 = L_O + XT_BYTES, L_H = L_X1 + XT_BYTES,
                 L_X2 = L_H + HT_BYTES, L_BYTES = L_X2 + XT_BYTES;

constexpr int X_SLOT = 8192, X_NS = 6;
constexpr int WR_OFF = X_NS * X_SLOT;
constexpr int W_SLOT = 12288, W_NS = 8, W_PD = 6;
constexpr int CS_OFF = WR_OFF + 98304, S2_OFF = CS_OFF + 1024, LDS_BYTES = S2_OFF + 2048;

#define STAMP(kid, idx) do {} while (0)
#define STAMPT(kid, idx, thr) do {} while (0)
#define WAIT_V(n) asm volatile("s_waitcnt vmcnt(%0)" ::"i"(n) : "memory")
#define BAR() asm volatile("s_barrier" ::: "memory")
#define BAR_L() asm volatile("s_waitcnt lgkmcnt(0)\n\ts_barrier" ::: "memory")
#define LGKM0() __builtin_amdgcn_s_waitcnt(0xC07F)
#define SB() __builtin_amdgcn_sched_barrier(0)

#define GL_ "global_load_lds_dwordx4 "
template <bool SC1> __device__ __forceinline__ void glds1(const void* sb, unsigned v, unsigned lds) {
    if constexpr (SC1) asm volatile("s_mov_b32 m0, %2\n\ts_nop 0\n\t" GL_ "%0, %1 sc1" :: "v"(v), "s"(sb), "s"(lds) : "memory");
    else asm volatile("s_mov_b32 m0, %2\n\ts_nop 0\n\t" GL_ "%0, %1" :: "v"(v), "s"(sb), "s"(lds) : "memory");
}
template <bool SC1> __device__ __forceinline__ void glds2(const void* sb, unsigned v0, unsigned v1, unsigned lds) {
    if constexpr (SC1) asm volatile("s_mov_b32 m0, %3\n\ts_nop 0\n\t" GL_ "%0, %2 sc1\n\t" GL_ "%1, %2 offset:1024 sc1" :: "v"(v0), "v"(v1), "s"(sb), "s"(lds) : "memory");
    else asm volatile("s_mov_b32 m0, %3\n\ts_nop 0\n\t" GL_ "%0, %2\n\t" GL_ "%1, %2 offset:1024" :: "v"(v0), "v"(v1), "s"(sb), "s"(lds) : "memory");
}
__device__ __forceinline__ void glds3(const void* sb, unsigned v0, unsigned v1, unsigned v2, unsigned lds) {
    asm volatile("s_mov_b32 m0, %4\n\ts_nop 0\n\t" GL_ "%0, %3\n\t" GL_ "%1, %3 offset:1024\n\t" GL_ "%2, %3 offset:2048"
                 :: "v"(v0), "v"(v1), "v"(v2), "s"(sb), "s"(lds) : "memory");
}
template <bool SC1> __device__ __forceinline__ void glds4(const void* sb, unsigned v0, unsigned v1, unsigned v2, unsigned v3, unsigned lds) {
    if constexpr (SC1) asm volatile("s_mov_b32 m0, %5\n\ts_nop 0\n\t" GL_ "%0, %4 sc1\n\t" GL_ "%1, %4 offset:1024 sc1\n\t" GL_ "%2, %4 offset:2048 sc1\n\t" GL_ "%3, %4 offset:3072 sc1"
                 :: "v"(v0), "v"(v1), "v"(v2), "v"(v3), "s"(sb), "s"(lds) : "memory");
    else asm volatile("s_mov_b32 m0, %5\n\ts_nop 0\n\t" GL_ "%0, %4\n\t" GL_ "%1, %4 offset:1024\n\t" GL_ "%2, %4 offset:2048\n\t" GL_ "%3, %4 offset:3072"
                 :: "v"(v0), "v"(v1), "v"(v2), "v"(v3), "s"(sb), "s"(lds) : "memory");
}
template <bool SC1> __device__ __forceinline__ u32x2 gload2(const void* sb, unsigned v) {
    u32x2 r;
    if constexpr (SC1) asm volatile("global_load_dwordx2 %0, %1, %2 sc1" : "=v"(r) : "v"(v), "s"(sb) : "memory");
    else asm volatile("global_load_dwordx2 %0, %1, %2" : "=v"(r) : "v"(v), "s"(sb) : "memory");
    return r;
}
template <bool SC1> __device__ __forceinline__ void gstore2(void* sb, unsigned v, u32x2 d) {
    if constexpr (SC1) asm volatile("global_store_dwordx2 %0, %1, %2 sc1" :: "v"(v), "v"(d), "s"(sb) : "memory");
    else asm volatile("global_store_dwordx2 %0, %1, %2" :: "v"(v), "v"(d), "s"(sb) : "memory");
}
template <bool SC1> __device__ __forceinline__ void gstore4(void* sb, unsigned v, u32x4 d) {
    if constexpr (SC1) asm volatile("global_store_dwordx4 %0, %1, %2 sc1\n\ts_nop 1" :: "v"(v), "v"(d), "s"(sb) : "memory");
    else asm volatile("global_store_dwordx4 %0, %1, %2\n\ts_nop 1" :: "v"(v), "v"(d), "s"(sb) : "memory");
}

typedef __bf16 bf2_t __attribute__((ext_vector_type(2)));
__device__ __forceinline__ float dot2(unsigned a, unsigned b, float c) {
    return __builtin_amdgcn_fdot2_f32_bf16(__builtin_bit_cast(bf2_t, a), __builtin_bit_cast(bf2_t, b), c, false);
}
__device__ __forceinline__ unsigned cvtpk(float lo, float hi) {
    f32x2 v = {lo, hi};
    return __builtin_bit_cast(unsigned, __builtin_convertvector(v, bf2_t));
}
__device__ __forceinline__ float bflo(unsigned u) { return __uint_as_float(u << 16); }
__device__ __forceinline__ float bfhi(unsigned u) { return __uint_as_float(u & 0xffff0000u); }
__device__ __forceinline__ bf16x8 ldsr128(const char* p) { return *(const bf16x8*)p; }
__device__ __forceinline__ s16x4 ldsr64(const char* p) { return *(const s16x4*)p; }
__device__ __forceinline__ s16x4 ldstr(const char* p) {
    return __builtin_bit_cast(s16x4, __builtin_amdgcn_ds_read_tr16_b64_v4i16((__attribute__((address_space(3))) s16x4*)p));
}
__device__ __forceinline__ bf16x8 ldstr2(const char* p, int d) { return __builtin_shufflevector(ldstr(p), ldstr(p + d), 0, 1, 2, 3, 4, 5, 6, 7); }
#define MFMA32(a, b, c) __builtin_amdgcn_mfma_f32_32x32x16_bf16((a), (b), (c), 0, 0, 0)
#define MFMA16(a, b, c) __builtin_amdgcn_mfma_f32_16x16x32_bf16((a), (b), (c), 0, 0, 0)
#define MFMA2B(a, b, c) __builtin_amdgcn_mfma_f32_32x32x4bf16_1k((a), (b), (c), 0, 0, 0)
#define CAT16(a, b) __builtin_shufflevector(a, b, 0,1,2,3,4,5,6,7,8,9,10,11,12,13,14,15,16,17,18,19,20,21,22,23,24,25,26,27,28,29,30,31)

__device__ __forceinline__ float xsum32(float a) {
    auto rr = __builtin_amdgcn_permlane32_swap(__float_as_uint(a), __float_as_uint(a), false, false);
    return __uint_as_float(rr[0]) + __uint_as_float(rr[1]);
}
__device__ __forceinline__ float xmax32(float a) {
    auto rr = __builtin_amdgcn_permlane32_swap(__float_as_uint(a), __float_as_uint(a), false, false);
    return fmaxf(__uint_as_float(rr[0]), __uint_as_float(rr[1]));
}
__device__ __forceinline__ float xsum16_32(float x) {
    auto s = __builtin_amdgcn_permlane16_swap(__float_as_uint(x), __float_as_uint(x), false, false);
    return xsum32(__uint_as_float(s[0]) + __uint_as_float(s[1]));
}
__device__ __forceinline__ f32x2 ln_finish(float s1, float s2) {
    const float mean = s1 * (1.f / 640.f);
    const float var = fmaxf(s2 * (1.f / 640.f) - mean * mean, 0.f);
    const float rstd = __builtin_amdgcn_rsqf(var + LN_EPS);
    return f32x2{rstd, -mean * rstd};
}

__device__ __forceinline__ void signal_flag(char* flag) {
    asm volatile("s_waitcnt vmcnt(0)" ::: "memory");
    if ((threadIdx.x & 63) == 0) __hip_atomic_store((gu32*)flag, 1u, __ATOMIC_RELAXED, __HIP_MEMORY_SCOPE_AGENT);
}
#define POLL_PRE 8
#define POLL_SLEEP 2
__device__ __forceinline__ void wait_flags(const char* group, int n, char* tmo) {
    const int lane = threadIdx.x & 63;
    const gu32* f = (const gu32*)(group + lane * 64);
    unsigned v = lane < n ? 0u : 1u;
    if (POLL_PRE) __builtin_amdgcn_s_sleep(POLL_PRE);
#pragma unroll 1
    for (unsigned spins = 0;; ++spins) {
        if (v == 0u) v = __hip_atomic_load(f, __ATOMIC_RELAXED, __HIP_MEMORY_SCOPE_AGENT);
        if (__all(v != 0u)) break;
        if (spins > 100000u) { if (lane == 0) __hip_atomic_store((gu32*)tmo, 1u, __ATOMIC_RELAXED, __HIP_MEMORY_SCOPE_AGENT); break; }
        __builtin_amdgcn_s_sleep(POLL_SLEEP);
    }
    asm volatile("" ::: "memory");
}

template <int NST, int NS, int PD, int D, class Issue, class Hook>
__device__ __forceinline__ void ring_loader(Issue issue, Hook hook) {
    static_assert(PD <= NS - 2 && (PD - 1) * D <= 63 && PD <= NST, "ring geometry");
#pragma unroll 1
    for (int s = 0; s < PD; ++s) issue(s, s);
    int slot = PD;
#pragma unroll 1
    for (int s = 0; s < NST - PD; ++s) {
        WAIT_V((PD - 1) * D);
        BAR();
        issue(s + PD, slot);
        hook();
        slot = slot + 1 == NS ? 0 : slot + 1;
    }
#pragma unroll
    for (int r = PD - 1; r >= 0; --r) {
        WAIT_V(r * D);
        BAR();
        hook();
    }
}
template <int N> __device__ __forceinline__ void idle_barriers() {
#pragma unroll 1
    for (int i = 0; i < N; ++i) BAR();
}
#define PDX 3

constexpr int QKV_NST = 20;

template <bool XROW>
__device__ __forceinline__ void x_frag_offs(int tt, int lane, int& o0, int& o1) {
    const int r32 = lane & 31, hh = lane >> 5;
    if (XROW) {
        const int sw = (r32 >> 2) & 3;
        o0 = tt * 2048 + r32 * 64 + ((hh ^ sw) * 16);
        o1 = tt * 2048 + r32 * 64 + (((2 + hh) ^ sw) * 16);
    } else {
        o0 = tt * 2048 + lane * 16;
        o1 = o0 + 1024;
    }
}
template <bool SC1>
__device__ __forceinline__ void ln_issue(u32x2 (&p)[8], const char* stats, int tok0) {
#pragma unroll
    for (int c = 0; c < 8; ++c) p[c] = gload2<SC1>(stats + ((size_t)c * S_ + tok0) * 8, (threadIdx.x & 63) * 8);
}
__device__ __forceinline__ f32x2 ln_collect(u32x2 (&p)[8]) {
    asm volatile("s_waitcnt vmcnt(0)" : "+v"(p[0]), "+v"(p[1]), "+v"(p[2]), "+v"(p[3]), "+v"(p[4]), "+v"(p[5]), "+v"(p[6]), "+v"(p[7]) :: "memory");
    f32x2 a = __builtin_bit_cast(f32x2, p[0]);
#pragma unroll
    for (int c = 1; c < 8; ++c) a += __builtin_bit_cast(f32x2, p[c]);
    return ln_finish(a[0], a[1]);
}
__device__ __forceinline__ void split_halves(f32x2 v, f32x2& lo, f32x2& hi) {
#pragma unroll
    for (int e = 0; e < 2; ++e) {
        auto rr = __builtin_amdgcn_permlane32_swap(__float_as_uint(v[e]), __float_as_uint(v[e]), false, false);
        lo[e] = __uint_as_float(rr[0]); hi[e] = __uint_as_float(rr[1]);
    }
}

template <bool FIRST, int WS0, bool ISV>
__device__ __forceinline__ void qkv_compute(char* smem, const char* stats, char* Qb, char* KVb, int tm, int tn, int w) {
    const int lane = threadIdx.x & 63;
    const int wm = w & 1, wn = w >> 1;
    const int r32 = lane & 31, hh = lane >> 5;
    int xoff[2];
    x_frag_offs<FIRST>(wm * 2, lane, xoff[0], xoff[1]);
    const int woff = WR_OFF + (lane >> 5) * 2048 + wn * 512 + ((lane >> 2) & 3) * 64 + ((lane >> 4) & 1) * 32 + (lane & 3) * 8;
    const bool isq = tn < 5;
    const int tstride = (!ISV && isq) ? 1536 : 3072;
    char* obase = ((!ISV && isq) ? Qb : KVb) + (size_t)(tm * 4 + wm * 2) * tstride;
    unsigned ooff[ISV ? 2 : 8];
    if (!ISV) {
        const int cbase = (isq ? tn : tn - 5) * 128 + wn * 64 + hh * 4;
#pragma unroll
        for (int q = 0; q < 8; ++q) {
            const int c = cbase + q * 8, head = c / 20, d0 = c - head * 20;
            ooff[q] = head * 64 * tstride + (d0 >> 3) * 512 + r32 * (d0 == 16 ? 8 : 16) + (d0 & 7) * 2;
        }
    } else {
#pragma unroll
        for (int i = 0; i < 2; ++i) {
            const int c = (tn - 10) * 128 + (wn * 2 + i) * 32 + r32, head = c / 20, d = c - head * 20;
            ooff[i] = head * 64 * 3072 + 1536 + hh * 384 + d * 16;
        }
    }
    f32x16 acc[2][2];
#pragma unroll
    for (int i = 0; i < 2; ++i)
#pragma unroll
        for (int j = 0; j < 2; ++j)
#pragma unroll
            for (int r = 0; r < 16; ++r) acc[i][j][r] = 0.f;
#pragma unroll
    for (int q = 0; q < (ISV ? 2 : 8); ++q) asm volatile("" : "+v"(ooff[q]));
    bf16x8 xf[2][2], wf[2][2];
    u32x2 part[8];
    asm volatile("" : "+v"(acc[0][0]), "+v"(acc[0][1]), "+v"(acc[1][0]), "+v"(acc[1][1]));
    BAR();
    STAMP(0, 1);
#pragma unroll
    for (int ks = 0; ks < 2; ++ks) {
#pragma unroll
        for (int j = 0; j < 2; ++j) xf[ks][j] = ldsr128(smem + xoff[ks] + j * 2048);
#pragma unroll
        for (int i = 0; i < 2; ++i) wf[ks][i] = ldstr2(smem + WS0 * W_SLOT + woff + ks * 4096 + i * 256, 1024);
    }
    if (!FIRST) ln_issue<true>(part, stats, tm * 128 + wm * 64);
    LGKM0();
    int xs = 1, ws = (WS0 + 1) & (W_NS - 1);
#define QM(i, j) acc[i][j] = ISV ? MFMA32(xf[ks][j], wf[ks][i], acc[i][j]) : MFMA32(wf[ks][i], xf[ks][j], acc[i][j])
#pragma unroll 1
    for (int s = 0; s < QKV_NST; ++s) {
        BAR();
        SB();
        const char* xp = smem + xs * X_SLOT;
        const char* wp = smem + ws * W_SLOT + woff;
#pragma unroll
        for (int ks = 0; ks < 2; ++ks) {
            QM(0, 0); SB();
            QM(0, 1); SB(); wf[ks][0] = ldstr2(wp + ks * 4096, 1024); SB();
            QM(1, 0); SB(); xf[ks][0] = ldsr128(xp + xoff[ks]); SB();
            QM(1, 1); SB(); wf[ks][1] = ldstr2(wp + ks * 4096 + 256, 1024); xf[ks][1] = ldsr128(xp + xoff[ks] + 2048); SB();
        }
        xs = xs + 1 == X_NS ? 0 : xs + 1;
        ws = (ws + 1) & (W_NS - 1);
    }
#undef QM
    BAR_L();
    STAMP(0, 2);
    const float* csl = (const float*)(smem + CS_OFF);
    f32x2 stl;
    if (FIRST) stl = *(const f32x2*)(smem + S2_OFF + (wm * 64 + lane) * 8);
    else stl = ln_collect(part);
    if (!ISV) {
        f32x2 st[2];
        split_halves(stl * (isq ? QSCALE : 1.f), st[0], st[1]);
#pragma unroll
        for (int q = 0; q < 8; ++q) {
            const f32x4 c4 = *(const f32x4*)(csl + wn * 64 + q * 8 + hh * 4);
            const f32x2 c0 = {c4[0], c4[1]}, c1 = {c4[2], c4[3]};
#pragma unroll
            for (int j = 0; j < 2; ++j) {
                const f32x16& a = acc[q >> 2][j];
                const int r = (q & 3) * 4;
                const f32x2 v0 = f32x2{a[r], a[r + 1]} * st[j][0] + c0 * st[j][1];
                const f32x2 v1 = f32x2{a[r + 2], a[r + 3]} * st[j][0] + c1 * st[j][1];
                gstore2<false>(obase + j * tstride, ooff[q], u32x2{cvtpk(v0[0], v0[1]), cvtpk(v1[0], v1[1])});
            }
        }
    } else {
        const char* sl = smem + S2_OFF + (FIRST ? wm * 512 : w * 512);
        if (!FIRST) *(f32x2*)(smem + S2_OFF + w * 512 + lane * 8) = stl;
        float c1[2];
#pragma unroll
        for (int i = 0; i < 2; ++i) c1[i] = csl[(wn * 2 + i) * 32 + r32];
#pragma unroll
        for (int j = 0; j < 2; ++j)
#pragma unroll
            for (int s2 = 0; s2 < 2; ++s2) {
                f32x4 sa[2], sb[2];
#pragma unroll
                for (int q = 0; q < 2; ++q) {
                    const char* sp = sl + (j * 32 + (s2 * 2 + q) * 8 + hh * 4) * 8;
                    sa[q] = *(const f32x4*)sp; sb[q] = *(const f32x4*)(sp + 16);
                }
#pragma unroll
                for (int i = 0; i < 2; ++i) {
                    float v[8];
#pragma unroll
                    for (int q = 0; q < 2; ++q) {
                        const int r = (s2 * 2 + q) * 4;
                        v[q * 4 + 0] = acc[i][j][r + 0] * sa[q][0] + c1[i] * sa[q][1];
                        v[q * 4 + 1] = acc[i][j][r + 1] * sa[q][2] + c1[i] * sa[q][3];
                        v[q * 4 + 2] = acc[i][j][r + 2] * sb[q][0] + c1[i] * sb[q][1];
                        v[q * 4 + 3] = acc[i][j][r + 3] * sb[q][2] + c1[i] * sb[q][3];
                    }
                    gstore4<false>(obase + j * 3072 + s2 * 768, ooff[i], u32x4{cvtpk(v[0], v[1]), cvtpk(v[2], v[3]), cvtpk(v[4], v[5]), cvtpk(v[6], v[7])});
                }
            }
#pragma unroll
        for (int i = 0; i < 2; ++i) {
            const int c = (tn - 10) * 128 + (wn * 2 + i) * 32 + r32;
            if (c % 20 == 19) {
#pragma unroll
                for (int j = 0; j < 2; ++j)
#pragma unroll
                    for (int s2 = 0; s2 < 2; ++s2) gstore4<false>(obase + j * 3072 + s2 * 768 + 16, ooff[i], u32x4{ONES2, ONES2, ONES2, ONES2});
            }
        }
    }
    STAMP(0, 3);
}

__device__ __forceinline__ bool qkv_tile(int& tm, int& tn) {
    const int b = blockIdx.x, xcd = b & 7, j = b >> 3;
    const int id = (xcd & 3) * 30 + j;
    tm = (xcd >> 2) * 8 + (id & 7); tn = id >> 3;
    return j < 30;
}
struct QkvW {
    const char* wsb; unsigned wv;
    __device__ __forceinline__ QkvW(const char* W, int tn) {
        const int lane = threadIdx.x & 63;
        wsb = W + tn * 256;
        wv = ((lane >> 2) & 3) * 3840 + (lane >> 4) * 64 + (lane & 3) * 16;
    }
    __device__ __forceinline__ void operator()(int s, unsigned dst) const {
        constexpr unsigned dv = 4 * 3840 - 1024;
        glds4<false>(wsb + (size_t)s * (32 * 3840), wv, wv + dv, wv + 2 * dv, wv + 3 * dv, dst);
        glds4<false>(wsb + (size_t)s * (32 * 3840) + 16 * 3840, wv, wv + dv, wv + 2 * dv, wv + 3 * dv, dst + 4096);
    }
};

template <bool FIRST, int WS0>
__device__ __forceinline__ void qkv_phase(char* smem, const char* X, const char* W, char* csq, const char* stats, char* Qb, char* KVb,
                                          const char* dep, char* tmo) {
    int tm, tn;
    if (!qkv_tile(tm, tn)) return;
    const int lane = threadIdx.x & 63;
    const int w = __builtin_amdgcn_readfirstlane(threadIdx.x >> 6);
    const unsigned lbase = (unsigned)(uintptr_t)(lds_t)smem;
    STAMP(0, 0);
    if (w < 4) {
        if (tn >= 10) qkv_compute<FIRST, WS0, true>(smem, stats, Qb, KVb, tm, tn, w);
        else qkv_compute<FIRST, WS0, false>(smem, stats, Qb, KVb, tm, tn, w);
    } else if (FIRST && w == 8) {
        const QkvW ld(W, tn);
        ring_loader<QKV_NST, W_NS, 3, 8>([&](int s, int slot) { ld(s, lbase + WR_OFF + slot * W_SLOT); }, [] {});
        BAR(); BAR();
    } else {
        const int lw = w - 4;
        float s1 = 0.f, s2 = 0.f, cs = 0.f;
        int xo[2], hx = 0, hw = 0;
        x_frag_offs<FIRST>(lw, lane, xo[0], xo[1]);
        const int wo = WR_OFF + (lane >> 5) * 2048 + lw * 256 + ((lane >> 2) & 3) * 64 + ((lane >> 4) & 1) * 32 + (lane & 3) * 8;
        auto hook = [&] {
            if (FIRST) {
                const char* xp = smem + hx * X_SLOT;
                const char* wp = smem + hw * W_SLOT + wo;
                const u32x4 x0 = __builtin_bit_cast(u32x4, ldsr128(xp + xo[0])), x1 = __builtin_bit_cast(u32x4, ldsr128(xp + xo[1]));
                const u32x4 w0 = __builtin_bit_cast(u32x4, ldstr2(wp, 1024)), w1 = __builtin_bit_cast(u32x4, ldstr2(wp + 4096, 1024));
#pragma unroll
                for (int e = 0; e < 4; ++e) {
                    s1 = dot2(x0[e], ONES2, s1); s2 = dot2(x0[e], x0[e], s2); cs = dot2(w0[e], ONES2, cs);
                    s1 = dot2(x1[e], ONES2, s1); s2 = dot2(x1[e], x1[e], s2); cs = dot2(w1[e], ONES2, cs);
                }
                hx = hx + 1 == X_NS ? 0 : hx + 1;
                hw = (hw + 1) & (W_NS - 1);
            }
        };
        if (lw < 2) {
            const char* xs = FIRST ? X + (size_t)(tm * 128 + lw * 64) * 1280 : X + (size_t)(tm * 4 + lw * 2) * 40 * 1024;
            const unsigned xv = FIRST ? (lane >> 2) * 1280 + (((lane & 3) ^ ((lane >> 4) & 3)) * 16) : lane * 16;
            constexpr unsigned d1 = FIRST ? 16 * 1280 - 1024 : 0, d2 = FIRST ? 32 * 1280 - 2048 : 40 * 1024 - 2048, d3 = FIRST ? 48 * 1280 - 3072 : 40 * 1024 - 2048;
            if (!FIRST) wait_flags(dep + (size_t)tm * FL_GROUP + lw * 2048, 32, tmo);
            ring_loader<QKV_NST, X_NS, PDX, 4>(
                [&](int s, int slot) { glds4<!FIRST>(xs + s * (FIRST ? 64 : 2048), xv, xv + d1, xv + d2, xv + d3, lbase + slot * X_SLOT + lw * 4096); }, hook);
        } else {
#pragma unroll 1
            for (int s = 0; s < QKV_NST; ++s) { BAR(); hook(); }
        }
        if (FIRST) {
            const f32x2 st = ln_finish(xsum32(s1), xsum32(s2));
            cs = xsum32(cs);
            if (lane < 32) {
                *(f32x2*)(smem + S2_OFF + (lw * 32 + lane) * 8) = st;
                *(float*)(smem + CS_OFF + (lw * 32 + lane) * 4) = cs;
                if (tm == 0) *(float*)(csq + (tn * 128 + lw * 32 + lane) * 4) = cs;
            }
        }
        BAR();
        BAR_L();
    }
}

constexpr int AT_SLOT = 8 * 3072, AT_NS = 3, AT_NCH = 8;

__device__ __forceinline__ float rowmax32(const f32x32& s) {
    float a = fmaxf(fmaxf(s[0], s[1]), s[2]), b = fmaxf(fmaxf(s[3], s[4]), s[5]);
#pragma unroll
    for (int r = 6; r < 30; r += 4) { a = fmaxf(fmaxf(a, s[r]), s[r + 1]); b = fmaxf(fmaxf(b, s[r + 2]), s[r + 3]); }
    a = fmaxf(fmaxf(a, s[30]), s[31]);
    return xmax32(fmaxf(a, b));
}

__device__ __forceinline__ void attn_phase(char* smem, const char* Qb, const char* KVb, char* O, char* sig) {
    const int tid = threadIdx.x, lane = tid & 63;
    const int w = __builtin_amdgcn_readfirstlane(tid >> 6);
    const int b = blockIdx.x, xcd = b & 7, j = b >> 3;
    const int head = xcd * 4 + (j & 3), qblk = j >> 2;
    const int r32 = lane & 31, hh = lane >> 5;
    const int qtile = qblk * 8 + w;
    STAMP(1, 0);
    const char* kv = KVb + (size_t)head * 64 * 3072;
    const unsigned lbase = (unsigned)(uintptr_t)(lds_t)smem;
    const unsigned lv = lane * 16;
    glds3(kv + w * 3072, lv, lv, lv, lbase + w * 3072);
    bf16x8 q0;
    s16x4 q4;
    {
        const char* qp = Qb + (size_t)(head * 64 + qtile) * 1536;
        asm volatile("global_load_dwordx4 %0, %1, %2" : "=v"(q0) : "v"(lv), "s"(qp) : "memory");
        asm volatile("global_load_dwordx2 %0, %1, %2 offset:1024" : "=v"(q4) : "v"(r32 * 8), "s"(qp) : "memory");
    }
    const int koff = lane * 16, khoff = hh * 3072 + 1024 + r32 * 8;
    const int vrow = r32 <= 20 ? r32 : (r32 < 28 ? 20 : 16);
    const int voff = 1536 + hh * 384 + vrow * 16;
    f32x16 zero;
#pragma unroll
    for (int r = 0; r < 16; ++r) zero[r] = 0.f;
    f32x16 o = zero, m16;
    f32x32 SA, SBn;
    asm volatile("s_waitcnt vmcnt(0)" : "+v"(q0), "+v"(q4) :: "memory");
    BAR();
    STAMP(1, 1);
    bf16x8 ka, kb, va0, va1, vb0, vb1;
    s16x4 kh;
    u32x4 pa0, pa1, pb0, pb1 = {0u, 0u, 0u, 0u};
    {
        ka = ldsr128(smem + koff); kb = ldsr128(smem + 3072 + koff); kh = ldsr64(smem + khoff);
        f32x16 n0 = MFMA32(ka, q0, zero), n1 = MFMA32(kb, q0, zero);
        SA = MFMA2B(kh, q4, CAT16(n0, n1));
        const float m = rowmax32(SA);
#pragma unroll
        for (int r = 0; r < 16; ++r) m16[r] = -m;
#pragma unroll
        for (int r = 0; r < 32; ++r) SA[r] -= m;
        ka = ldsr128(smem + 2 * 3072 + koff); kb = ldsr128(smem + 3 * 3072 + koff); kh = ldsr64(smem + 2 * 3072 + khoff);
        va0 = ldsr128(smem + voff); va1 = ldsr128(smem + voff + 768); vb0 = ldsr128(smem + 3072 + voff); vb1 = vb0;
    }
    asm volatile("" : "+v"(m16));
    LGKM0();
    SB();
#define EX(x) x = __builtin_amdgcn_exp2f(x)
#define BF8(x) __builtin_bit_cast(bf16x8, x)
#define G4(S, i, P, jj) EX(S[i]); EX(S[i + 1]); SB(); EX(S[i + 2]); P[jj] = cvtpk(S[i], S[i + 1]); SB(); EX(S[i + 3]); SB();
#define STEP2(S, SN, V1, V2, V3, K4, K5, KH4) do { \
        f32x16 n0, n1; \
        o = MFMA32(vb1, BF8(pb1), o); SB(); vb1 = ldsr128((V1) + 768); SB(); \
        G4(S, 0, pa0, 0) \
        n0 = MFMA32(ka, q0, m16); SB(); ka = ldsr128(K4); pa0[1] = cvtpk(S[2], S[3]); SB(); \
        G4(S, 4, pa0, 2) \
        n1 = MFMA32(kb, q0, m16); SB(); kb = ldsr128(K5); pa0[3] = cvtpk(S[6], S[7]); SB(); \
        G4(S, 8, pa1, 0) \
        o = MFMA32(va0, BF8(pa0), o); SB(); va0 = ldsr128(V2); pa1[1] = cvtpk(S[10], S[11]); SB(); \
        G4(S, 12, pa1, 2) \
        SN = MFMA2B(kh, q4, CAT16(n0, n1)); SB(); kh = ldsr64(KH4); pa1[3] = cvtpk(S[14], S[15]); SB(); \
        G4(S, 16, pb0, 0) \
        pb0[1] = cvtpk(S[18], S[19]); SB(); \
        G4(S, 20, pb0, 2) \
        o = MFMA32(va1, BF8(pa1), o); SB(); va1 = ldsr128((V2) + 768); pb0[3] = cvtpk(S[22], S[23]); SB(); \
        G4(S, 24, pb1, 0) \
        pb1[1] = cvtpk(S[26], S[27]); SB(); \
        o = MFMA32(vb0, BF8(pb0), o); SB(); vb0 = ldsr128(V3); SB(); \
        G4(S, 28, pb1, 2) \
        pb1[3] = cvtpk(S[30], S[31]); SB(); \
    } while (0)
    int slot = 0;
#pragma unroll 1
    for (int c = 0; c < AT_NCH; ++c) {
        const int nslot = slot + 1 == AT_NS ? 0 : slot + 1;
        const char* cv = smem + slot * AT_SLOT + voff;   const char* ck = smem + slot * AT_SLOT + koff;   const char* ch = smem + slot * AT_SLOT + khoff;
        const char* nv = smem + nslot * AT_SLOT + voff;  const char* nk = smem + nslot * AT_SLOT + koff;  const char* nh = smem + nslot * AT_SLOT + khoff;
        STEP2(SA, SBn, cv + 1 * 3072, cv + 2 * 3072, cv + 3 * 3072, ck + 4 * 3072, ck + 5 * 3072, ch + 4 * 3072);
        STEP2(SBn, SA, cv + 3 * 3072, cv + 4 * 3072, cv + 5 * 3072, ck + 6 * 3072, ck + 7 * 3072, ch + 6 * 3072);
        BAR();
        SB();
        STEP2(SA, SBn, cv + 5 * 3072, cv + 6 * 3072, cv + 7 * 3072, nk, nk + 3072, nh);
        STEP2(SBn, SA, cv + 7 * 3072, nv, nv + 3072, nk + 2 * 3072, nk + 3 * 3072, nh + 2 * 3072);
        slot = nslot;
    }
    o = MFMA32(vb1, BF8(pb1), o);
    STAMP(1, 2);
    float l;
    {
        auto rr = __builtin_amdgcn_permlane32_swap(__float_as_uint(o[8]), __float_as_uint(o[8]), false, false);
        l = __uint_as_float(rr[1]);
    }
    float big = fmaxf(fmaxf(fabsf(o[0]), fabsf(o[1])), fabsf(o[2]));
#pragma unroll
    for (int r = 3; r < 9; r += 2) big = fmaxf(fmaxf(big, fabsf(o[r])), fabsf(o[r + 1]));
    float sum = ((o[0] + o[1]) + (o[2] + o[3])) + ((o[4] + o[5]) + (o[6] + o[7])) + o[8];
    if (!hh) { big = fmaxf(fmaxf(big, fabsf(o[9])), fmaxf(fabsf(o[10]), fabsf(o[11]))); sum += (o[9] + o[10]) + o[11]; }
    const bool bad = !(l < 1.8446744073709552e19f) || !(big < 1.0e37f) || !(sum == sum);
    if (__builtin_expect(__any(bad), 0)) {
        float m = -INFINITY;
        o = zero;
#pragma unroll 1
        for (int t = 0; t < 64; t += 2) {
            const char* tp = kv + (size_t)t * 3072;
            f32x16 n0 = MFMA32(*(const bf16x8*)(tp + koff), q0, zero), n1 = MFMA32(*(const bf16x8*)(tp + 3072 + koff), q0, zero);
            f32x32 sc = MFMA2B(*(const s16x4*)(tp + khoff), q4, CAT16(n0, n1));
            const float mn = fmaxf(m, rowmax32(sc));
            const float alpha = __builtin_amdgcn_exp2f(m - mn);
            m = mn;
#pragma unroll
            for (int r = 0; r < 16; ++r) o[r] *= alpha;
#pragma unroll
            for (int q = 0; q < 4; ++q) {
                u32x4 pp;
#pragma unroll
                for (int e = 0; e < 4; ++e) pp[e] = cvtpk(__builtin_amdgcn_exp2f(sc[q * 8 + 2 * e] - mn), __builtin_amdgcn_exp2f(sc[q * 8 + 2 * e + 1] - mn));
                o = MFMA32(*(const bf16x8*)(tp + (q >> 1) * 3072 + voff + (q & 1) * 768), BF8(pp), o);
            }
        }
        auto rr = __builtin_amdgcn_permlane32_swap(__float_as_uint(o[8]), __float_as_uint(o[8]), false, false);
        l = __uint_as_float(rr[1]);
    }
    const float inv = __builtin_amdgcn_rcpf(l);
    char* ob = O + (size_t)qtile * 40 * 1024;
#pragma unroll
    for (int g = 0; g < 3; ++g) {
        if (g < 2 || !hh) {
            const int col = head * 20 + g * 8 + hh * 4;
            gstore2<true>(ob, (col >> 3) * 512 + (col & 4) * 2 + r32 * 16,
                           u32x2{cvtpk(o[g * 4] * inv, o[g * 4 + 1] * inv), cvtpk(o[g * 4 + 2] * inv, o[g * 4 + 3] * inv)});
        }
    }
    STAMP(1, 4);
    signal_flag(sig + (size_t)(qtile >> 1) * FL_GROUP + ((qtile & 1) * 32 + head) * 64);
    STAMP(1, 3);
    BAR_L();
}
#undef EX
#undef G4
#undef STEP2

constexpr int GU_NST = 20;
__device__ __forceinline__ void gu_tile(int& tm, int& tn) {
    const int b = blockIdx.x, xcd = b & 7, j0 = b >> 3;
    tm = (xcd >> 2) * 8 + (j0 & 7); tn = (xcd & 3) * 4 + (j0 >> 3);
}
struct GuW {
    const char *gb, *ub; unsigned gv[3];
    __device__ __forceinline__ GuW(const char* Wg, const char* Wu, int tn) {
        const int lane = threadIdx.x & 63;
#pragma unroll
        for (int i = 0; i < 3; ++i) {
            const int idx = i * 64 + lane, kq = idx / 48, rem = idx - kq * 48, sub = rem >> 4, kr = (rem >> 2) & 3, c = rem & 3;
            gv[i] = (kq * 4 + kr) * 3072 + sub * 64 + c * 16 - i * 1024;
        }
        gb = Wg + tn * 192; ub = Wu + tn * 192;
    }
    __device__ __forceinline__ void operator()(int s, unsigned d) const {
        const size_t o = (size_t)s * (32 * 3072);
        glds3(gb + o, gv[0], gv[1], gv[2], d);
        glds3(ub + o, gv[0] ^ 32u, gv[1] ^ 32u, gv[2] ^ 32u, d + 6144);
        glds3(gb + o + 16 * 3072, gv[0], gv[1], gv[2], d + 3072);
        glds3(ub + o + 16 * 3072, gv[0] ^ 32u, gv[1] ^ 32u, gv[2] ^ 32u, d + 9216);
    }
};

template <int WS0>
__device__ __forceinline__ void gu_phase(char* smem, const char* X, const char* stats, char* Hh, const char* dep, char* sig, char* tmo) {
    const int lane = threadIdx.x & 63;
    const int w = __builtin_amdgcn_readfirstlane(threadIdx.x >> 6);
    int tm, tn;
    gu_tile(tm, tn);
    const unsigned lbase = (unsigned)(uintptr_t)(lds_t)smem;
    STAMP(3, 0);
    if (w >= 6) { idle_barriers<GU_NST + 2>(); return; }
    if (w >= 4) {
        const int lw = w - 4;
        const char* xs = X + (size_t)(tm * 4 + lw * 2) * 40 * 1024;
        const unsigned xv = lane * 16, xv2 = xv + 40 * 1024 - 2048;
        wait_flags(dep + (size_t)tm * FL_GROUP + lw * 2048, 32, tmo);
        STAMPT(3, 5, 256);
        ring_loader<GU_NST, X_NS, PDX, 4>([&](int s, int slot) { glds4<true>(xs + s * 2048, xv, xv, xv2, xv2, lbase + slot * X_SLOT + lw * 4096); }, [] {});
        BAR(); BAR();
        return;
    }
    const int wm = w & 1, wn = w >> 1;
    const int hh = lane >> 5, upl = (lane >> 4) & 1;
    const int xoff = wm * 4096 + lane * 16;
    int woff[3];
#pragma unroll
    for (int i = 0; i < 3; ++i) {
        const int t = wn * 3 + i;
        woff[i] = WR_OFF + upl * 6144 + hh * 1536 + ((lane >> 2) & 3) * 64 + (lane & 3) * 8 + (t >> 1) * 256 + (((t & 1) ^ upl) * 32);
    }
    f32x16 acc[3][2];
#pragma unroll
    for (int i = 0; i < 3; ++i)
#pragma unroll
        for (int j = 0; j < 2; ++j)
#pragma unroll
            for (int r = 0; r < 16; ++r) acc[i][j][r] = 0.f;
    bf16x8 xf[2][2], wf[2][3];
    u32x2 part[8];
    asm volatile("" : "+v"(acc[0][0]), "+v"(acc[0][1]), "+v"(acc[1][0]), "+v"(acc[1][1]), "+v"(acc[2][0]), "+v"(acc[2][1]), "+v"(woff[0]), "+v"(woff[1]), "+v"(woff[2]));
    BAR();
    STAMP(3, 1);
#pragma unroll
    for (int ks = 0; ks < 2; ++ks) {
#pragma unroll
        for (int j = 0; j < 2; ++j) xf[ks][j] = ldsr128(smem + xoff + ks * 1024 + j * 2048);
#pragma unroll
        for (int i = 0; i < 3; ++i) wf[ks][i] = ldstr2(smem + WS0 * W_SLOT + woff[i] + ks * 3072, 768);
    }
    ln_issue<true>(part, stats, tm * 128 + wm * 64);
    LGKM0();
    int xs = 1, ws = (WS0 + 1) & (W_NS - 1);
#define GM(i, j) acc[i][j] = MFMA32(wf[ks][i], xf[ks][j], acc[i][j])
#pragma unroll 1
    for (int s = 0; s < GU_NST; ++s) {
        BAR();
        SB();
        const char* xp = smem + xs * X_SLOT + xoff;
        const char* wp = smem + ws * W_SLOT;
#pragma unroll
        for (int ks = 0; ks < 2; ++ks) {
            GM(0, 0); SB();
            GM(0, 1); SB(); wf[ks][0] = ldstr2(wp + woff[0] + ks * 3072, 768); SB();
            GM(1, 0); SB();
            GM(1, 1); SB(); wf[ks][1] = ldstr2(wp + woff[1] + ks * 3072, 768); SB();
            GM(2, 0); SB(); xf[ks][0] = ldsr128(xp + ks * 1024); SB();
            GM(2, 1); SB(); wf[ks][2] = ldstr2(wp + woff[2] + ks * 3072, 768); xf[ks][1] = ldsr128(xp + ks * 1024 + 2048); SB();
        }
        xs = xs + 1 == X_NS ? 0 : xs + 1;
        ws = (ws + 1) & (W_NS - 1);
    }
#undef GM
    STAMP(3, 2);
    const float* csl = (const float*)(smem + CS_OFF);
    f32x2 st[2];
    split_halves(ln_collect(part), st[0], st[1]);
    char* hb = Hh + (size_t)((tm * 4 + wm * 2) * 96 + tn * 6 + wn * 3) * 1024;
#pragma unroll
    for (int i = 0; i < 3; ++i) {
        const int t = wn * 3 + i;
        f32x4 c4[4];
#pragma unroll
        for (int g = 0; g < 4; ++g) c4[g] = *(const f32x4*)(csl + t * 32 + g * 8 + hh * 4);
#pragma unroll
        for (int j = 0; j < 2; ++j) {
            u32x4 o;
#pragma unroll
            for (int e = 0; e < 4; ++e) {
                const f32x2 cg = {c4[e >> 1][(e & 1) * 2], c4[e >> 1][(e & 1) * 2 + 1]}, cu = {c4[2 + (e >> 1)][(e & 1) * 2], c4[2 + (e >> 1)][(e & 1) * 2 + 1]};
                f32x2 ga = f32x2{acc[i][j][2 * e], acc[i][j][2 * e + 1]} * st[j][0] + cg * st[j][1];
                const f32x2 up = f32x2{acc[i][j][8 + 2 * e], acc[i][j][9 + 2 * e]} * st[j][0] + cu * st[j][1];
                ga[0] = fmaxf(ga[0], 0.f); ga[1] = fmaxf(ga[1], 0.f);
                const f32x2 hv = ga * up;
                o[e] = cvtpk(hv[0], hv[1]);
            }
            gstore4<true>(hb + (j * 96 + i) * 1024, lane * 16, o);
        }
    }
    STAMP(3, 4);
    signal_flag(sig + (size_t)(tm * 2 + wm) * FL_GROUP + (tn * 2 + wn) * 64);
    STAMP(3, 3);
    BAR_L();
}

__device__ __forceinline__ void proj_tile(int& tm, int& tn) {
    const int b = blockIdx.x, xcd = b & 7, j0 = b >> 3;
    tm = (xcd >> 1) * 8 + (j0 & 7); tn = (xcd & 1) * 4 + (j0 >> 3);
}
template <bool SC1>
struct ProjW {
    const char* wb; unsigned lv;
    __device__ __forceinline__ ProjW(const char* WT) { wb = WT; lv = (threadIdx.x & 63) * 16; }
    __device__ __forceinline__ void operator()(int s, unsigned d) const {
        glds4<SC1>(wb + s * 10240, lv, lv, lv, lv, d);
        glds4<SC1>(wb + s * 10240 + 4096, lv, lv, lv, lv, d + 4096);
        glds2<SC1>(wb + s * 10240 + 8192, lv, lv, d + 8192);
    }
};

template <int KTOT, int WS0, bool RESROW, bool OUTROW, bool RES_SC1>
__device__ __forceinline__ void proj_phase(char* smem, const char* A, const char* Xold, char* Xnew, char* stats,
                                           const char* dep, char* sig, char* tmo) {
    constexpr int NST = KTOT / 64, KS16 = KTOT / 16, KID = KTOT == 640 ? 2 : 4;
    const int lane = threadIdx.x & 63;
    const int w = __builtin_amdgcn_readfirstlane(threadIdx.x >> 6);
    int tm, tn;
    proj_tile(tm, tn);
    const unsigned lbase = (unsigned)(uintptr_t)(lds_t)smem;
    STAMP(KID, 0);
    if (w >= 6) { idle_barriers<NST + 2>(); return; }
    if (w >= 4) {
        const int lw = w - 4;
        const char* as = A + (size_t)((tm * 2 + lw) * KS16) * 1024;
        const unsigned xv = lane * 16;
        wait_flags(dep + (size_t)tm * FL_GROUP + (KTOT == 640 ? lw * 2048 : 0), 32, tmo);
        STAMPT(KID, 5, 256);
        ring_loader<NST, X_NS, PDX + 1, 4>([&](int s, int slot) { glds4<true>(as + s * 4096, xv, xv, xv, xv, lbase + slot * X_SLOT + lw * 4096); }, [] {});
        BAR(); BAR();
        return;
    }
    const int g = lane >> 4, c16 = lane & 15;
    const int r32 = (w & 1) * 16 + c16;
    const unsigned rowv = c16 * 1280 + g * 8, tilv = (g >> 1) * 512 + r32 * 16 + (g & 1) * 8;
    const size_t rowb = ((size_t)(tm * 64 + w * 16) * 640 + tn * 80) * 2, tilb = (size_t)((tm * 2 + (w >> 1)) * 40 + tn * 5) * 1024;
    u32x2 res[5];
#pragma unroll
    for (int i = 0; i < 5; ++i) res[i] = RESROW ? gload2<false>(Xold + rowb + i * 32, rowv) : gload2<RES_SC1>(Xold + tilb + i * 1024, tilv);
    const int aoff = (w >> 1) * 4096 + g * 512 + r32 * 16;
    const int woff = WR_OFF + lane * 16;
    f32x4 acc[5];
#pragma unroll
    for (int i = 0; i < 5; ++i) acc[i] = f32x4{0.f, 0.f, 0.f, 0.f};
    bf16x8 af[2], wf[2][5];
    asm volatile("" : "+v"(acc[0]), "+v"(acc[1]), "+v"(acc[2]), "+v"(acc[3]), "+v"(acc[4]));
    BAR();
    STAMP(KID, 1);
#pragma unroll
    for (int kk = 0; kk < 2; ++kk) {
        af[kk] = ldsr128(smem + aoff + kk * 2048);
#pragma unroll
        for (int i = 0; i < 5; ++i) wf[kk][i] = ldsr128(smem + WS0 * W_SLOT + woff + (kk * 5 + i) * 1024);
    }
    LGKM0();
    int xs = 1, ws = (WS0 + 1) & (W_NS - 1);
#pragma unroll 1
    for (int s = 0; s < NST; ++s) {
        BAR();
        SB();
        const char* xp = smem + xs * X_SLOT + aoff;
        const char* wp = smem + ws * W_SLOT + woff;
#pragma unroll
        for (int kk = 0; kk < 2; ++kk) {
#pragma unroll
            for (int i = 0; i < 5; ++i) {
                acc[i] = MFMA16(wf[kk][i], af[kk], acc[i]); SB();
                wf[kk][i] = ldsr128(wp + (kk * 5 + i) * 1024);
                if (i == 4) af[kk] = ldsr128(xp + kk * 2048);
                SB();
            }
        }
        xs = xs + 1 == X_NS ? 0 : xs + 1;
        ws = (ws + 1) & (W_NS - 1);
    }
    STAMP(KID, 2);
    asm volatile("s_waitcnt vmcnt(0)" : "+v"(res[0]), "+v"(res[1]), "+v"(res[2]), "+v"(res[3]), "+v"(res[4]) :: "memory");
#pragma unroll
    for (int i = 0; i < 5; ++i) {
        acc[i] += f32x4{bflo(res[i][0]), bfhi(res[i][0]), bflo(res[i][1]), bfhi(res[i][1])};
        const u32x2 o = {cvtpk(acc[i][0], acc[i][1]), cvtpk(acc[i][2], acc[i][3])};
        if (OUTROW) gstore2<false>(Xnew + rowb + i * 32, rowv, o);
        else gstore2<true>(Xnew + tilb + i * 1024, tilv, o);
    }
    if (!OUTROW) {
        f32x4 s1 = acc[0], s2 = acc[0] * acc[0];
#pragma unroll
        for (int i = 1; i < 5; ++i) { s1 += acc[i]; s2 += acc[i] * acc[i]; }
        const float t1 = xsum16_32((s1[0] + s1[1]) + (s1[2] + s1[3])), t2 = xsum16_32((s2[0] + s2[1]) + (s2[2] + s2[3]));
        if (g == 0) gstore2<true>(stats + ((size_t)tn * S_ + tm * 64 + w * 16) * 8, c16 * 8, u32x2{__float_as_uint(t1), __float_as_uint(t2)});
        STAMP(KID, 4);
        signal_flag(sig + (size_t)(tm >> 1) * FL_GROUP + ((tm & 1) * 32 + tn * 4 + w) * 64);
    }
    STAMP(KID, 3);
    BAR_L();
}

struct Params {
    const char *x0, *wqkv, *wo, *wg, *wu, *wd;
    char *ws, *out;
};
constexpr int WS_WO = 2, WS_GU = (WS_WO + 10) & 7, WS_WD = (WS_GU + 20) & 7, WS_QKV = (WS_WD + 24) & 7;

template <int NST, int WS0, class Cur, class Next>
__device__ __forceinline__ void w_phase(unsigned ring, const Cur& cur, const Next& next) {
    int slot = (WS0 + W_PD) & (W_NS - 1);
#pragma unroll 1
    for (int s = 0; s < NST - W_PD; ++s) {
        WAIT_V(40);
        BAR();
        cur(s + W_PD, ring + slot * W_SLOT);
        slot = (slot + 1) & (W_NS - 1);
    }
#pragma unroll 1
    for (int s = 0; s < W_PD; ++s) {
        WAIT_V(40);
        BAR();
        next(s, ring + slot * W_SLOT);
        slot = (slot + 1) & (W_NS - 1);
    }
    BAR(); BAR();
}
template <int NST, int WS0, int D, class Cur>
__device__ __forceinline__ void w_phase_last(unsigned ring, const Cur& cur) {
    int slot = (WS0 + W_PD) & (W_NS - 1);
#pragma unroll 1
    for (int s = 0; s < NST - W_PD; ++s) {
        WAIT_V(40);
        BAR();
        cur(s + W_PD, ring + slot * W_SLOT);
        slot = (slot + 1) & (W_NS - 1);
    }
#pragma unroll
    for (int r = W_PD - 1; r >= 0; --r) {
        WAIT_V(r * D < 40 ? r * D : 40);
        BAR();
    }
    BAR(); BAR();
}

template <int L>
__device__ __forceinline__ void loader_wave(char* smem, const Params& p, char* fl, char* tmo) {
    const int lane = threadIdx.x & 63;
    const int b = blockIdx.x, xcd = b & 7, j = b >> 3;
    const unsigned lbase = (unsigned)(uintptr_t)(lds_t)smem, ring = lbase + WR_OFF;
    const unsigned lv = lane * 16;
    int ptm, ptn;
    proj_tile(ptm, ptn);
    char* wt = p.ws + WS_WT + (size_t)ptn * 68 * 5120;
    const ProjW<L == 0> wo(wt), wd(wt + 20 * 5120);
    {
        const char* kv = p.ws + WS_L0 + (size_t)L * L_BYTES + L_KV + (size_t)(xcd * 4 + (j & 3)) * 64 * 3072;
        int slot = 1;
#pragma unroll 1
        for (int c = 1; c < AT_NCH; ++c) {
            BAR();
#pragma unroll
            for (int i = 0; i < 6; ++i) glds4<false>(kv + (size_t)c * AT_SLOT + i * 4096, lv, lv, lv, lv, lbase + slot * AT_SLOT + i * 4096);
            slot = slot + 1 == AT_NS ? 0 : slot + 1;
            if (L == 0) {
                const int u = ptm + (c - 2) * 32;
                if (c >= 2 && c <= 4 && u < 68) {
                    const bool isd = u >= 20;
                    const char* src = (isd ? p.wd + (size_t)(u - 20) * 32 * 1280 : p.wo + (size_t)u * 32 * 1280) + ((ptn * 80) & ~31) * 2;
                    unsigned wv[3];
#pragma unroll
                    for (int i = 0; i < 3; ++i) {
                        const int idx = i * 64 + lane, kq = idx / 48, rem = idx - kq * 48;
                        wv[i] = (kq * 4 + ((rem >> 2) & 3)) * 1280 + (rem >> 4) * 64 + (rem & 3) * 16 - i * 1024;
                    }
                    constexpr int SCR = WR_OFF + 7 * W_SLOT;
                    glds3(src, wv[0], wv[1], wv[2], lbase + SCR);
                    glds3(src + 16 * 1280, wv[0], wv[1], wv[2], lbase + SCR + 3072);
                    WAIT_V(0);
                    const int g = lane >> 4;
                    const int q0 = isd ? (g >> 1) * 4 + (g & 1) : g * 2, qd = isd ? 1536 : 768;
                    const char* rp = smem + SCR + q0 * 768 + ((lane >> 2) & 3) * 64 + (lane & 3) * 8;
#pragma unroll
                    for (int i = 0; i < 5; ++i) {
                        const int cc = (ptn & 1) + i;
                        const char* r2 = rp + (cc >> 1) * 256 + (cc & 1) * 32;
                        const bf16x8 f = __builtin_shufflevector(ldstr(r2), ldstr(r2 + qd), 0, 1, 2, 3, 4, 5, 6, 7);
                        gstore4<true>(wt + u * 5120 + i * 1024, lv, __builtin_bit_cast(u32x4, f));
                    }
                }
                if (c == 4) signal_flag(fl + FL_P + (size_t)ptn * FL_GROUP + ptm * 64);
                if (c == 6) wait_flags(fl + FL_P + (size_t)ptn * FL_GROUP, 32, tmo);
            }
            if (c == 7) {
#pragma unroll 1
                for (int s = 0; s < 3; ++s) wo(s, ring + (WS_WO + s) * W_SLOT);
            }
            WAIT_V(0);
        }
        BAR();
#pragma unroll 1
        for (int s = 3; s < W_PD; ++s) wo(s, ring + (WS_WO + s) * W_SLOT);
        BAR();
        BAR();
    }
    int gtm, gtn;
    gu_tile(gtm, gtn);
    const GuW gu(p.wg, p.wu, gtn);
    w_phase<10, WS_WO>(ring, wo, gu);
    glds1<false>(p.ws + WS_CSG + gtn * 768, lv, lbase + CS_OFF);
    w_phase<GU_NST, WS_GU>(ring, gu, wd);
    int qtm, qtn;
    if (L == 0 && qkv_tile(qtm, qtn)) {
        const QkvW qk(p.wqkv, qtn);
        w_phase<24, WS_WD>(ring, wd, qk);
        glds1<false>(p.ws + WS_CSQ + qtn * 512, lv, lbase + CS_OFF);
        w_phase_last<QKV_NST, WS_QKV, 8>(ring, qk);
    } else {
        w_phase_last<24, WS_WD, 10>(ring, wd);
    }
}

__global__ __launch_bounds__(576, 3) void k_first(Params p) {
    __shared__ __attribute__((aligned(1024))) char smem[LDS_BYTES];
    const int b = blockIdx.x, t = threadIdx.x;
    if ((b >> 3) >= 30) {
        const int sidx = (b & 7) * 2 + ((b >> 3) - 30);
        constexpr int NFL = (int)((2 * FL_BYTES + 4096) / 64 / 16);
        for (int i = t; i < NFL; i += 576) *(unsigned*)(p.ws + WS_FLAGS + ((size_t)sidx * NFL + i) * 64) = 0u;
        const char* wsrc = (sidx < 8 ? p.wg : p.wu) + (sidx & 7) * 384;
        const int cg = t % 24, kr = t / 24;
        float a[8] = {0, 0, 0, 0, 0, 0, 0, 0};
        if (kr < 20) {
#pragma unroll 8
            for (int i = 0; i < 32; ++i) {
                const u32x4 v = *(const u32x4*)(wsrc + (size_t)(i * 20 + kr) * 3072 + cg * 16);
#pragma unroll
                for (int j = 0; j < 4; ++j) { a[2 * j] += bflo(v[j]); a[2 * j + 1] += bfhi(v[j]); }
            }
            float* red = (float*)smem;
#pragma unroll
            for (int j = 0; j < 8; ++j) red[kr * 192 + cg * 8 + j] = a[j];
        }
        __syncthreads();
        if (t < 192) {
            float s = 0.f;
#pragma unroll
            for (int i = 0; i < 20; ++i) s += ((const float*)smem)[i * 192 + t];
            const int c = (sidx & 7) * 192 + t;
            ((float*)(p.ws + WS_CSG))[(c >> 4) * 32 + (sidx < 8 ? 0 : 16) + (c & 15)] = s;
        }
        return;
    }
    char* lw = p.ws + WS_L0;
    qkv_phase<true, 0>(smem, p.x0, p.wqkv, p.ws + WS_CSQ, nullptr, lw + L_Q, lw + L_KV, nullptr, nullptr);
}

template <int L>
__global__ __launch_bounds__(576, 3) void k_layer(Params p) {
    __shared__ __attribute__((aligned(1024))) char smem[LDS_BYTES];
    char* ws = p.ws;
    char* lw = ws + WS_L0 + (size_t)L * L_BYTES;
    char* fl = ws + WS_FLAGS + (size_t)L * FL_BYTES;
    char* tmo = ws + WS_TMO;
    char* st_b = ws + WS_ST + (size_t)(2 * L) * ST_BYTES;
    char* st_c = ws + WS_ST + ST_BYTES;
    if (__builtin_amdgcn_readfirstlane(threadIdx.x >> 6) == 8) {
        loader_wave<L>(smem, p, fl, tmo);
        return;
    }
    attn_phase(smem, lw + L_Q, lw + L_KV, lw + L_O, fl + FL_A);
    proj_phase<640, WS_WO, L == 0, false, false>(smem, lw + L_O, L == 0 ? p.x0 : ws + WS_L0 + L_X2, lw + L_X1, st_b, fl + FL_A, fl + FL_W, tmo);
    gu_phase<WS_GU>(smem, lw + L_X1, st_b, lw + L_H, fl + FL_W, fl + FL_G, tmo);
    proj_phase<1536, WS_WD, false, L == 1, true>(smem, lw + L_H, lw + L_X1, L == 1 ? p.out : lw + L_X2, st_c, fl + FL_G, fl + FL_D, tmo);
    if (L == 0) qkv_phase<false, WS_QKV>(smem, lw + L_X2, p.wqkv, ws + WS_CSQ, st_c, lw + L_BYTES + L_Q, lw + L_BYTES + L_KV, fl + FL_D, tmo);
}

extern "C" void kernel_launch(void* const* d_in, const int* in_sizes, int n_in, void* d_out, int out_size, void* d_ws, size_t ws_size,
                              hipStream_t stream) {
    Params p;
    p.x0 = (const char*)d_in[0];
    p.wqkv = (const char*)d_in[1];
    p.wo = (const char*)d_in[2];
    p.wg = (const char*)d_in[4];
    p.wu = (const char*)d_in[5];
    p.wd = (const char*)d_in[6];
    p.ws = (char*)d_ws;
    p.out = (char*)d_out;
    k_first<<<256, 576, 0, stream>>>(p);
    k_layer<0><<<256, 576, 0, stream>>>(p);
    k_layer<1><<<256, 576, 0, stream>>>(p);
}
```

```cpp
#include <hip/hip_runtime.h>
#include <hip/hip_bf16.h>
#include <stdint.h>

typedef short bf16x8 __attribute__((ext_vector_type(8)));
typedef short s16x4 __attribute__((ext_vector_type(4)));
typedef float f32x32 __attribute__((ext_vector_type(32)));
typedef float f32x16 __attribute__((ext_vector_type(16)));
typedef float f32x4 __attribute__((ext_vector_type(4)));
typedef float f32x2 __attribute__((ext_vector_type(2)));
typedef unsigned u32x4 __attribute__((ext_vector_type(4)));
typedef unsigned u32x2 __attribute__((ext_vector_type(2)));
typedef unsigned short u16;
typedef __attribute__((address_space(3))) char* lds_t;
typedef __attribute__((address_space(1))) unsigned gu32;

constexpr int S_ = 2048, D_ = 640, H_ = 32, F_ = 1536;
constexpr float LN_EPS = 1e-5f;
constexpr float QSCALE = 0.22360679774997896f * 1.4426950408889634f;
constexpr unsigned ONES2 = 0x3f803f80u;

constexpr size_t XT_BYTES = (size_t)S_ * D_ * 2;
constexpr size_t HT_BYTES = (size_t)S_ * F_ * 2;
constexpr int KV_REC = 82048, KV_V = 40960, KV_ONES = 81920;
constexpr size_t KV_BYTES = (size_t)64 * KV_REC;
constexpr size_t ST_BYTES = (size_t)8 * S_ * 8;
constexpr size_t FL_GROUP = 64 * 64;
constexpr size_t FL_A = 0, FL_W = FL_A + 32 * FL_GROUP, FL_G = FL_W + 16 * FL_GROUP, FL_D = FL_G + 32 * FL_GROUP,
                 FL_P = FL_D + 16 * FL_GROUP, FL_Q = FL_P + 8 * FL_GROUP, FL_BYTES = FL_Q + 16 * FL_GROUP;
constexpr size_t WS_FLAGS = 0;
constexpr size_t WS_TMO = WS_FLAGS + 2 * FL_BYTES;
constexpr size_t WS_CSQ = WS_TMO + 4096;
constexpr size_t WS_CSG = WS_CSQ + 16384;
constexpr size_t WS_WT = WS_CSG + 16384;
constexpr size_t WS_L0 = WS_WT + (size_t)8 * 68 * 5120;
constexpr size_t L_X0 = 0, L_SA = L_X0 + XT_BYTES, L_Q = L_SA + ST_BYTES, L_KV = L_Q + XT_BYTES, L_O = L_KV + KV_BYTES, L_X1 = L_O + XT_BYTES,
                 L_SB = L_X1 + XT_BYTES, L_H = L_SB + ST_BYTES, L_BYTES = L_H + HT_BYTES;

constexpr int X_SLOT = 8192, X_NS = 6;
constexpr int WR_OFF = X_NS * X_SLOT;
constexpr int W_SLOT = 12288, W_NS = 8, W_PD = 6;
constexpr int CS_OFF = WR_OFF + 98304, S2_OFF = CS_OFF + 1024, STP_OFF = S2_OFF + 2048;

constexpr int LDS_BYTES = STP_OFF;
#define STAMP(kid, idx) do {} while (0)
#define STAMPT(kid, idx, thr) do {} while (0)
#define WAIT_V(n) asm volatile("s_waitcnt vmcnt(%0)" ::"i"(n) : "memory")
#define BAR() asm volatile("s_barrier" ::: "memory")
#define BAR_L() asm volatile("s_waitcnt lgkmcnt(0)\n\ts_barrier" ::: "memory")
#define LGKM0() __builtin_amdgcn_s_waitcnt(0xC07F)
#define SB() __builtin_amdgcn_sched_barrier(0)

__device__ __forceinline__ int TID() { int t = threadIdx.x; asm volatile("" : "+v"(t)); return t; }
__device__ __forceinline__ int BID() { int t = blockIdx.x; asm volatile("" : "+s"(t)); return t; }

#define GL_ "global_load_lds_dwordx4 "
template <bool SC1> __device__ __forceinline__ void glds1(const void* sb, unsigned v, unsigned lds) {
    if constexpr (SC1) asm volatile("s_mov_b32 m0, %2\n\ts_nop 0\n\t" GL_ "%0, %1 sc1" :: "v"(v), "s"(sb), "s"(lds) : "memory");
    else asm volatile("s_mov_b32 m0, %2\n\ts_nop 0\n\t" GL_ "%0, %1" :: "v"(v), "s"(sb), "s"(lds) : "memory");
}
template <bool SC1> __device__ __forceinline__ void glds2(const void* sb, unsigned v0, unsigned v1, unsigned lds) {
    if constexpr (SC1) asm volatile("s_mov_b32 m0, %3\n\ts_nop 0\n\t" GL_ "%0, %2 sc1\n\t" GL_ "%1, %2 offset:1024 sc1" :: "v"(v0), "v"(v1), "s"(sb), "s"(lds) : "memory");
    else asm volatile("s_mov_b32 m0, %3\n\ts_nop 0\n\t" GL_ "%0, %2\n\t" GL_ "%1, %2 offset:1024" :: "v"(v0), "v"(v1), "s"(sb), "s"(lds) : "memory");
}
template <bool SC1 = false> __device__ __forceinline__ void glds3(const void* sb, unsigned v0, unsigned v1, unsigned v2, unsigned lds) {
    if constexpr (SC1) asm volatile("s_mov_b32 m0, %4\n\ts_nop 0\n\t" GL_ "%0, %3 sc1\n\t" GL_ "%1, %3 offset:1024 sc1\n\t" GL_ "%2, %3 offset:2048 sc1"
                 :: "v"(v0), "v"(v1), "v"(v2), "s"(sb), "s"(lds) : "memory");
    else asm volatile("s_mov_b32 m0, %4\n\ts_nop 0\n\t" GL_ "%0, %3\n\t" GL_ "%1, %3 offset:1024\n\t" GL_ "%2, %3 offset:2048"
                 :: "v"(v0), "v"(v1), "v"(v2), "s"(sb), "s"(lds) : "memory");
}
template <bool SC1> __device__ __forceinline__ void glds4(const void* sb, unsigned v0, unsigned v1, unsigned v2, unsigned v3, unsigned lds) {
    if constexpr (SC1) asm volatile("s_mov_b32 m0, %5\n\ts_nop 0\n\t" GL_ "%0, %4 sc1\n\t" GL_ "%1, %4 offset:1024 sc1\n\t" GL_ "%2, %4 offset:2048 sc1\n\t" GL_ "%3, %4 offset:3072 sc1"
                 :: "v"(v0), "v"(v1), "v"(v2), "v"(v3), "s"(sb), "s"(lds) : "memory");
    else asm volatile("s_mov_b32 m0, %5\n\ts_nop 0\n\t" GL_ "%0, %4\n\t" GL_ "%1, %4 offset:1024\n\t" GL_ "%2, %4 offset:2048\n\t" GL_ "%3, %4 offset:3072"
                 :: "v"(v0), "v"(v1), "v"(v2), "v"(v3), "s"(sb), "s"(lds) : "memory");
}
template <bool SC1> __device__ __forceinline__ u32x2 gload2(const void* sb, unsigned v) {
    u32x2 r;
    if constexpr (SC1) asm volatile("global_load_dwordx2 %0, %1, %2 sc1" : "=v"(r) : "v"(v), "s"(sb) : "memory");
    else asm volatile("global_load_dwordx2 %0, %1, %2" : "=v"(r) : "v"(v), "s"(sb) : "memory");
    return r;
}
template <bool SC1> __device__ __forceinline__ void gstore2(void* sb, unsigned v, u32x2 d) {
    if constexpr (SC1) asm volatile("global_store_dwordx2 %0, %1, %2 sc1" :: "v"(v), "v"(d), "s"(sb) : "memory");
    else asm volatile("global_store_dwordx2 %0, %1, %2" :: "v"(v), "v"(d), "s"(sb) : "memory");
}
template <bool SC1> __device__ __forceinline__ void gstore4(void* sb, unsigned v, u32x4 d) {
    if constexpr (SC1) asm volatile("global_store_dwordx4 %0, %1, %2 sc1\n\ts_nop 1" :: "v"(v), "v"(d), "s"(sb) : "memory");
    else asm volatile("global_store_dwordx4 %0, %1, %2\n\ts_nop 1" :: "v"(v), "v"(d), "s"(sb) : "memory");
}

typedef __bf16 bf2_t __attribute__((ext_vector_type(2)));
__device__ __forceinline__ unsigned cvtpk(float lo, float hi) {
    f32x2 v = {lo, hi};
    return __builtin_bit_cast(unsigned, __builtin_convertvector(v, bf2_t));
}
__device__ __forceinline__ float bflo(unsigned u) { return __uint_as_float(u << 16); }
__device__ __forceinline__ float bfhi(unsigned u) { return __uint_as_float(u & 0xffff0000u); }
__device__ __forceinline__ bf16x8 ldsr128(const char* p) { return *(const bf16x8*)p; }
__device__ __forceinline__ s16x4 ldsr64(const char* p) { return *(const s16x4*)p; }
__device__ __forceinline__ s16x4 ldstr(const char* p) {
    return __builtin_bit_cast(s16x4, __builtin_amdgcn_ds_read_tr16_b64_v4i16((__attribute__((address_space(3))) s16x4*)p));
}
__device__ __forceinline__ bf16x8 ldstr2(const char* p, int d) { return __builtin_shufflevector(ldstr(p), ldstr(p + d), 0, 1, 2, 3, 4, 5, 6, 7); }
#define MFMA32(a, b, c) __builtin_amdgcn_mfma_f32_32x32x16_bf16((a), (b), (c), 0, 0, 0)
#define MFMA16(a, b, c) __builtin_amdgcn_mfma_f32_16x16x32_bf16((a), (b), (c), 0, 0, 0)
#define MFMA2B(a, b, c) __builtin_amdgcn_mfma_f32_32x32x4bf16_1k((a), (b), (c), 0, 0, 0)
#define CAT16(a, b) __builtin_shufflevector(a, b, 0,1,2,3,4,5,6,7,8,9,10,11,12,13,14,15,16,17,18,19,20,21,22,23,24,25,26,27,28,29,30,31)

__device__ __forceinline__ float xsum32(float a) {
    auto rr = __builtin_amdgcn_permlane32_swap(__float_as_uint(a), __float_as_uint(a), false, false);
    return __uint_as_float(rr[0]) + __uint_as_float(rr[1]);
}
__device__ __forceinline__ float xmax32(float a) {
    auto rr = __builtin_amdgcn_permlane32_swap(__float_as_uint(a), __float_as_uint(a), false, false);
    return fmaxf(__uint_as_float(rr[0]), __uint_as_float(rr[1]));
}
__device__ __forceinline__ float xsum16_32(float x) {
    auto s = __builtin_amdgcn_permlane16_swap(__float_as_uint(x), __float_as_uint(x), false, false);
    return xsum32(__uint_as_float(s[0]) + __uint_as_float(s[1]));
}
__device__ __forceinline__ f32x2 ln_finish(float s1, float s2) {
    const float mean = s1 * (1.f / 640.f);
    const float var = fmaxf(s2 * (1.f / 640.f) - mean * mean, 0.f);
    const float rstd = __builtin_amdgcn_rsqf(var + LN_EPS);
    return f32x2{rstd, -mean * rstd};
}

__device__ __forceinline__ void signal_flag(char* flag) {
    asm volatile("s_waitcnt vmcnt(0)" ::: "memory");
    if ((TID() & 63) == 0) __hip_atomic_store((gu32*)flag, 1u, __ATOMIC_RELAXED, __HIP_MEMORY_SCOPE_AGENT);
}
#define POLL_PRE 8
#define POLL_SLEEP 2
__device__ __forceinline__ void wait_flags_at(const char* base, unsigned off, bool use, char* tmo) {
    const gu32* f = (const gu32*)(base + off);
    unsigned v = use ? 0u : 1u;
#pragma unroll 1
    for (unsigned spins = 0;; ++spins) {
        if (v == 0u) v = __hip_atomic_load(f, __ATOMIC_RELAXED, __HIP_MEMORY_SCOPE_AGENT);
        if (__all(v != 0u)) break;
        if (spins > 100000u) { if ((TID() & 63) == 0) __hip_atomic_store((gu32*)tmo, 1u, __ATOMIC_RELAXED, __HIP_MEMORY_SCOPE_AGENT); break; }
        __builtin_amdgcn_s_sleep(POLL_SLEEP);
    }
    asm volatile("" ::: "memory");
}
__device__ __forceinline__ void wait_flags(const char* group, int n, char* tmo) {
    const int lane = TID() & 63;
    __builtin_amdgcn_s_sleep(POLL_PRE);
    wait_flags_at(group, lane * 64, lane < n, tmo);
}

template <int NST, int NS, int PD, int D, class Issue>
__device__ __forceinline__ void ring_loader(Issue issue) {
    static_assert(PD <= NS - 2 && (PD - 1) * D <= 63 && PD <= NST, "ring geometry");
#pragma unroll 1
    for (int s = 0; s < PD; ++s) issue(s, s);
    int slot = PD;
#pragma unroll 1
    for (int s = 0; s < NST - PD; ++s) {
        WAIT_V((PD - 1) * D);
        BAR();
        issue(s + PD, slot);
        slot = slot + 1 == NS ? 0 : slot + 1;
    }
#pragma unroll
    for (int r = PD - 1; r >= 0; --r) {
        WAIT_V(r * D);
        BAR();
    }
}
template <int N> __device__ __forceinline__ void idle_barriers() {
#pragma unroll 1
    for (int i = 0; i < N; ++i) BAR();
}
#define PDX 3

constexpr int QKV_NST = 20;

__device__ __forceinline__ void ln_issue(u32x2 (&p)[8], const char* stats, int tok0) {
#pragma unroll
    for (int c = 0; c < 8; ++c) p[c] = gload2<true>(stats + ((size_t)c * S_ + tok0) * 8, (TID() & 63) * 8);
}
__device__ __forceinline__ f32x2 ln_collect(u32x2 (&p)[8]) {
    asm volatile("s_waitcnt vmcnt(0)" : "+v"(p[0]), "+v"(p[1]), "+v"(p[2]), "+v"(p[3]), "+v"(p[4]), "+v"(p[5]), "+v"(p[6]), "+v"(p[7]) :: "memory");
    f32x2 a = __builtin_bit_cast(f32x2, p[0]);
#pragma unroll
    for (int c = 1; c < 8; ++c) a += __builtin_bit_cast(f32x2, p[c]);
    return ln_finish(a[0], a[1]);
}
__device__ __forceinline__ void split_halves(f32x2 v, f32x2& lo, f32x2& hi) {
#pragma unroll
    for (int e = 0; e < 2; ++e) {
        auto rr = __builtin_amdgcn_permlane32_swap(__float_as_uint(v[e]), __float_as_uint(v[e]), false, false);
        lo[e] = __uint_as_float(rr[0]); hi[e] = __uint_as_float(rr[1]);
    }
}

template <int WS0, bool ISV>
__device__ __forceinline__ void qkv_compute(char* smem, const char* stats, char* Qb, char* KVb, char* sig, int tm, int tn, int w, int L) {
    const int lane = TID() & 63;
    const int wm = w & 1, wn = w >> 1;
    const int r32 = lane & 31, hh = lane >> 5;
    const int xoff = wm * 4096 + lane * 16;
    const int woff = WR_OFF + (lane >> 5) * 2048 + wn * 512 + ((lane >> 2) & 3) * 64 + ((lane >> 4) & 1) * 32 + (lane & 3) * 8;
    f32x16 acc[2][2];
#pragma unroll
    for (int i = 0; i < 2; ++i)
#pragma unroll
        for (int j = 0; j < 2; ++j)
#pragma unroll
            for (int r = 0; r < 16; ++r) acc[i][j][r] = 0.f;
    bf16x8 xf[2][2], wf[2][2];
    u32x2 part[8];
    asm volatile("" : "+v"(acc[0][0]), "+v"(acc[0][1]), "+v"(acc[1][0]), "+v"(acc[1][1]));
    BAR();
    STAMP(0, 1);
#pragma unroll
    for (int ks = 0; ks < 2; ++ks) {
#pragma unroll
        for (int j = 0; j < 2; ++j) xf[ks][j] = ldsr128(smem + xoff + ks * 1024 + j * 2048);
#pragma unroll
        for (int i = 0; i < 2; ++i) wf[ks][i] = ldstr2(smem + WS0 * W_SLOT + woff + ks * 4096 + i * 256, 1024);
    }
    ln_issue(part, stats, tm * 128 + wm * 64);
    LGKM0();
    int xs = 1, ws = (WS0 + 1) & (W_NS - 1);
#define QM(i, j) acc[i][j] = ISV ? MFMA32(xf[ks][j], wf[ks][i], acc[i][j]) : MFMA32(wf[ks][i], xf[ks][j], acc[i][j])
#pragma unroll 1
    for (int s = 0; s < QKV_NST; ++s) {
        BAR();
        SB();
        const char* xp = smem + xs * X_SLOT + xoff;
        const char* wp = smem + ws * W_SLOT + woff;
#pragma unroll
        for (int ks = 0; ks < 2; ++ks) {
            QM(0, 0); SB();
            QM(0, 1); SB(); wf[ks][0] = ldstr2(wp + ks * 4096, 1024); SB();
            QM(1, 0); SB(); xf[ks][0] = ldsr128(xp + ks * 1024); SB();
            QM(1, 1); SB(); wf[ks][1] = ldstr2(wp + ks * 4096 + 256, 1024); xf[ks][1] = ldsr128(xp + ks * 1024 + 2048); SB();
        }
        xs = xs + 1 == X_NS ? 0 : xs + 1;
        ws = (ws + 1) & (W_NS - 1);
    }
#undef QM
    STAMP(0, 2);
    const float* csl = (const float*)(smem + CS_OFF);
    const f32x2 stl = ln_collect(part);
    const int tile0 = tm * 4 + wm * 2;
    if (!ISV) {
        const bool isq = tn < 5;
        f32x2 st[2];
        split_halves(stl * (isq ? QSCALE : 1.f), st[0], st[1]);
        const int tstride = isq ? 40960 : KV_REC;
        char* ob = (isq ? Qb : KVb) + (size_t)tile0 * tstride + ((isq ? tn : tn - 5) * 16 + wn * 8) * 512;
#pragma unroll
        for (int q = 0; q < 4; ++q) {
            f32x4 c4[2];
#pragma unroll
            for (int e = 0; e < 2; ++e) c4[e] = *(const f32x4*)(csl + wn * 64 + q * 16 + e * 8 + hh * 4);
#pragma unroll
            for (int j = 0; j < 2; ++j) {
                const f32x16& a = acc[q >> 1][j];
                unsigned pk[2][2];
#pragma unroll
                for (int e = 0; e < 2; ++e) {
                    const int r = (q & 1) * 8 + e * 4;
                    const f32x2 v0 = f32x2{a[r], a[r + 1]} * st[j][0] + f32x2{c4[e][0], c4[e][1]} * st[j][1];
                    const f32x2 v1 = f32x2{a[r + 2], a[r + 3]} * st[j][0] + f32x2{c4[e][2], c4[e][3]} * st[j][1];
                    pk[e][0] = cvtpk(v0[0], v0[1]); pk[e][1] = cvtpk(v1[0], v1[1]);
                }
                auto r0 = __builtin_amdgcn_permlane32_swap(pk[0][0], pk[1][0], false, false);
                auto r1 = __builtin_amdgcn_permlane32_swap(pk[0][1], pk[1][1], false, false);
                gstore4<true>(ob + j * tstride + q * 1024, lane * 16, u32x4{r0[0], r1[0], r0[1], r1[1]});
            }
        }
    } else {
        const char* sl = smem + S2_OFF + w * 512;
        *(f32x2*)(smem + S2_OFF + w * 512 + lane * 8) = stl;
        char* ob = KVb + (size_t)tile0 * KV_REC + KV_V + ((tn - 10) * 128 + wn * 64) * 16;
        const unsigned ov = hh * 10240 + r32 * 16;
        float c1[2];
#pragma unroll
        for (int i = 0; i < 2; ++i) c1[i] = csl[(wn * 2 + i) * 32 + r32];
#pragma unroll
        for (int j = 0; j < 2; ++j)
#pragma unroll
            for (int s2 = 0; s2 < 2; ++s2) {
                f32x4 sa[2], sb[2];
#pragma unroll
                for (int q = 0; q < 2; ++q) {
                    const char* sp = sl + (j * 32 + (s2 * 2 + q) * 8 + hh * 4) * 8;
                    sa[q] = *(const f32x4*)sp; sb[q] = *(const f32x4*)(sp + 16);
                }
#pragma unroll
                for (int i = 0; i < 2; ++i) {
                    float v[8];
#pragma unroll
                    for (int q = 0; q < 2; ++q) {
                        const int r = (s2 * 2 + q) * 4;
                        v[q * 4 + 0] = acc[i][j][r + 0] * sa[q][0] + c1[i] * sa[q][1];
                        v[q * 4 + 1] = acc[i][j][r + 1] * sa[q][2] + c1[i] * sa[q][3];
                        v[q * 4 + 2] = acc[i][j][r + 2] * sb[q][0] + c1[i] * sb[q][1];
                        v[q * 4 + 3] = acc[i][j][r + 3] * sb[q][2] + c1[i] * sb[q][3];
                    }
                    gstore4<true>(ob + j * KV_REC + s2 * 20480 + i * 512, ov, u32x4{cvtpk(v[0], v[1]), cvtpk(v[2], v[3]), cvtpk(v[4], v[5]), cvtpk(v[6], v[7])});
                }
            }
    }
    STAMP(0, 4);
    signal_flag(sig + (size_t)(((tm >> 1) * 32 + tn * 2 + wn) * 4 + (tm & 1) * 2 + wm) * 64);
    STAMP(0, 3);
    BAR_L();
}

__device__ __forceinline__ bool qkv_tile(int& tm, int& tn) {
    const int b = BID(), xcd = b & 7, j = b >> 3;
    const int id = (xcd & 3) * 30 + j;
    tm = (xcd >> 2) * 8 + (id & 7); tn = id >> 3;
    return j < 30;
}
struct QkvW {
    const char* wsb; unsigned wv;
    __device__ __forceinline__ QkvW(const char* W, int tn) {
        const int lane = TID() & 63;
        wsb = W + tn * 256;
        wv = ((lane >> 2) & 3) * 3840 + (lane >> 4) * 64 + (lane & 3) * 16;
    }
    __device__ __forceinline__ void operator()(int s, unsigned dst) const {
        constexpr unsigned dv = 4 * 3840 - 1024;
        glds4<false>(wsb + (size_t)s * (32 * 3840), wv, wv + dv, wv + 2 * dv, wv + 3 * dv, dst);
        glds4<false>(wsb + (size_t)s * (32 * 3840) + 16 * 3840, wv, wv + dv, wv + 2 * dv, wv + 3 * dv, dst + 4096);
    }
};

template <int WS0>
__device__ __forceinline__ void qkv_phase(char* smem, const char* X, const char* stats, char* Qb, char* KVb, const char* dep, char* sig,
                                          char* tmo, int L) {
    int tm, tn;
    if (!qkv_tile(tm, tn)) return;
    const int lane = TID() & 63;
    const int w = __builtin_amdgcn_readfirstlane(TID() >> 6);
    const unsigned lbase = (unsigned)(uintptr_t)(lds_t)smem;
    STAMP(0, 0);
    if (w < 4) {
        if (tn >= 10) qkv_compute<WS0, true>(smem, stats, Qb, KVb, sig, tm, tn, w, L);
        else qkv_compute<WS0, false>(smem, stats, Qb, KVb, sig, tm, tn, w, L);
    } else if (w < 6) {
        const int lw = w - 4;
        const char* xs = X + (size_t)(tm * 4 + lw * 2) * 40 * 1024;
        const unsigned xv = lane * 16, xv2 = xv + 40 * 1024 - 2048;
        if (L) wait_flags(dep + (size_t)tm * FL_GROUP + lw * 2048, 32, tmo);
        STAMPT(0, 5, 256);
        ring_loader<QKV_NST, X_NS, PDX, 4>([&](int s, int slot) { glds4<true>(xs + s * 2048, xv, xv, xv2, xv2, lbase + slot * X_SLOT + lw * 4096); });
        BAR(); BAR();
    } else {
        idle_barriers<QKV_NST + 2>();
    }
}

constexpr int AT_SLOT = 8 * 3072, AT_NS = 3, AT_NCH = 8;

__device__ __forceinline__ float rowmax32(const f32x32& s) {
    float a = fmaxf(fmaxf(s[0], s[1]), s[2]), b = fmaxf(fmaxf(s[3], s[4]), s[5]);
#pragma unroll
    for (int r = 6; r < 30; r += 4) { a = fmaxf(fmaxf(a, s[r]), s[r + 1]); b = fmaxf(fmaxf(b, s[r + 2]), s[r + 3]); }
    a = fmaxf(fmaxf(a, s[30]), s[31]);
    return xmax32(fmaxf(a, b));
}

__device__ __forceinline__ void attn_tile(int& head, int& qblk) {
    const int b = BID(), xcd = b & 7, j = b >> 3;
    head = xcd * 4 + (j & 3); qblk = j >> 2;
}
__device__ __forceinline__ void kv_piece_offs(int head, unsigned (&v)[3]) {
    const int lane = TID() & 63;
    const int c0 = (head * 5) >> 1, odd = head & 1;
    v[0] = (c0 + odd) * 512 + lane * 16;
#pragma unroll
    for (int i = 1; i < 3; ++i) {
        const int idx = i * 64 + lane - 96, sh = idx / 24, row = idx - sh * 24;
        unsigned o = row < 20 ? KV_V + sh * 10240 + (head * 20 + row) * 16 : KV_ONES + (row - 20) * 16;
        if (idx < 0) o = (odd ? c0 : c0 + 2) * 512 + lane * 16;
        v[i] = o - i * 1024;
    }
}

__device__ __forceinline__ void attn_phase(char* smem, const char* Qb, const char* KVb, char* O, char* sig, int L) {
    const int tid = TID(), lane = tid & 63;
    const int w = __builtin_amdgcn_readfirstlane(tid >> 6);
    int head, qblk;
    attn_tile(head, qblk);
    const int r32 = lane & 31, hh = lane >> 5;
    const int qtile = qblk * 8 + w;
    const int c0 = (head * 5) >> 1, odd = head & 1;
    STAMP(1, 0);
    const unsigned lbase = (unsigned)(uintptr_t)(lds_t)smem;
    unsigned pv[3];
    kv_piece_offs(head, pv);
    const unsigned qhv = (odd ? c0 : c0 + 2) * 512 + r32 * 16 + odd * 8;
    const int koff = lane * 16, khoff = hh * 3072 + 1024 + r32 * 16 + odd * 8;
    const int vrow = r32 <= 20 ? r32 : (r32 < 28 ? 20 : 16);
    const int voff = 1536 + hh * 384 + vrow * 16;
    asm volatile("" : "+v"(pv[0]), "+v"(pv[1]), "+v"(pv[2]));
    BAR();
    STAMP(1, 5);
    glds3<true>(KVb + (size_t)w * KV_REC, pv[0], pv[1], pv[2], lbase + w * 3072);
    bf16x8 q0;
    s16x4 q4;
    {
        const char* qp = Qb + (size_t)qtile * 40960;
        asm volatile("global_load_dwordx4 %0, %1, %2 sc1" : "=v"(q0) : "v"(pv[0]), "s"(qp) : "memory");
        asm volatile("global_load_dwordx2 %0, %1, %2 sc1" : "=v"(q4) : "v"(qhv), "s"(qp) : "memory");
    }
    f32x16 zero;
#pragma unroll
    for (int r = 0; r < 16; ++r) zero[r] = 0.f;
    f32x16 o = zero, m16;
    f32x32 SA, SBn;
    asm volatile("s_waitcnt vmcnt(0)" : "+v"(q0), "+v"(q4) :: "memory");
    BAR();
    STAMP(1, 1);
    bf16x8 ka, kb, va0, va1, vb0, vb1;
    s16x4 kh;
    u32x4 pa0, pa1, pb0, pb1 = {0u, 0u, 0u, 0u};
    {
        ka = ldsr128(smem + koff); kb = ldsr128(smem + 3072 + koff); kh = ldsr64(smem + khoff);
        f32x16 n0 = MFMA32(ka, q0, zero), n1 = MFMA32(kb, q0, zero);
        SA = MFMA2B(kh, q4, CAT16(n0, n1));
        const float m = rowmax32(SA);
#pragma unroll
        for (int r = 0; r < 16; ++r) m16[r] = -m;
#pragma unroll
        for (int r = 0; r < 32; ++r) SA[r] -= m;
        ka = ldsr128(smem + 2 * 3072 + koff); kb = ldsr128(smem + 3 * 3072 + koff); kh = ldsr64(smem + 2 * 3072 + khoff);
        va0 = ldsr128(smem + voff); va1 = ldsr128(smem + voff + 768); vb0 = ldsr128(smem + 3072 + voff); vb1 = vb0;
    }
    asm volatile("" : "+v"(m16));
    LGKM0();
    SB();
#define EX(x) x = __builtin_amdgcn_exp2f(x)
#define BF8(x) __builtin_bit_cast(bf16x8, x)
#define G4(S, i, P, jj) EX(S[i]); EX(S[i + 1]); SB(); EX(S[i + 2]); P[jj] = cvtpk(S[i], S[i + 1]); SB(); EX(S[i + 3]); SB();
#define STEP2(S, SN, V1, V2, V3, K4, K5, KH4) do { \
        f32x16 n0, n1; \
        o = MFMA32(vb1, BF8(pb1), o); SB(); vb1 = ldsr128((V1) + 768); SB(); \
        G4(S, 0, pa0, 0) \
        n0 = MFMA32(ka, q0, m16); SB(); ka = ldsr128(K4); pa0[1] = cvtpk(S[2], S[3]); SB(); \
        G4(S, 4, pa0, 2) \
        n1 = MFMA32(kb, q0, m16); SB(); kb = ldsr128(K5); pa0[3] = cvtpk(S[6], S[7]); SB(); \
        G4(S, 8, pa1, 0) \
        o = MFMA32(va0, BF8(pa0), o); SB(); va0 = ldsr128(V2); pa1[1] = cvtpk(S[10], S[11]); SB(); \
        G4(S, 12, pa1, 2) \
        SN = MFMA2B(kh, q4, CAT16(n0, n1)); SB(); kh = ldsr64(KH4); pa1[3] = cvtpk(S[14], S[15]); SB(); \
        G4(S, 16, pb0, 0) \
        pb0[1] = cvtpk(S[18], S[19]); SB(); \
        G4(S, 20, pb0, 2) \
        o = MFMA32(va1, BF8(pa1), o); SB(); va1 = ldsr128((V2) + 768); pb0[3] = cvtpk(S[22], S[23]); SB(); \
        G4(S, 24, pb1, 0) \
        pb1[1] = cvtpk(S[26], S[27]); SB(); \
        o = MFMA32(vb0, BF8(pb0), o); SB(); vb0 = ldsr128(V3); SB(); \
        G4(S, 28, pb1, 2) \
        pb1[3] = cvtpk(S[30], S[31]); SB(); \
    } while (0)
    int slot = 0;
#pragma unroll 1
    for (int c = 0; c < AT_NCH; ++c) {
        const int nslot = slot + 1 == AT_NS ? 0 : slot + 1;
        const char* cv = smem + slot * AT_SLOT + voff;   const char* ck = smem + slot * AT_SLOT + koff;   const char* ch = smem + slot * AT_SLOT + khoff;
        const char* nv = smem + nslot * AT_SLOT + voff;  const char* nk = smem + nslot * AT_SLOT + koff;  const char* nh = smem + nslot * AT_SLOT + khoff;
        STEP2(SA, SBn, cv + 1 * 3072, cv + 2 * 3072, cv + 3 * 3072, ck + 4 * 3072, ck + 5 * 3072, ch + 4 * 3072);
        STEP2(SBn, SA, cv + 3 * 3072, cv + 4 * 3072, cv + 5 * 3072, ck + 6 * 3072, ck + 7 * 3072, ch + 6 * 3072);
        BAR();
        SB();
        STEP2(SA, SBn, cv + 5 * 3072, cv + 6 * 3072, cv + 7 * 3072, nk, nk + 3072, nh);
        STEP2(SBn, SA, cv + 7 * 3072, nv, nv + 3072, nk + 2 * 3072, nk + 3 * 3072, nh + 2 * 3072);
        slot = nslot;
    }
    o = MFMA32(vb1, BF8(pb1), o);
    STAMP(1, 2);
    float l;
    {
        auto rr = __builtin_amdgcn_permlane32_swap(__float_as_uint(o[8]), __float_as_uint(o[8]), false, false);
        l = __uint_as_float(rr[1]);
    }
    float big = fmaxf(fmaxf(fabsf(o[0]), fabsf(o[1])), fabsf(o[2]));
#pragma unroll
    for (int r = 3; r < 9; r += 2) big = fmaxf(fmaxf(big, fabsf(o[r])), fabsf(o[r + 1]));
    float sum = ((o[0] + o[1]) + (o[2] + o[3])) + ((o[4] + o[5]) + (o[6] + o[7])) + o[8];
    if (!hh) { big = fmaxf(fmaxf(big, fabsf(o[9])), fmaxf(fabsf(o[10]), fabsf(o[11]))); sum += (o[9] + o[10]) + o[11]; }
    const bool bad = !(l < 1.8446744073709552e19f) || !(big < 1.0e37f) || !(sum == sum);
    if (__builtin_expect(__any(bad), 0)) {
        float m = -INFINITY;
        o = zero;
        const unsigned gk = (c0 + odd) * 512 + lane * 16, gh = hh * KV_REC + qhv;
        const unsigned gv0 = r32 < 20 ? KV_V + hh * 10240 + (head * 20 + r32) * 16 : KV_ONES, gv1 = r32 < 20 ? gv0 + 20480 : KV_ONES;
#pragma unroll 1
        for (int t = 0; t < 64; t += 2) {
            const char* tp = KVb + (size_t)t * KV_REC;
            bf16x8 fk[2], fv[4];
            s16x4 fh;
            asm volatile("global_load_dwordx4 %0, %7, %11 sc1\n\tglobal_load_dwordx4 %1, %7, %12 sc1\n\tglobal_load_dwordx2 %2, %8, %11 sc1\n\t"
                         "global_load_dwordx4 %3, %9, %11 sc1\n\tglobal_load_dwordx4 %4, %10, %11 sc1\n\t"
                         "global_load_dwordx4 %5, %9, %12 sc1\n\tglobal_load_dwordx4 %6, %10, %12 sc1\n\ts_waitcnt vmcnt(0)"
                         : "=&v"(fk[0]), "=&v"(fk[1]), "=&v"(fh), "=&v"(fv[0]), "=&v"(fv[1]), "=&v"(fv[2]), "=&v"(fv[3])
                         : "v"(gk), "v"(gh), "v"(gv0), "v"(gv1), "s"(tp), "s"(tp + KV_REC) : "memory");
            f32x16 n0 = MFMA32(fk[0], q0, zero), n1 = MFMA32(fk[1], q0, zero);
            f32x32 sc = MFMA2B(fh, q4, CAT16(n0, n1));
            const float mn = fmaxf(m, rowmax32(sc));
            const float alpha = __builtin_amdgcn_exp2f(m - mn);
            m = mn;
#pragma unroll
            for (int r = 0; r < 16; ++r) o[r] *= alpha;
#pragma unroll
            for (int q = 0; q < 4; ++q) {
                u32x4 pp;
#pragma unroll
                for (int e = 0; e < 4; ++e) pp[e] = cvtpk(__builtin_amdgcn_exp2f(sc[q * 8 + 2 * e] - mn), __builtin_amdgcn_exp2f(sc[q * 8 + 2 * e + 1] - mn));
                o = MFMA32(fv[q], BF8(pp), o);
            }
        }
        auto rr = __builtin_amdgcn_permlane32_swap(__float_as_uint(o[8]), __float_as_uint(o[8]), false, false);
        l = __uint_as_float(rr[1]);
    }
    const float inv = __builtin_amdgcn_rcpf(l);
    char* ob = O + (size_t)qtile * 40 * 1024;
#pragma unroll
    for (int g = 0; g < 3; ++g) {
        if (g < 2 || !hh) {
            const int col = head * 20 + g * 8 + hh * 4;
            gstore2<true>(ob, (col >> 3) * 512 + (col & 4) * 2 + r32 * 16,
                           u32x2{cvtpk(o[g * 4] * inv, o[g * 4 + 1] * inv), cvtpk(o[g * 4 + 2] * inv, o[g * 4 + 3] * inv)});
        }
    }
    STAMP(1, 4);
    signal_flag(sig + (size_t)(qtile >> 1) * FL_GROUP + ((qtile & 1) * 32 + head) * 64);
    STAMP(1, 3);
    BAR_L();
}
#undef EX
#undef G4
#undef STEP2

constexpr int GU_NST = 20;
__device__ __forceinline__ void gu_tile(int& tm, int& tn) {
    const int b = BID(), xcd = b & 7, j0 = b >> 3;
    tm = (xcd >> 2) * 8 + (j0 & 7); tn = (xcd & 3) * 4 + (j0 >> 3);
}
struct GuW {
    const char *gb, *ub; unsigned gv[3];
    __device__ __forceinline__ GuW(const char* Wg, const char* Wu, int tn) {
        const int lane = TID() & 63;
#pragma unroll
        for (int i = 0; i < 3; ++i) {
            const int idx = i * 64 + lane, kq = idx / 48, rem = idx - kq * 48, sub = rem >> 4, kr = (rem >> 2) & 3, c = rem & 3;
            gv[i] = (kq * 4 + kr) * 3072 + sub * 64 + c * 16 - i * 1024;
        }
        gb = Wg + tn * 192; ub = Wu + tn * 192;
    }
    __device__ __forceinline__ void operator()(int s, unsigned d) const {
        const size_t o = (size_t)s * (32 * 3072);
        glds3(gb + o, gv[0], gv[1], gv[2], d);
        glds3(ub + o, gv[0] ^ 32u, gv[1] ^ 32u, gv[2] ^ 32u, d + 6144);
        glds3(gb + o + 16 * 3072, gv[0], gv[1], gv[2], d + 3072);
        glds3(ub + o + 16 * 3072, gv[0] ^ 32u, gv[1] ^ 32u, gv[2] ^ 32u, d + 9216);
    }
};

template <int WS0>
__device__ __forceinline__ void gu_phase(char* smem, const char* X, const char* stats, char* Hh, const char* dep, char* sig, char* tmo,
                                         int L) {
    const int lane = TID() & 63;
    const int w = __builtin_amdgcn_readfirstlane(TID() >> 6);
    int tm, tn;
    gu_tile(tm, tn);
    const unsigned lbase = (unsigned)(uintptr_t)(lds_t)smem;
    STAMP(3, 0);
    if (w >= 6) { idle_barriers<GU_NST + 2>(); return; }
    if (w >= 4) {
        const int lw = w - 4;
        const char* xs = X + (size_t)(tm * 4 + lw * 2) * 40 * 1024;
        const unsigned xv = lane * 16, xv2 = xv + 40 * 1024 - 2048;
        wait_flags(dep + (size_t)tm * FL_GROUP + lw * 2048, 32, tmo);
        STAMPT(3, 5, 256);
        ring_loader<GU_NST, X_NS, PDX, 4>([&](int s, int slot) { glds4<true>(xs + s * 2048, xv, xv, xv2, xv2, lbase + slot * X_SLOT + lw * 4096); });
        BAR(); BAR();
        return;
    }
    const int wm = w & 1, wn = w >> 1;
    const int hh = lane >> 5, upl = (lane >> 4) & 1;
    const int xoff = wm * 4096 + lane * 16;
    int woff[3];
#pragma unroll
    for (int i = 0; i < 3; ++i) {
        const int t = wn * 3 + i;
        woff[i] = WR_OFF + upl * 6144 + hh * 1536 + ((lane >> 2) & 3) * 64 + (lane & 3) * 8 + (t >> 1) * 256 + (((t & 1) ^ upl) * 32);
    }
    f32x16 acc[3][2];
#pragma unroll
    for (int i = 0; i < 3; ++i)
#pragma unroll
        for (int j = 0; j < 2; ++j)
#pragma unroll
            for (int r = 0; r < 16; ++r) acc[i][j][r] = 0.f;
    bf16x8 xf[2][2], wf[2][3];
    u32x2 part[8];
    asm volatile("" : "+v"(acc[0][0]), "+v"(acc[0][1]), "+v"(acc[1][0]), "+v"(acc[1][1]), "+v"(acc[2][0]), "+v"(acc[2][1]), "+v"(woff[0]), "+v"(woff[1]), "+v"(woff[2]));
    BAR();
    STAMP(3, 1);
#pragma unroll
    for (int ks = 0; ks < 2; ++ks) {
#pragma unroll
        for (int j = 0; j < 2; ++j) xf[ks][j] = ldsr128(smem + xoff + ks * 1024 + j * 2048);
#pragma unroll
        for (int i = 0; i < 3; ++i) wf[ks][i] = ldstr2(smem + WS0 * W_SLOT + woff[i] + ks * 3072, 768);
    }
    ln_issue(part, stats, tm * 128 + wm * 64);
    LGKM0();
    int xs = 1, ws = (WS0 + 1) & (W_NS - 1);
#define GM(i, j) acc[i][j] = MFMA32(wf[ks][i], xf[ks][j], acc[i][j])
#pragma unroll 1
    for (int s = 0; s < GU_NST; ++s) {
        BAR();
        SB();
        const char* xp = smem + xs * X_SLOT + xoff;
        const char* wp = smem + ws * W_SLOT;
#pragma unroll
        for (int ks = 0; ks < 2; ++ks) {
            GM(0, 0); SB();
            GM(0, 1); SB(); wf[ks][0] = ldstr2(wp + woff[0] + ks * 3072, 768); SB();
            GM(1, 0); SB();
            GM(1, 1); SB(); wf[ks][1] = ldstr2(wp + woff[1] + ks * 3072, 768); SB();
            GM(2, 0); SB(); xf[ks][0] = ldsr128(xp + ks * 1024); SB();
            GM(2, 1); SB(); wf[ks][2] = ldstr2(wp + woff[2] + ks * 3072, 768); xf[ks][1] = ldsr128(xp + ks * 1024 + 2048); SB();
        }
        xs = xs + 1 == X_NS ? 0 : xs + 1;
        ws = (ws + 1) & (W_NS - 1);
    }
#undef GM
    STAMP(3, 2);
    const float* csl = (const float*)(smem + CS_OFF);
    f32x2 st[2];
    split_halves(ln_collect(part), st[0], st[1]);
    char* hb = Hh + (size_t)((tm * 4 + wm * 2) * 96 + tn * 6 + wn * 3) * 1024;
#pragma unroll
    for (int i = 0; i < 3; ++i) {
        const int t = wn * 3 + i;
        f32x4 c4[4];
#pragma unroll
        for (int g = 0; g < 4; ++g) c4[g] = *(const f32x4*)(csl + t * 32 + g * 8 + hh * 4);
#pragma unroll
        for (int j = 0; j < 2; ++j) {
            u32x4 o;
#pragma unroll
            for (int e = 0; e < 4; ++e) {
                const f32x2 cg = {c4[e >> 1][(e & 1) * 2], c4[e >> 1][(e & 1) * 2 + 1]}, cu = {c4[2 + (e >> 1)][(e & 1) * 2], c4[2 + (e >> 1)][(e & 1) * 2 + 1]};
                f32x2 ga = f32x2{acc[i][j][2 * e], acc[i][j][2 * e + 1]} * st[j][0] + cg * st[j][1];
                const f32x2 up = f32x2{acc[i][j][8 + 2 * e], acc[i][j][9 + 2 * e]} * st[j][0] + cu * st[j][1];
                ga[0] = fmaxf(ga[0], 0.f); ga[1] = fmaxf(ga[1], 0.f);
                const f32x2 hv = ga * up;
                o[e] = cvtpk(hv[0], hv[1]);
            }
            gstore4<true>(hb + (j * 96 + i) * 1024, lane * 16, o);
        }
    }
    STAMP(3, 4);
    signal_flag(sig + (size_t)(tm * 2 + wm) * FL_GROUP + (tn * 2 + wn) * 64);
    STAMP(3, 3);
    BAR_L();
}

__device__ __forceinline__ void proj_tile(int& tm, int& tn) {
    const int b = BID(), xcd = b & 7, j0 = b >> 3;
    tm = (xcd >> 1) * 8 + (j0 & 7); tn = (xcd & 1) * 4 + (j0 >> 3);
}
struct ProjW {
    const char* wb; unsigned lv;
    __device__ __forceinline__ ProjW(const char* WT) { wb = WT; lv = (TID() & 63) * 16; }
    __device__ __forceinline__ void operator()(int s, unsigned d) const {
        glds4<true>(wb + s * 10240, lv, lv, lv, lv, d);
        glds4<true>(wb + s * 10240 + 4096, lv, lv, lv, lv, d + 4096);
        glds2<true>(wb + s * 10240 + 8192, lv, lv, d + 8192);
    }
};

template <int KTOT, int WS0>
__device__ __forceinline__ void proj_phase(char* smem, const char* A, const char* Xold, char* Xnew, char* stats, char* out,
                                           const char* dep, char* sig, char* tmo, int L) {
    constexpr int NST = KTOT / 64, KS16 = KTOT / 16, KID = KTOT == 640 ? 2 : 4;
    const int lane = TID() & 63;
    const int w = __builtin_amdgcn_readfirstlane(TID() >> 6);
    int tm, tn;
    proj_tile(tm, tn);
    const unsigned lbase = (unsigned)(uintptr_t)(lds_t)smem;
    STAMP(KID, 0);
    if (w >= 6) { idle_barriers<NST + 2>(); return; }
    if (w >= 4) {
        const int lw = w - 4;
        const char* as = A + (size_t)((tm * 2 + lw) * KS16) * 1024;
        const unsigned xv = lane * 16;
        wait_flags(dep + (size_t)tm * FL_GROUP + (KTOT == 640 ? lw * 2048 : 0), 32, tmo);
        STAMPT(KID, 5, 256);
        ring_loader<NST, X_NS, PDX + 1, 4>([&](int s, int slot) { glds4<true>(as + s * 4096, xv, xv, xv, xv, lbase + slot * X_SLOT + lw * 4096); });
        BAR(); BAR();
        return;
    }
    const int g = lane >> 4, c16 = lane & 15;
    const int r32 = (w & 1) * 16 + c16;
    const unsigned tilv = (g >> 1) * 512 + r32 * 16 + (g & 1) * 8;
    const size_t tilb = (size_t)((tm * 2 + (w >> 1)) * 40 + tn * 5) * 1024;
    u32x2 res[5];
#pragma unroll
    for (int i = 0; i < 5; ++i) res[i] = gload2<true>(Xold + tilb + i * 1024, tilv);
    const int aoff = (w >> 1) * 4096 + g * 512 + r32 * 16;
    const int woff = WR_OFF + lane * 16;
    f32x4 acc[5];
#pragma unroll
    for (int i = 0; i < 5; ++i) acc[i] = f32x4{0.f, 0.f, 0.f, 0.f};
    bf16x8 af[2], wf[2][5];
    asm volatile("" : "+v"(acc[0]), "+v"(acc[1]), "+v"(acc[2]), "+v"(acc[3]), "+v"(acc[4]));
    BAR();
    STAMP(KID, 1);
#pragma unroll
    for (int kk = 0; kk < 2; ++kk) {
        af[kk] = ldsr128(smem + aoff + kk * 2048);
#pragma unroll
        for (int i = 0; i < 5; ++i) wf[kk][i] = ldsr128(smem + WS0 * W_SLOT + woff + (kk * 5 + i) * 1024);
    }
    LGKM0();
    int xs = 1, ws = (WS0 + 1) & (W_NS - 1);
#pragma unroll 1
    for (int s = 0; s < NST; ++s) {
        BAR();
        SB();
        const char* xp = smem + xs * X_SLOT + aoff;
        const char* wp = smem + ws * W_SLOT + woff;
#pragma unroll
        for (int kk = 0; kk < 2; ++kk) {
#pragma unroll
            for (int i = 0; i < 5; ++i) {
                acc[i] = MFMA16(wf[kk][i], af[kk], acc[i]); SB();
                wf[kk][i] = ldsr128(wp + (kk * 5 + i) * 1024);
                if (i == 4) af[kk] = ldsr128(xp + kk * 2048);
                SB();
            }
        }
        xs = xs + 1 == X_NS ? 0 : xs + 1;
        ws = (ws + 1) & (W_NS - 1);
    }
    STAMP(KID, 2);
    asm volatile("s_waitcnt vmcnt(0)" : "+v"(res[0]), "+v"(res[1]), "+v"(res[2]), "+v"(res[3]), "+v"(res[4]) :: "memory");
    u32x2 o[5];
#pragma unroll
    for (int i = 0; i < 5; ++i) {
        acc[i] += f32x4{bflo(res[i][0]), bfhi(res[i][0]), bflo(res[i][1]), bfhi(res[i][1])};
        o[i] = u32x2{cvtpk(acc[i][0], acc[i][1]), cvtpk(acc[i][2], acc[i][3])};
    }
    if (KTOT == 1536 && out != nullptr) {
        char* ob = out + ((size_t)(tm * 64 + w * 16) * 640 + tn * 80) * 2;
#pragma unroll
        for (int i = 0; i < 5; ++i) gstore2<false>(ob + i * 32, c16 * 1280 + g * 8, o[i]);
    } else {
#pragma unroll
        for (int i = 0; i < 5; ++i) gstore2<true>(Xnew + tilb + i * 1024, tilv, o[i]);
        f32x4 s1 = acc[0], s2 = acc[0] * acc[0];
#pragma unroll
        for (int i = 1; i < 5; ++i) { s1 += acc[i]; s2 += acc[i] * acc[i]; }
        const float t1 = xsum16_32((s1[0] + s1[1]) + (s1[2] + s1[3])), t2 = xsum16_32((s2[0] + s2[1]) + (s2[2] + s2[3]));
        if (g == 0) gstore2<true>(stats + ((size_t)tn * S_ + tm * 64 + w * 16) * 8, c16 * 8, u32x2{__float_as_uint(t1), __float_as_uint(t2)});
        STAMP(KID, 4);
        signal_flag(sig + (size_t)(tm >> 1) * FL_GROUP + ((tm & 1) * 32 + tn * 4 + w) * 64);
    }
    STAMP(KID, 3);
    BAR_L();
}

struct Params {
    const char *x0, *wqkv, *wo, *wg, *wu, *wd;
    char *ws, *out;
};
constexpr int WS_WO = 2, WS_GU = (WS_WO + 10) & 7, WS_WD = (WS_GU + 20) & 7, WS_QKV = (WS_WD + 24) & 7;

template <int NST, int WS0, class Cur, class Next>
__device__ __forceinline__ void w_phase(unsigned ring, const Cur& cur, const Next& next) {
    int slot = (WS0 + W_PD) & (W_NS - 1);
#pragma unroll 1
    for (int s = 0; s < NST - W_PD; ++s) {
        WAIT_V(40);
        BAR();
        cur(s + W_PD, ring + slot * W_SLOT);
        slot = (slot + 1) & (W_NS - 1);
    }
#pragma unroll 1
    for (int s = 0; s < W_PD; ++s) {
        WAIT_V(40);
        BAR();
        next(s, ring + slot * W_SLOT);
        slot = (slot + 1) & (W_NS - 1);
    }
    BAR(); BAR();
}
template <int NST, int WS0, int D, class Cur>
__device__ __forceinline__ void w_phase_last(unsigned ring, const Cur& cur) {
    int slot = (WS0 + W_PD) & (W_NS - 1);
#pragma unroll 1
    for (int s = 0; s < NST - W_PD; ++s) {
        WAIT_V(40);
        BAR();
        cur(s + W_PD, ring + slot * W_SLOT);
        slot = (slot + 1) & (W_NS - 1);
    }
#pragma unroll
    for (int r = W_PD - 1; r >= 0; --r) {
        WAIT_V(r * D < 40 ? r * D : 40);
        BAR();
    }
    BAR(); BAR();
}

__device__ __forceinline__ void loader_wave(char* smem, const Params& p) {
    const int lane = TID() & 63;
    const unsigned lbase = (unsigned)(uintptr_t)(lds_t)smem, ring = lbase + WR_OFF;
    const unsigned lv = lane * 16;
    char* tmo = p.ws + WS_TMO;
    int ptm, ptn, gtm, gtn, qtm, qtn, head, qblk;
    const bool hasq = qkv_tile(qtm, qtn);
    const QkvW qk(p.wqkv, qtn);
    if (hasq) {
        glds1<false>(p.ws + WS_CSQ + qtn * 512, lv, lbase + CS_OFF);
        ring_loader<QKV_NST, W_NS, 3, 8>([&](int s, int slot) { qk(s, ring + slot * W_SLOT); });
        BAR(); BAR();
    }
    static_assert(WS_QKV == 0, "first QKV phase starts at slot 0");
    proj_tile(ptm, ptn);
    gu_tile(gtm, gtn);
    attn_tile(head, qblk);
    char* wt = p.ws + WS_WT + (size_t)ptn * 68 * 5120;
    const ProjW wo(wt), wd(wt + 20 * 5120);
    const GuW gu(p.wg, p.wu, gtn);
    unsigned pv[3];
    kv_piece_offs(head, pv);
    const unsigned fr = ((head * 20 + ((lane >> 2) & 1) * 19) >> 6) * 256 + (lane & 3) * 64;
    const unsigned fo = (((lane >> 3) & 1) + 1) * 2560 + fr;
    const unsigned fo0 = lane < 32 ? (lane >> 4) * 8192 + fo : qblk * 8192 + fr;
#pragma unroll 1
    for (int L = 0; L < 2; ++L) {
        char* fl = p.ws + WS_FLAGS + (size_t)L * FL_BYTES;
        const char* kv = p.ws + WS_L0 + (size_t)L * L_BYTES + L_KV;
        __builtin_amdgcn_s_sleep(POLL_PRE);
        wait_flags_at(fl + FL_Q, fo0, lane < 40, tmo);
        BAR();
        int slot = 1;
#pragma unroll 1
        for (int c = 1; c < AT_NCH; ++c) {
            BAR();
#pragma unroll
            for (int i = 0; i < 8; ++i) glds3<true>(kv + (size_t)(c * 8 + i) * KV_REC, pv[0], pv[1], pv[2], lbase + slot * AT_SLOT + i * 3072);
            if (c + 1 < AT_NCH) wait_flags_at(fl + FL_Q + (c + 1) * 8192, fo, lane < 16, tmo);
            WAIT_V(0);
            slot = slot + 1 == AT_NS ? 0 : slot + 1;
            if (L == 0) {
                constexpr int SCR = WR_OFF + 7 * W_SLOT;
                const int u = ptm + (c - 2) * 32;
                if (c >= 2 && c <= 4 && u < 68) {
                    const bool isd = u >= 20;
                    const int g = lane >> 4;
                    const int q0 = isd ? (g >> 1) * 4 + (g & 1) : g * 2, qd = isd ? 1536 : 768;
                    const char* rp = smem + SCR + (c & 1) * 6144 + q0 * 768 + ((lane >> 2) & 3) * 64 + (lane & 3) * 8;
#pragma unroll
                    for (int i = 0; i < 5; ++i) {
                        const int c2 = (ptn & 1) + i;
                        const char* r2 = rp + (c2 >> 1) * 256 + (c2 & 1) * 32;
                        const bf16x8 f = __builtin_shufflevector(ldstr(r2), ldstr(r2 + qd), 0, 1, 2, 3, 4, 5, 6, 7);
                        gstore4<true>(wt + u * 5120 + i * 1024, lv, __builtin_bit_cast(u32x4, f));
                    }
                }
                const int un = u + 32;
                if (c <= 3 && un < 68) {
                    const char* src = (un >= 20 ? p.wd + (size_t)(un - 20) * 32 * 1280 : p.wo + (size_t)un * 32 * 1280) + ((ptn * 80) & ~31) * 2;
                    unsigned wv[3];
#pragma unroll
                    for (int i = 0; i < 3; ++i) {
                        const int idx = i * 64 + lane, kq = idx / 48, rem = idx - kq * 48;
                        wv[i] = (kq * 4 + ((rem >> 2) & 3)) * 1280 + (rem >> 4) * 64 + (rem & 3) * 16 - i * 1024;
                    }
                    const unsigned d = lbase + SCR + ((c + 1) & 1) * 6144;
                    glds3(src, wv[0], wv[1], wv[2], d);
                    glds3(src + 16 * 1280, wv[0], wv[1], wv[2], d + 3072);
                }
                if (c == 5) signal_flag(fl + FL_P + (size_t)ptn * FL_GROUP + ptm * 64);
                if (c == 6) wait_flags(fl + FL_P + (size_t)ptn * FL_GROUP, 32, tmo);
            }
            if (c == 7) {
#pragma unroll 1
                for (int s = 0; s < 3; ++s) wo(s, ring + (WS_WO + s) * W_SLOT);
            }
        }
        BAR();
#pragma unroll 1
        for (int s = 3; s < W_PD; ++s) wo(s, ring + (WS_WO + s) * W_SLOT);
        BAR();
        BAR();
        w_phase<10, WS_WO>(ring, wo, gu);
        glds1<false>(p.ws + WS_CSG + gtn * 768, lv, lbase + CS_OFF);
        w_phase<GU_NST, WS_GU>(ring, gu, wd);
        if (L == 0 && hasq) {
            w_phase<24, WS_WD>(ring, wd, qk);
            glds1<false>(p.ws + WS_CSQ + qtn * 512, lv, lbase + CS_OFF);
            w_phase_last<QKV_NST, WS_QKV, 8>(ring, qk);
        } else {
            w_phase_last<24, WS_WD, 10>(ring, wd);
        }
    }
}

constexpr int PREP_BLOCKS = 128 + 60 + 96;
__global__ __launch_bounds__(256) void k_prep(Params p) {
    __shared__ __attribute__((aligned(16))) float red[16 * 328];
    const int b = blockIdx.x, t = threadIdx.x;
    if (b < 128) {
        const int rl = t >> 4, row = b * 16 + rl, l16 = t & 15;
        u32x4 v[5];
#pragma unroll
        for (int i = 0; i < 5; ++i) v[i] = *(const u32x4*)(p.x0 + (size_t)row * 1280 + (i * 16 + l16) * 16);
        float s1 = 0.f, s2 = 0.f;
#pragma unroll
        for (int i = 0; i < 5; ++i) {
            *(u32x4*)((char*)red + rl * 1296 + (i * 16 + l16) * 16) = v[i];
#pragma unroll
            for (int j = 0; j < 4; ++j) { const float a = bflo(v[i][j]), c = bfhi(v[i][j]); s1 += a + c; s2 += a * a + c * c; }
        }
        __syncthreads();
        char* xt = p.ws + WS_L0 + L_X0 + (size_t)(b >> 1) * 40960 + (b & 1) * 256 + l16 * 16;
#pragma unroll
        for (int i = 0; i < 5; ++i) *(u32x4*)(xt + (i * 16 + rl) * 512) = *(const u32x4*)((char*)red + l16 * 1296 + (i * 16 + rl) * 16);
#pragma unroll
        for (int m = 1; m < 16; m <<= 1) { s1 += __shfl_xor(s1, m); s2 += __shfl_xor(s2, m); }
        __syncthreads();
        if (l16 == 0) *(f32x2*)(red + rl * 2) = f32x2{s1, s2};
        __syncthreads();
        if (t < 128) *(f32x2*)(p.ws + WS_L0 + L_SA + ((size_t)rl * S_ + b * 16 + l16) * 8) = rl == 0 ? *(const f32x2*)(red + l16 * 2) : f32x2{0.f, 0.f};
        return;
    }
    {
        constexpr int NV = (int)((2 * FL_BYTES + 4096) / 16);
        for (int i = (b - 128) * 256 + t; i < NV; i += 156 * 256) *(u32x4*)(p.ws + WS_FLAGS + (size_t)i * 16) = u32x4{0u, 0u, 0u, 0u};
        if (b < 132) {
            const int r = ((b & 1) * 256 + t) >> 3, q = t & 7;
            *(u32x4*)(p.ws + WS_L0 + (size_t)((b - 128) >> 1) * L_BYTES + L_KV + (size_t)r * KV_REC + KV_ONES + q * 16) = u32x4{ONES2, ONES2, ONES2, ONES2};
        }
    }
    const int sb = b - 128;
    const bool isq = sb < 60, isg = sb < 108;
    const int c0 = (isq ? sb : (isg ? sb - 60 : sb - 108)) * 32, ld = isq ? 3840 : 3072;
    const char* src = (isq ? p.wqkv : (isg ? p.wg : p.wu)) + c0 * 2 + (t & 3) * 16 + (size_t)(t >> 2) * ld;
    u32x4 v[10];
#pragma unroll
    for (int i = 0; i < 10; ++i) v[i] = *(const u32x4*)(src + (size_t)i * 64 * ld);
    float a[8] = {0, 0, 0, 0, 0, 0, 0, 0};
#pragma unroll
    for (int i = 0; i < 10; ++i)
#pragma unroll
        for (int j = 0; j < 4; ++j) { a[2 * j] += bflo(v[i][j]); a[2 * j + 1] += bfhi(v[i][j]); }
    *(f32x4*)(red + (t >> 2) * 32 + (t & 3) * 8) = f32x4{a[0], a[1], a[2], a[3]};
    *(f32x4*)(red + (t >> 2) * 32 + (t & 3) * 8 + 4) = f32x4{a[4], a[5], a[6], a[7]};
    __syncthreads();
    float s = 0.f;
#pragma unroll
    for (int i = 0; i < 8; ++i) s += red[((t >> 5) * 8 + i) * 32 + (t & 31)];
    red[2048 + t] = s;
    __syncthreads();
    if (t < 32) {
        s = 0.f;
#pragma unroll
        for (int i = 0; i < 8; ++i) s += red[2048 + i * 32 + t];
        const int c = c0 + t;
        if (isq) ((float*)(p.ws + WS_CSQ))[c] = s;
        else ((float*)(p.ws + WS_CSG))[(c >> 4) * 32 + (isg ? 0 : 16) + (c & 15)] = s;
    }
}

__global__ __launch_bounds__(576, 3) void k_main(Params p) {
    __shared__ __attribute__((aligned(1024))) char smem[LDS_BYTES];
    if (__builtin_amdgcn_readfirstlane(threadIdx.x >> 6) == 8) {
        loader_wave(smem, p);
        return;
    }
    char* tmo = p.ws + WS_TMO;
#pragma unroll 1
    for (int L = 0; L < 2; ++L) {
        char* lw = p.ws + WS_L0 + (size_t)L * L_BYTES;
        char* fl = p.ws + WS_FLAGS + (size_t)L * FL_BYTES;
        qkv_phase<WS_QKV>(smem, lw + L_X0, lw + L_SA, lw + L_Q, lw + L_KV, fl + FL_D, fl + FL_Q, tmo, L);
        attn_phase(smem, lw + L_Q, lw + L_KV, lw + L_O, fl + FL_A, L);
        proj_phase<640, WS_WO>(smem, lw + L_O, lw + L_X0, lw + L_X1, lw + L_SB, nullptr, fl + FL_A, fl + FL_W, tmo, L);
        gu_phase<WS_GU>(smem, lw + L_X1, lw + L_SB, lw + L_H, fl + FL_W, fl + FL_G, tmo, L);
        proj_phase<1536, WS_WD>(smem, lw + L_H, lw + L_X1, lw + L_BYTES + L_X0, lw + L_BYTES + L_SA, L ? p.out : nullptr, fl + FL_G,
                                fl + FL_BYTES + FL_D, tmo, L);
    }
}

extern "C" void kernel_launch(void* const* d_in, const int* in_sizes, int n_in, void* d_out, int out_size, void* d_ws, size_t ws_size,
                              hipStream_t stream) {
    Params p;
    p.x0 = (const char*)d_in[0];
    p.wqkv = (const char*)d_in[1];
    p.wo = (const char*)d_in[2];
    p.wg = (const char*)d_in[4];
    p.wu = (const char*)d_in[5];
    p.wd = (const char*)d_in[6];
    p.ws = (char*)d_ws;
    p.out = (char*)d_out;
    k_prep<<<PREP_BLOCKS, 256, 0, stream>>>(p);
    k_main<<<256, 576, 0, stream>>>(p);
}
```

```cpp
#include <hip/hip_runtime.h>
#include <hip/hip_bf16.h>
#include <stdint.h>

typedef short bf16x8 __attribute__((ext_vector_type(8)));
typedef short s16x4 __attribute__((ext_vector_type(4)));
typedef float f32x32 __attribute__((ext_vector_type(32)));
typedef float f32x16 __attribute__((ext_vector_type(16)));
typedef float f32x4 __attribute__((ext_vector_type(4)));
typedef float f32x2 __attribute__((ext_vector_type(2)));
typedef unsigned u32x4 __attribute__((ext_vector_type(4)));
typedef unsigned u32x2 __attribute__((ext_vector_type(2)));
typedef unsigned short u16;
typedef __attribute__((address_space(3))) char* lds_t;
typedef __attribute__((address_space(1))) unsigned gu32;
typedef unsigned long long u64;
typedef __attribute__((address_space(1))) u64 gu64;

#define MAP 8
constexpr int S_ = 2048, D_ = 640, H_ = 32, F_ = 1536;
constexpr float LN_EPS = 1e-5f;
constexpr float QSCALE = 0.22360679774997896f * 1.4426950408889634f;
constexpr unsigned ONES2 = 0x3f803f80u;

constexpr size_t XT_BYTES = (size_t)S_ * D_ * 2;
constexpr size_t HT_BYTES = (size_t)S_ * F_ * 2;
constexpr int KV_REC = 82048, KV_V = 40960, KV_ONES = 81920;
constexpr size_t KV_BYTES = (size_t)64 * KV_REC;
constexpr size_t ST_BYTES = (size_t)8 * S_ * 8;
constexpr size_t FL_GROUP = 64 * 64;
constexpr size_t FL_A = 0, FL_W = FL_A + 32 * FL_GROUP, FL_G = FL_W + 16 * FL_GROUP, FL_D = FL_G + 32 * FL_GROUP,
                 FL_P = FL_D + 16 * FL_GROUP, FL_Q = FL_P + 8 * FL_GROUP, FL_U = FL_Q + 16 * FL_GROUP, FL_T = FL_U + FL_GROUP, FL_BYTES = FL_T + 4 * FL_GROUP;
constexpr size_t WS_FLAGS = 0;
constexpr size_t WS_TAG = WS_FLAGS + 2 * FL_BYTES;
constexpr size_t WS_CSG = WS_TAG + 4096;
constexpr size_t WS_WT = WS_CSG + 16384;
constexpr size_t WS_GT = WS_WT + (size_t)8 * 68 * 5120;
constexpr size_t WS_L0 = WS_GT + (size_t)16 * 20 * 12288;
constexpr size_t L_X0 = 0, L_SA = L_X0 + XT_BYTES, L_Q = L_SA + ST_BYTES, L_KV = L_Q + XT_BYTES, L_O = L_KV + KV_BYTES, L_X1 = L_O + XT_BYTES,
                 L_SB = L_X1 + XT_BYTES, L_H = L_SB + ST_BYTES, L_BYTES = L_H + HT_BYTES;

constexpr int X_SLOT = 8192, X_NS = 6;
constexpr int WR_OFF = X_NS * X_SLOT;
constexpr int W_SLOT = 12288, W_NS = 8, W_PD = 6;
constexpr int CS_OFF = WR_OFF + 98304, CQ_OFF = CS_OFF + 1024, S2_OFF = CQ_OFF + 512, STP_OFF = S2_OFF + 2048;

constexpr int LDS_BYTES = STP_OFF;
#define STAMP(kid, idx) do {} while (0)
#define STAMPT(kid, idx, thr) do {} while (0)
#define WAIT_V(n) asm volatile("s_waitcnt vmcnt(%0)" ::"i"(n) : "memory")
#define BAR() asm volatile("s_barrier" ::: "memory")
#define BAR_L() asm volatile("s_waitcnt lgkmcnt(0)\n\ts_barrier" ::: "memory")
#define LGKM0() __builtin_amdgcn_s_waitcnt(0xC07F)
#define SB() __builtin_amdgcn_sched_barrier(0)

__device__ __forceinline__ int TID() { int t = threadIdx.x; asm volatile("" : "+v"(t)); return t; }
__device__ __forceinline__ int BID() { int t = blockIdx.x; asm volatile("" : "+s"(t)); return t; }

#define GL_ "global_load_lds_dwordx4 "
template <bool SC1> __device__ __forceinline__ void glds1(const void* sb, unsigned v, unsigned lds) {
    if constexpr (SC1) asm volatile("s_mov_b32 m0, %2\n\ts_nop 0\n\t" GL_ "%0, %1 sc1" :: "v"(v), "s"(sb), "s"(lds) : "memory");
    else asm volatile("s_mov_b32 m0, %2\n\ts_nop 0\n\t" GL_ "%0, %1" :: "v"(v), "s"(sb), "s"(lds) : "memory");
}
template <bool SC1> __device__ __forceinline__ void glds2(const void* sb, unsigned v0, unsigned v1, unsigned lds) {
    if constexpr (SC1) asm volatile("s_mov_b32 m0, %3\n\ts_nop 0\n\t" GL_ "%0, %2 sc1\n\t" GL_ "%1, %2 offset:1024 sc1" :: "v"(v0), "v"(v1), "s"(sb), "s"(lds) : "memory");
    else asm volatile("s_mov_b32 m0, %3\n\ts_nop 0\n\t" GL_ "%0, %2\n\t" GL_ "%1, %2 offset:1024" :: "v"(v0), "v"(v1), "s"(sb), "s"(lds) : "memory");
}
template <bool SC1 = false> __device__ __forceinline__ void glds3(const void* sb, unsigned v0, unsigned v1, unsigned v2, unsigned lds) {
    if constexpr (SC1) asm volatile("s_mov_b32 m0, %4\n\ts_nop 0\n\t" GL_ "%0, %3 sc1\n\t" GL_ "%1, %3 offset:1024 sc1\n\t" GL_ "%2, %3 offset:2048 sc1"
                 :: "v"(v0), "v"(v1), "v"(v2), "s"(sb), "s"(lds) : "memory");
    else asm volatile("s_mov_b32 m0, %4\n\ts_nop 0\n\t" GL_ "%0, %3\n\t" GL_ "%1, %3 offset:1024\n\t" GL_ "%2, %3 offset:2048"
                 :: "v"(v0), "v"(v1), "v"(v2), "s"(sb), "s"(lds) : "memory");
}
template <bool SC1> __device__ __forceinline__ void glds4(const void* sb, unsigned v0, unsigned v1, unsigned v2, unsigned v3, unsigned lds) {
    if constexpr (SC1) asm volatile("s_mov_b32 m0, %5\n\ts_nop 0\n\t" GL_ "%0, %4 sc1\n\t" GL_ "%1, %4 offset:1024 sc1\n\t" GL_ "%2, %4 offset:2048 sc1\n\t" GL_ "%3, %4 offset:3072 sc1"
                 :: "v"(v0), "v"(v1), "v"(v2), "v"(v3), "s"(sb), "s"(lds) : "memory");
    else asm volatile("s_mov_b32 m0, %5\n\ts_nop 0\n\t" GL_ "%0, %4\n\t" GL_ "%1, %4 offset:1024\n\t" GL_ "%2, %4 offset:2048\n\t" GL_ "%3, %4 offset:3072"
                 :: "v"(v0), "v"(v1), "v"(v2), "v"(v3), "s"(sb), "s"(lds) : "memory");
}
template <bool SC1> __device__ __forceinline__ u32x2 gload2(const void* sb, unsigned v) {
    u32x2 r;
    if constexpr (SC1) asm volatile("global_load_dwordx2 %0, %1, %2 sc1" : "=v"(r) : "v"(v), "s"(sb) : "memory");
    else asm volatile("global_load_dwordx2 %0, %1, %2" : "=v"(r) : "v"(v), "s"(sb) : "memory");
    return r;
}
template <bool SC1> __device__ __forceinline__ void gstore2(void* sb, unsigned v, u32x2 d) {
    if constexpr (SC1) asm volatile("global_store_dwordx2 %0, %1, %2 sc1" :: "v"(v), "v"(d), "s"(sb) : "memory");
    else asm volatile("global_store_dwordx2 %0, %1, %2" :: "v"(v), "v"(d), "s"(sb) : "memory");
}
template <bool SC1> __device__ __forceinline__ void gstore4(void* sb, unsigned v, u32x4 d) {
    if constexpr (SC1) asm volatile("global_store_dwordx4 %0, %1, %2 sc1\n\ts_nop 1" :: "v"(v), "v"(d), "s"(sb) : "memory");
    else asm volatile("global_store_dwordx4 %0, %1, %2\n\ts_nop 1" :: "v"(v), "v"(d), "s"(sb) : "memory");
}

typedef __bf16 bf2_t __attribute__((ext_vector_type(2)));
__device__ __forceinline__ unsigned cvtpk(float lo, float hi) {
    f32x2 v = {lo, hi};
    return __builtin_bit_cast(unsigned, __builtin_convertvector(v, bf2_t));
}
__device__ __forceinline__ float bflo(unsigned u) { return __uint_as_float(u << 16); }
__device__ __forceinline__ float bfhi(unsigned u) { return __uint_as_float(u & 0xffff0000u); }
__device__ __forceinline__ bf16x8 ldsr128(const char* p) { return *(const bf16x8*)p; }
__device__ __forceinline__ s16x4 ldsr64(const char* p) { return *(const s16x4*)p; }
__device__ __forceinline__ s16x4 ldstr(const char* p) {
    return __builtin_bit_cast(s16x4, __builtin_amdgcn_ds_read_tr16_b64_v4i16((__attribute__((address_space(3))) s16x4*)p));
}
__device__ __forceinline__ bf16x8 ldstr2(const char* p, int d) { return __builtin_shufflevector(ldstr(p), ldstr(p + d), 0, 1, 2, 3, 4, 5, 6, 7); }
#define MFMA32(a, b, c) __builtin_amdgcn_mfma_f32_32x32x16_bf16((a), (b), (c), 0, 0, 0)
#define MFMA16(a, b, c) __builtin_amdgcn_mfma_f32_16x16x32_bf16((a), (b), (c), 0, 0, 0)
#define MFMA4(a, b, c) __builtin_amdgcn_mfma_f32_4x4x4bf16_1k((a), (b), (c), 0, 0, 0)
#define MFMA2B(a, b, c) __builtin_amdgcn_mfma_f32_32x32x4bf16_1k((a), (b), (c), 0, 0, 0)
#define CAT16(a, b) __builtin_shufflevector(a, b, 0,1,2,3,4,5,6,7,8,9,10,11,12,13,14,15,16,17,18,19,20,21,22,23,24,25,26,27,28,29,30,31)

__device__ __forceinline__ float xsum32(float a) {
    auto rr = __builtin_amdgcn_permlane32_swap(__float_as_uint(a), __float_as_uint(a), false, false);
    return __uint_as_float(rr[0]) + __uint_as_float(rr[1]);
}
__device__ __forceinline__ float xmax32(float a) {
    auto rr = __builtin_amdgcn_permlane32_swap(__float_as_uint(a), __float_as_uint(a), false, false);
    return fmaxf(__uint_as_float(rr[0]), __uint_as_float(rr[1]));
}
__device__ __forceinline__ float xsum16_32(float x) {
    auto s = __builtin_amdgcn_permlane16_swap(__float_as_uint(x), __float_as_uint(x), false, false);
    return xsum32(__uint_as_float(s[0]) + __uint_as_float(s[1]));
}
__device__ __forceinline__ f32x2 ln_finish(float s1, float s2) {
    const float mean = s1 * (1.f / 640.f);
    const float var = fmaxf(s2 * (1.f / 640.f) - mean * mean, 0.f);
    const float rstd = __builtin_amdgcn_rsqf(var + LN_EPS);
    return f32x2{rstd, -mean * rstd};
}

__device__ __forceinline__ void signal_flag(char* flag, u64 tag) {
    asm volatile("s_waitcnt vmcnt(0)" : "+s"(tag), "+s"(flag) :: "memory");
    if ((TID() & 63) == 0) __hip_atomic_store((gu64*)flag, tag, __ATOMIC_RELAXED, __HIP_MEMORY_SCOPE_AGENT);
}
#define POLL_PRE 8
#define POLL_SLEEP 2
__device__ __forceinline__ void wait_flags_at(const char* base, unsigned off, bool use, u64 tag) {
    const gu64* f = (const gu64*)(base + off);
    bool ok = !use;
#pragma unroll 1
    for (unsigned spins = 0; spins < 100000u; ++spins) {
        if (!ok) ok = __hip_atomic_load(f, __ATOMIC_RELAXED, __HIP_MEMORY_SCOPE_AGENT) == tag;
        if (__all(ok)) break;
        __builtin_amdgcn_s_sleep(POLL_SLEEP);
    }
    asm volatile("" ::: "memory");
}
__device__ __forceinline__ void wait_flags(const char* group, int n, u64 tag) {
    const int lane = TID() & 63;
    __builtin_amdgcn_s_sleep(POLL_PRE);
    wait_flags_at(group, lane * 64, lane < n, tag);
}

template <int NST, int NS, int PD, int D, bool PRE = true, class Issue>
__device__ __forceinline__ void ring_loader(Issue issue) {
    static_assert(PD <= NS - 2 && (PD - 1) * D <= 63 && PD <= NST, "ring geometry");
    if (PRE) {
#pragma unroll 1
        for (int s = 0; s < PD; ++s) issue(s, s);
    }
    int slot = PD;
#pragma unroll 1
    for (int s = 0; s < NST - PD; ++s) {
        WAIT_V((PD - 1) * D);
        BAR();
        issue(s + PD, slot);
        slot = slot + 1 == NS ? 0 : slot + 1;
    }
#pragma unroll
    for (int r = PD - 1; r >= 0; --r) {
        WAIT_V(r * D);
        BAR();
    }
}
template <int N> __device__ __forceinline__ void idle_barriers() {
#pragma unroll 1
    for (int i = 0; i < N; ++i) BAR();
}
#define PDX 3
#define PDXP 4

constexpr int QKV_NST = 20;

__device__ __forceinline__ void ln_issue(u32x2 (&p)[8], const char* stats, int tok0) {
#pragma unroll
    for (int c = 0; c < 8; ++c) p[c] = gload2<true>(stats + ((size_t)c * S_ + tok0) * 8, (TID() & 63) * 8);
}
__device__ __forceinline__ f32x2 ln_collect(u32x2 (&p)[8]) {
    asm volatile("s_waitcnt vmcnt(0)" : "+v"(p[0]), "+v"(p[1]), "+v"(p[2]), "+v"(p[3]), "+v"(p[4]), "+v"(p[5]), "+v"(p[6]), "+v"(p[7]) :: "memory");
    f32x2 a = __builtin_bit_cast(f32x2, p[0]);
#pragma unroll
    for (int c = 1; c < 8; ++c) a += __builtin_bit_cast(f32x2, p[c]);
    return ln_finish(a[0], a[1]);
}
__device__ __forceinline__ void split_halves(f32x2 v, f32x2& lo, f32x2& hi) {
#pragma unroll
    for (int e = 0; e < 2; ++e) {
        auto rr = __builtin_amdgcn_permlane32_swap(__float_as_uint(v[e]), __float_as_uint(v[e]), false, false);
        lo[e] = __uint_as_float(rr[0]); hi[e] = __uint_as_float(rr[1]);
    }
}

template <bool FIRST, int WS0, bool ISV>
__device__ __forceinline__ void qkv_compute(char* smem, const char* stats, char* Qb, char* KVb, char* sig, u64 tag, int tm, int tn, int w, int L) {
    const int lane = TID() & 63;
    const int wm = w & 1, wn = w >> 1;
    const int r32 = lane & 31, hh = lane >> 5;
    int xo[2][2], woff[2];
#pragma unroll
    for (int ks = 0; ks < 2; ++ks)
#pragma unroll
        for (int j = 0; j < 2; ++j) {
            const int tt = wm * 2 + (j ^ wn);
            xo[ks][j] = FIRST ? (tt * 32 + r32) * 128 + (((ks * 2 + hh) ^ ((r32 >> 1) & 7)) * 16) : tt * 2048 + ks * 1024 + lane * 16;
        }
#pragma unroll
    for (int i = 0; i < 2; ++i) woff[i] = WR_OFF + hh * 2048 + (wn * 2 + (i ^ wm)) * 256 + ((lane >> 2) & 3) * 64 + ((lane >> 4) & 1) * 32 + (lane & 3) * 8;
    f32x16 acc[2][2];
#pragma unroll
    for (int i = 0; i < 2; ++i)
#pragma unroll
        for (int j = 0; j < 2; ++j)
#pragma unroll
            for (int r = 0; r < 16; ++r) acc[i][j][r] = 0.f;
    bf16x8 xf[2][2], wf[2][2];
    u32x2 part[8];
    f32x4 s1 = {0.f, 0.f, 0.f, 0.f}, s2 = s1, cs = s1;
    s16x4 ones = {0x3f80, 0x3f80, 0x3f80, 0x3f80};
    asm volatile("" : "+v"(acc[0][0]), "+v"(acc[0][1]), "+v"(acc[1][0]), "+v"(acc[1][1]), "+v"(xo[0][0]), "+v"(xo[0][1]), "+v"(woff[0]), "+v"(woff[1]));
    if (FIRST) asm volatile("" : "+v"(s1), "+v"(s2), "+v"(cs), "+v"(ones), "+v"(xo[1][0]), "+v"(xo[1][1]));
    BAR();
    STAMP(0, 1);
#define XADDR(ks, j, key) (FIRST ? smem + (xo[ks][j] ^ (key)) : smem + (key) + xo[0][j] + (ks) * 1024)
#pragma unroll
    for (int ks = 0; ks < 2; ++ks) {
#pragma unroll
        for (int j = 0; j < 2; ++j) xf[ks][j] = ldsr128(XADDR(ks, j, 0));
#pragma unroll
        for (int i = 0; i < 2; ++i) wf[ks][i] = ldstr2(smem + WS0 * W_SLOT + woff[i] + ks * 4096, 1024);
    }
    if (!FIRST) ln_issue(part, stats, tm * 128 + wm * 64);
    LGKM0();
    int xk = FIRST ? 64 : 8192, ws = ((WS0 + 1) & (W_NS - 1)) * W_SLOT;
    int xc = 0, wa0 = WS0 * W_SLOT + woff[0], wa1 = WS0 * W_SLOT + woff[1];
#define QM(ks, i, j) acc[i][j] = ISV ? MFMA32(xf[ks][j], wf[ks][i], acc[i][j]) : MFMA32(wf[ks][i], xf[ks][j], acc[i][j]); SB()
#define QW(ks, i) wf[ks][i] = ldstr2(smem + (i ? wa1 : wa0) + ks * 4096, 1024)
#define QX(ks, j) xf[ks][j] = ldsr128(XADDR(ks, j, xc))
#define LO4(v) __builtin_shufflevector(v, v, 0, 1, 2, 3)
#define HI4(v) __builtin_shufflevector(v, v, 4, 5, 6, 7)
#define QS0(ks) if (FIRST) { s1 = MFMA4(ones, LO4(xf[ks][0]), s1); s2 = MFMA4(LO4(xf[ks][0]), LO4(xf[ks][0]), s2); SB(); }
#define QS1(ks) if (FIRST) { cs = MFMA4(ones, LO4(wf[ks][0]), cs); s1 = MFMA4(ones, HI4(xf[ks][0]), s1); SB(); }
#define QS2(ks) if (FIRST) { s2 = MFMA4(HI4(xf[ks][0]), HI4(xf[ks][0]), s2); cs = MFMA4(ones, HI4(wf[ks][0]), cs); SB(); }
#pragma unroll 1
    for (int s = 0; s < QKV_NST; ++s) {
        QM(0, 1, 1); QW(1, 0); QX(1, 0); SB();
        xc = xk; wa0 = ws + woff[0]; wa1 = ws + woff[1];
        asm volatile("" : "+v"(wa0), "+v"(wa1));
        SB();
        QS0(0)
        QM(0, 1, 0); BAR(); SB();
        QS1(0)
        QM(0, 0, 1); QW(0, 1); SB();
        QS2(0)
        QM(0, 0, 0); QX(0, 1); SB();
        QM(1, 1, 1); QW(0, 0); QX(0, 0); SB();
        QS0(1)
        QM(1, 1, 0);
        if (FIRST) xk = (xk & 64) ? (xk == 2 * 16384 + 64 ? 0 : xk + 16384 - 64) : xk + 64;
        else xk = xk + 8192 == 6 * 8192 ? 0 : xk + 8192;
        ws = ws + W_SLOT == W_NS * W_SLOT ? 0 : ws + W_SLOT;
        asm volatile("" : "+s"(xk), "+s"(ws));
        SB();
        QS1(1)
        QM(1, 0, 1); QW(1, 1); SB();
        QS2(1)
        QM(1, 0, 0); QX(1, 1); SB();
    }
#undef QW
#undef QX
#undef QS0
#undef QS1
#undef QS2
#undef QM
#undef XADDR
    STAMP(0, 2);
    f32x2 stl;
    if (FIRST) {
        const float q2 = (lane & 2) ? ((lane & 1) ? s2[3] : s2[2]) : ((lane & 1) ? s2[1] : s2[0]);
        const f32x2 st = ln_finish(xsum32(s1[0]), xsum32(q2));
        const float c = xsum32(cs[0]);
        if (lane < 32) {
            *(f32x2*)(smem + CS_OFF + ((wm * 2 + wn) * 32 + lane) * 8) = st;
            *(float*)(smem + CQ_OFF + ((wn * 2 + wm) * 32 + lane) * 4) = c;
        }
        BAR_L();
        stl = *(const f32x2*)(smem + CS_OFF + (wm * 64 + lane) * 8);
    } else {
        stl = ln_collect(part);
    }
    const float* csl = (const float*)(smem + CQ_OFF);
    const int tile0 = tm * 4 + wm * 2;
    if (!ISV) {
        const bool isq = tn < 5;
        f32x2 lo, hi, st[2];
        split_halves(stl * (isq ? QSCALE : 1.f), lo, hi);
        st[0] = wn ? hi : lo; st[1] = wn ? lo : hi;
        const int tstride = isq ? 40960 : KV_REC;
        char* ob = (isq ? Qb : KVb) + (size_t)tile0 * tstride + ((isq ? tn : tn - 5) * 16 + wn * 8) * 512;
#pragma unroll
        for (int q = 0; q < 4; ++q) {
            const int qq = q ^ (wm * 2);
            f32x4 c4[2];
#pragma unroll
            for (int e = 0; e < 2; ++e) c4[e] = *(const f32x4*)(csl + wn * 64 + qq * 16 + e * 8 + hh * 4);
#pragma unroll
            for (int j = 0; j < 2; ++j) {
                const f32x16& a = acc[q >> 1][j];
                unsigned pk[2][2];
#pragma unroll
                for (int e = 0; e < 2; ++e) {
                    const int r = (q & 1) * 8 + e * 4;
                    const f32x2 v0 = f32x2{a[r], a[r + 1]} * st[j][0] + f32x2{c4[e][0], c4[e][1]} * st[j][1];
                    const f32x2 v1 = f32x2{a[r + 2], a[r + 3]} * st[j][0] + f32x2{c4[e][2], c4[e][3]} * st[j][1];
                    pk[e][0] = cvtpk(v0[0], v0[1]); pk[e][1] = cvtpk(v1[0], v1[1]);
                }
                auto r0 = __builtin_amdgcn_permlane32_swap(pk[0][0], pk[1][0], false, false);
                auto r1 = __builtin_amdgcn_permlane32_swap(pk[0][1], pk[1][1], false, false);
                gstore4<true>(ob + (j ^ wn) * tstride + qq * 1024, lane * 16, u32x4{r0[0], r1[0], r0[1], r1[1]});
            }
        }
    } else {
        const char* sl = smem + S2_OFF + w * 512;
        *(f32x2*)(smem + S2_OFF + w * 512 + lane * 8) = stl;
        char* ob = KVb + (size_t)tile0 * KV_REC + KV_V + ((tn - 10) * 128 + wn * 64) * 16;
        const unsigned ov = hh * 10240 + r32 * 16;
        float c1[2];
#pragma unroll
        for (int i = 0; i < 2; ++i) c1[i] = csl[(wn * 2 + (i ^ wm)) * 32 + r32];
#pragma unroll
        for (int j = 0; j < 2; ++j)
#pragma unroll
            for (int s2_ = 0; s2_ < 2; ++s2_) {
                f32x4 sa[2], sb[2];
#pragma unroll
                for (int q = 0; q < 2; ++q) {
                    const char* sp = sl + ((j ^ wn) * 32 + (s2_ * 2 + q) * 8 + hh * 4) * 8;
                    sa[q] = *(const f32x4*)sp; sb[q] = *(const f32x4*)(sp + 16);
                }
#pragma unroll
                for (int i = 0; i < 2; ++i) {
                    float v[8];
#pragma unroll
                    for (int q = 0; q < 2; ++q) {
                        const int r = (s2_ * 2 + q) * 4;
                        v[q * 4 + 0] = acc[i][j][r + 0] * sa[q][0] + c1[i] * sa[q][1];
                        v[q * 4 + 1] = acc[i][j][r + 1] * sa[q][2] + c1[i] * sa[q][3];
                        v[q * 4 + 2] = acc[i][j][r + 2] * sb[q][0] + c1[i] * sb[q][1];
                        v[q * 4 + 3] = acc[i][j][r + 3] * sb[q][2] + c1[i] * sb[q][3];
                    }
                    gstore4<true>(ob + (j ^ wn) * KV_REC + s2_ * 20480 + (i ^ wm) * 512, ov,
                                  u32x4{cvtpk(v[0], v[1]), cvtpk(v[2], v[3]), cvtpk(v[4], v[5]), cvtpk(v[6], v[7])});
                }
            }
        if (lane < 16) {
            unsigned one = ONES2;
            asm volatile("" : "+v"(one));
            gstore4<true>(KVb + (size_t)tile0 * KV_REC + KV_ONES, (lane >> 3) * KV_REC + (lane & 7) * 16, u32x4{one, one, one, one});
        }
    }
    STAMP(0, 4);
    signal_flag(sig + (size_t)(((tm >> 1) * 32 + tn * 2 + wn) * 4 + (tm & 1) * 2 + wm) * 64, tag);
    STAMP(0, 3);
    BAR_L();
}

__device__ __forceinline__ bool qkv_tile(int& tm, int& tn) {
    const int b = BID(), xcd = b & 7, j = b >> 3;
    const int id = (xcd & 3) * 30 + j;
    tm = (xcd >> 2) * 8 + (id & 7); tn = id >> 3;
    return j < 30;
}
struct QkvW {
    const char* wsb; unsigned wv;
    __device__ __forceinline__ QkvW(const char* W, int tn) {
        const int lane = TID() & 63;
        wsb = W + tn * 256;
        wv = ((lane >> 2) & 3) * 3840 + (lane >> 4) * 64 + (lane & 3) * 16;
    }
    __device__ __forceinline__ void operator()(int s, unsigned dst) const {
        constexpr unsigned dv = 4 * 3840 - 1024;
        glds4<false>(wsb + (size_t)s * (32 * 3840), wv, wv + dv, wv + 2 * dv, wv + 3 * dv, dst);
        glds4<false>(wsb + (size_t)s * (32 * 3840) + 16 * 3840, wv, wv + dv, wv + 2 * dv, wv + 3 * dv, dst + 4096);
    }
};

struct Params {
    const char *x0, *wqkv, *wo, *wg, *wu, *wd;
    char *ws, *out;
};
__device__ __forceinline__ void acc8(float (&a)[8], const u32x4& v) {
#pragma unroll
    for (int j = 0; j < 4; ++j) { a[2 * j] += bflo(v[j]); a[2 * j + 1] += bfhi(v[j]); }
}
__device__ __forceinline__ void pre_csg(char* smem, const Params& p, u64 tag) {
    const int t = TID(), b = BID();
    const int sidx = (b & 7) * 2 + ((b >> 3) - 30);
    const int lane = t & 63, w = __builtin_amdgcn_readfirstlane(t >> 6), cw = w % 3, half = w / 3;
    const char* src = (sidx < 8 ? p.wg : p.wu) + (sidx & 7) * 384 + cw * 128 + (lane & 7) * 16 + (size_t)(half * 320 + (lane >> 3)) * 3072;
    float a[8] = {0, 0, 0, 0, 0, 0, 0, 0};
#pragma unroll 1
    for (int o = 0; o < 2; ++o) {
        u32x4 v[20];
#pragma unroll
        for (int i = 0; i < 20; ++i) v[i] = *(const u32x4*)(src + (size_t)(o * 20 + i) * 8 * 3072);
#pragma unroll
        for (int i = 0; i < 20; ++i) acc8(a, v[i]);
    }
#pragma unroll
    for (int j = 0; j < 8; ++j) {
        a[j] += __int_as_float(__builtin_amdgcn_ds_swizzle(__float_as_int(a[j]), 0x201f));
        a[j] = xsum16_32(a[j]);
    }
    volatile float* xch = (volatile float*)(smem + S2_OFF) + cw * 64;
    volatile u64* lf = (volatile u64*)(smem + S2_OFF + 1024) + cw;
    if (half) {
        if (lane < 8) {
#pragma unroll
            for (int j = 0; j < 8; ++j) xch[lane * 8 + j] = a[j];
        }
        asm volatile("s_waitcnt lgkmcnt(0)" ::: "memory");
        if (lane == 0) *lf = tag;
        return;
    }
#pragma unroll 1
    for (unsigned spins = 0; spins < 1000000u && *lf != tag; ++spins) __builtin_amdgcn_s_sleep(2);
    if (lane < 8) {
#pragma unroll
        for (int j = 0; j < 8; ++j) a[j] += xch[lane * 8 + j];
        const int c = (sidx & 7) * 192 + cw * 64 + lane * 8;
        const unsigned o = ((c >> 4) * 32 + (sidx < 8 ? 0 : 16) + (c & 15)) * 4;
        gstore4<true>(p.ws + WS_CSG, o, u32x4{__float_as_uint(a[0]), __float_as_uint(a[1]), __float_as_uint(a[2]), __float_as_uint(a[3])});
        gstore4<true>(p.ws + WS_CSG + 16, o, u32x4{__float_as_uint(a[4]), __float_as_uint(a[5]), __float_as_uint(a[6]), __float_as_uint(a[7])});
    }
    signal_flag(p.ws + WS_FLAGS + FL_U + (size_t)(sidx * 3 + cw) * 8, tag);
}

struct XRow {
    const char* xs; unsigned v0, v1, dst;
    __device__ __forceinline__ XRow(const char* x0, int tm, int lw, unsigned lbase) {
        const int lane = TID() & 63, r8 = lane >> 3;
        xs = x0 + (size_t)(tm * 128 + lw * 64) * 1280;
        v0 = r8 * 1280 + (((lane & 7) ^ (r8 >> 1)) * 16);
        v1 = (r8 + 8) * 1280 - 1024 + (((lane & 7) ^ (4 + (r8 >> 1))) * 16);
        dst = lbase + lw * 8192;
    }
    __device__ __forceinline__ void operator()(int d, int slot) const {
        constexpr unsigned d2 = 16 * 1280 - 2048;
        glds4<false>(xs + d * 128, v0, v1, v0 + d2, v1 + d2, dst + slot * 16384);
        glds4<false>(xs + d * 128 + 32 * 1280, v0, v1, v0 + d2, v1 + d2, dst + slot * 16384 + 4096);
    }
};
__device__ __forceinline__ void startup(char* smem, const Params& p) {
    int tm, tn;
    const bool has = qkv_tile(tm, tn);
    const int w = __builtin_amdgcn_readfirstlane(TID() >> 6);
    const unsigned lbase = (unsigned)(uintptr_t)(lds_t)smem;
    if (w == 8) {
        if (has) {
            const QkvW qk(p.wqkv, tn);
#pragma unroll 1
            for (int s = 0; s < 3; ++s) qk(s, lbase + WR_OFF + s * W_SLOT);
        }
    } else if ((w == 4 || w == 5) && has) {
        const XRow xr(p.x0, tm, w - 4, lbase);
        xr(0, 0); xr(1, 1);
    }
}

template <int L, int WS0>
__device__ __forceinline__ void qkv_phase(char* smem, const Params& p, const char* X, const char* stats, char* Qb, char* KVb, const char* dep,
                                          char* sig, u64 tag) {
    int tm, tn;
    const bool has = qkv_tile(tm, tn);
    const int lane = TID() & 63;
    const int w = __builtin_amdgcn_readfirstlane(TID() >> 6);
    const unsigned lbase = (unsigned)(uintptr_t)(lds_t)smem;
    STAMP(0, 0);
    if (!has) {
        if (L == 0 && w < 6) pre_csg(smem, p, tag);
        return;
    }
    if (w >= 6) {
        idle_barriers<QKV_NST + 2>();
        if (L == 0) BAR();
    } else if (w < 4) {
        if (tn >= 10) qkv_compute<L == 0, WS0, true>(smem, stats, Qb, KVb, sig, tag, tm, tn, w, L);
        else qkv_compute<L == 0, WS0, false>(smem, stats, Qb, KVb, sig, tag, tm, tn, w, L);
    } else if (L) {
        const int lw = w - 4;
        const char* xs = X + (size_t)(tm * 4 + lw * 2) * 40 * 1024;
        const unsigned xv = lane * 16, xv2 = xv + 40 * 1024 - 2048;
        wait_flags(dep + (size_t)tm * FL_GROUP + lw * 2048, 32, tag);
        STAMPT(0, 5, 256);
        ring_loader<QKV_NST, X_NS, PDX, 4>([&](int s, int slot) { glds4<true>(xs + s * 2048, xv, xv, xv2, xv2, lbase + slot * X_SLOT + lw * 4096); });
        BAR(); BAR();
    } else {
        const XRow issue(p.x0, tm, w - 4, lbase);
        int slot = 2;
#pragma unroll 1
        for (int d = 0; d < 8; ++d) {
            WAIT_V(8);
            BAR();
            BAR();
            issue(d + 2, slot);
            slot = slot == 2 ? 0 : slot + 1;
        }
        WAIT_V(8);
        BAR(); BAR();
        WAIT_V(0);
        BAR(); BAR();
        BAR(); BAR(); BAR();
    }
}

constexpr int AT_SLOT = 8 * 3072, AT_NS = 3, AT_NCH = 8;

__device__ __forceinline__ float rowmax32(const f32x32& s) {
    float a = fmaxf(fmaxf(s[0], s[1]), s[2]), b = fmaxf(fmaxf(s[3], s[4]), s[5]);
#pragma unroll
    for (int r = 6; r < 30; r += 4) { a = fmaxf(fmaxf(a, s[r]), s[r + 1]); b = fmaxf(fmaxf(b, s[r + 2]), s[r + 3]); }
    a = fmaxf(fmaxf(a, s[30]), s[31]);
    return xmax32(fmaxf(a, b));
}

__device__ __forceinline__ void attn_tile(int& head, int& qblk) {
    const int b = BID(), xcd = b & 7, j = b >> 3;
    head = xcd * 4 + (j & 3); qblk = j >> 2;
}
__device__ __forceinline__ void kv_piece_offs(int head, unsigned (&v)[3]) {
    const int lane = TID() & 63;
    const int c0 = (head * 5) >> 1, odd = head & 1;
    v[0] = (c0 + odd) * 512 + lane * 16;
#pragma unroll
    for (int i = 1; i < 3; ++i) {
        const int idx = i * 64 + lane - 96, sh = idx / 24, row = idx - sh * 24;
        unsigned o = row < 20 ? KV_V + sh * 10240 + (head * 20 + row) * 16 : KV_ONES + (row - 20) * 16;
        if (idx < 0) o = (odd ? c0 : c0 + 2) * 512 + lane * 16;
        v[i] = o - i * 1024;
    }
}

__device__ __forceinline__ void attn_phase(char* smem, const char* Qb, const char* KVb, char* O, char* sig, u64 tag, int L) {
    const int tid = TID(), lane = tid & 63;
    const int w = __builtin_amdgcn_readfirstlane(tid >> 6);
    int head, qblk;
    attn_tile(head, qblk);
    const int r32 = lane & 31, hh = lane >> 5;
    const int qtile = qblk * 8 + w;
    const int c0 = (head * 5) >> 1, odd = head & 1;
    STAMP(1, 0);
    const unsigned lbase = (unsigned)(uintptr_t)(lds_t)smem;
    unsigned pv[3];
    kv_piece_offs(head, pv);
    const unsigned qhv = (odd ? c0 : c0 + 2) * 512 + r32 * 16 + odd * 8;
    const int koff = lane * 16, khoff = hh * 3072 + 1024 + r32 * 16 + odd * 8;
    const int vrow = r32 <= 20 ? r32 : (r32 < 28 ? 20 : 16);
    const int voff = 1536 + hh * 384 + vrow * 16;
    asm volatile("" : "+v"(pv[0]), "+v"(pv[1]), "+v"(pv[2]));
    BAR();
    STAMP(1, 5);
    glds3<true>(KVb + (size_t)w * KV_REC, pv[0], pv[1], pv[2], lbase + w * 3072);
    bf16x8 q0;
    s16x4 q4;
    {
        const char* qp = Qb + (size_t)qtile * 40960;
        asm volatile("global_load_dwordx4 %0, %1, %2 sc1" : "=v"(q0) : "v"(pv[0]), "s"(qp) : "memory");
        asm volatile("global_load_dwordx2 %0, %1, %2 sc1" : "=v"(q4) : "v"(qhv), "s"(qp) : "memory");
    }
    f32x16 zero;
#pragma unroll
    for (int r = 0; r < 16; ++r) zero[r] = 0.f;
    f32x16 o = zero, m16;
    f32x32 SA, SBn;
    asm volatile("s_waitcnt vmcnt(0)" : "+v"(q0), "+v"(q4) :: "memory");
    BAR();
    STAMP(1, 1);
    bf16x8 ka, kb, va0, va1, vb0, vb1;
    s16x4 kh;
    u32x4 pa0, pa1, pb0, pb1 = {0u, 0u, 0u, 0u};
    {
        ka = ldsr128(smem + koff); kb = ldsr128(smem + 3072 + koff); kh = ldsr64(smem + khoff);
        f32x16 n0 = MFMA32(ka, q0, zero), n1 = MFMA32(kb, q0, zero);
        SA = MFMA2B(kh, q4, CAT16(n0, n1));
        const float m = rowmax32(SA);
#pragma unroll
        for (int r = 0; r < 16; ++r) m16[r] = -m;
#pragma unroll
        for (int r = 0; r < 32; ++r) SA[r] -= m;
        ka = ldsr128(smem + 2 * 3072 + koff); kb = ldsr128(smem + 3 * 3072 + koff); kh = ldsr64(smem + 2 * 3072 + khoff);
        va0 = ldsr128(smem + voff); va1 = ldsr128(smem + voff + 768); vb0 = ldsr128(smem + 3072 + voff); vb1 = vb0;
    }
    asm volatile("" : "+v"(m16));
    LGKM0();
    SB();
#define EX(x) x = __builtin_amdgcn_exp2f(x)
#define BF8(x) __builtin_bit_cast(bf16x8, x)
#define G4(S, i, P, jj) EX(S[i]); EX(S[i + 1]); SB(); EX(S[i + 2]); P[jj] = cvtpk(S[i], S[i + 1]); SB(); EX(S[i + 3]); SB();
#define STEP2(S, SN, V1, V2, V3, K4, K5, KH4) do { \
        f32x16 n0, n1; \
        o = MFMA32(vb1, BF8(pb1), o); SB(); vb1 = ldsr128((V1) + 768); SB(); \
        G4(S, 0, pa0, 0) \
        n0 = MFMA32(ka, q0, m16); SB(); ka = ldsr128(K4); pa0[1] = cvtpk(S[2], S[3]); SB(); \
        G4(S, 4, pa0, 2) \
        n1 = MFMA32(kb, q0, m16); SB(); kb = ldsr128(K5); pa0[3] = cvtpk(S[6], S[7]); SB(); \
        G4(S, 8, pa1, 0) \
        o = MFMA32(va0, BF8(pa0), o); SB(); va0 = ldsr128(V2); pa1[1] = cvtpk(S[10], S[11]); SB(); \
        G4(S, 12, pa1, 2) \
        SN = MFMA2B(kh, q4, CAT16(n0, n1)); SB(); kh = ldsr64(KH4); pa1[3] = cvtpk(S[14], S[15]); SB(); \
        G4(S, 16, pb0, 0) \
        pb0[1] = cvtpk(S[18], S[19]); SB(); \
        G4(S, 20, pb0, 2) \
        o = MFMA32(va1, BF8(pa1), o); SB(); va1 = ldsr128((V2) + 768); pb0[3] = cvtpk(S[22], S[23]); SB(); \
        G4(S, 24, pb1, 0) \
        pb1[1] = cvtpk(S[26], S[27]); SB(); \
        o = MFMA32(vb0, BF8(pb0), o); SB(); vb0 = ldsr128(V3); SB(); \
        G4(S, 28, pb1, 2) \
        pb1[3] = cvtpk(S[30], S[31]); SB(); \
    } while (0)
    int slot = 0;
#pragma unroll 1
    for (int c = 0; c < AT_NCH; ++c) {
        const int nslot = slot + 1 == AT_NS ? 0 : slot + 1;
        const char* cv = smem + slot * AT_SLOT + voff;   const char* ck = smem + slot * AT_SLOT + koff;   const char* ch = smem + slot * AT_SLOT + khoff;
        const char* nv = smem + nslot * AT_SLOT + voff;  const char* nk = smem + nslot * AT_SLOT + koff;  const char* nh = smem + nslot * AT_SLOT + khoff;
        STEP2(SA, SBn, cv + 1 * 3072, cv + 2 * 3072, cv + 3 * 3072, ck + 4 * 3072, ck + 5 * 3072, ch + 4 * 3072);
        STEP2(SBn, SA, cv + 3 * 3072, cv + 4 * 3072, cv + 5 * 3072, ck + 6 * 3072, ck + 7 * 3072, ch + 6 * 3072);
        BAR();
        SB();
        STEP2(SA, SBn, cv + 5 * 3072, cv + 6 * 3072, cv + 7 * 3072, nk, nk + 3072, nh);
        STEP2(SBn, SA, cv + 7 * 3072, nv, nv + 3072, nk + 2 * 3072, nk + 3 * 3072, nh + 2 * 3072);
        slot = nslot;
    }
    o = MFMA32(vb1, BF8(pb1), o);
    STAMP(1, 2);
    float l;
    {
        auto rr = __builtin_amdgcn_permlane32_swap(__float_as_uint(o[8]), __float_as_uint(o[8]), false, false);
        l = __uint_as_float(rr[1]);
    }
    float big = fmaxf(fmaxf(fabsf(o[0]), fabsf(o[1])), fabsf(o[2]));
#pragma unroll
    for (int r = 3; r < 9; r += 2) big = fmaxf(fmaxf(big, fabsf(o[r])), fabsf(o[r + 1]));
    float sum = ((o[0] + o[1]) + (o[2] + o[3])) + ((o[4] + o[5]) + (o[6] + o[7])) + o[8];
    if (!hh) { big = fmaxf(fmaxf(big, fabsf(o[9])), fmaxf(fabsf(o[10]), fabsf(o[11]))); sum += (o[9] + o[10]) + o[11]; }
    const bool bad = !(l < 1.8446744073709552e19f) || !(big < 1.0e37f) || !(sum == sum);
    if (__builtin_expect(__any(bad), 0)) {
        float m = -INFINITY;
        o = zero;
        const unsigned gk = (c0 + odd) * 512 + lane * 16, gh = hh * KV_REC + qhv;
        const unsigned gv0 = r32 < 20 ? KV_V + hh * 10240 + (head * 20 + r32) * 16 : KV_ONES, gv1 = r32 < 20 ? gv0 + 20480 : KV_ONES;
#pragma unroll 1
        for (int t = 0; t < 64; t += 2) {
            const char* tp = KVb + (size_t)t * KV_REC;
            bf16x8 fk[2], fv[4];
            s16x4 fh;
            asm volatile("global_load_dwordx4 %0, %7, %11 sc1\n\tglobal_load_dwordx4 %1, %7, %12 sc1\n\tglobal_load_dwordx2 %2, %8, %11 sc1\n\t"
                         "global_load_dwordx4 %3, %9, %11 sc1\n\tglobal_load_dwordx4 %4, %10, %11 sc1\n\t"
                         "global_load_dwordx4 %5, %9, %12 sc1\n\tglobal_load_dwordx4 %6, %10, %12 sc1\n\ts_waitcnt vmcnt(0)"
                         : "=&v"(fk[0]), "=&v"(fk[1]), "=&v"(fh), "=&v"(fv[0]), "=&v"(fv[1]), "=&v"(fv[2]), "=&v"(fv[3])
                         : "v"(gk), "v"(gh), "v"(gv0), "v"(gv1), "s"(tp), "s"(tp + KV_REC) : "memory");
            f32x16 n0 = MFMA32(fk[0], q0, zero), n1 = MFMA32(fk[1], q0, zero);
            f32x32 sc = MFMA2B(fh, q4, CAT16(n0, n1));
            const float mn = fmaxf(m, rowmax32(sc));
            const float alpha = __builtin_amdgcn_exp2f(m - mn);
            m = mn;
#pragma unroll
            for (int r = 0; r < 16; ++r) o[r] *= alpha;
#pragma unroll
            for (int q = 0; q < 4; ++q) {
                u32x4 pp;
#pragma unroll
                for (int e = 0; e < 4; ++e) pp[e] = cvtpk(__builtin_amdgcn_exp2f(sc[q * 8 + 2 * e] - mn), __builtin_amdgcn_exp2f(sc[q * 8 + 2 * e + 1] - mn));
                o = MFMA32(fv[q], BF8(pp), o);
            }
        }
        auto rr = __builtin_amdgcn_permlane32_swap(__float_as_uint(o[8]), __float_as_uint(o[8]), false, false);
        l = __uint_as_float(rr[1]);
    }
    const float inv = __builtin_amdgcn_rcpf(l);
    char* ob = O + (size_t)qtile * 40 * 1024;
#pragma unroll
    for (int g = 0; g < 3; ++g) {
        if (g < 2 || !hh) {
            const int col = head * 20 + g * 8 + hh * 4;
            gstore2<true>(ob, (col >> 3) * 512 + (col & 4) * 2 + r32 * 16,
                           u32x2{cvtpk(o[g * 4] * inv, o[g * 4 + 1] * inv), cvtpk(o[g * 4 + 2] * inv, o[g * 4 + 3] * inv)});
        }
    }
    STAMP(1, 4);
    signal_flag(sig + (size_t)(qtile >> 1) * FL_GROUP + ((qtile & 1) * 32 + head) * 64, tag);
    STAMP(1, 3);
    BAR_L();
}
#undef EX
#undef G4
#undef STEP2

constexpr int GU_NST = 20;
__device__ __forceinline__ void gu_tile(int& tm, int& tn) {
    const int b = BID(), xcd = b & 7, j0 = b >> 3;
    tm = (xcd >> 2) * 8 + (j0 & 7); tn = (xcd & 3) * 4 + (j0 >> 3);
}
struct GuW {
    const char* wb; unsigned lv;
    __device__ __forceinline__ GuW(const char* GT) { wb = GT; lv = (TID() & 63) * 16; }
    __device__ __forceinline__ void operator()(int s, unsigned d) const {
#pragma unroll
        for (int i = 0; i < 3; ++i) glds4<true>(wb + s * 12288 + i * 4096, lv, lv, lv, lv, d + i * 4096);
    }
};
struct GuNat {
    const char *gb, *ub; unsigned gv[3];
    __device__ __forceinline__ GuNat(const char* Wg, const char* Wu, int tn) {
        const int lane = TID() & 63;
#pragma unroll
        for (int i = 0; i < 3; ++i) {
            const int idx = i * 64 + lane, kq = idx / 48, rem = idx - kq * 48, sub = rem >> 4, kr = (rem >> 2) & 3, c = rem & 3;
            gv[i] = (kq * 4 + kr) * 3072 + sub * 64 + c * 16 - i * 1024;
        }
        gb = Wg + tn * 192; ub = Wu + tn * 192;
    }
    __device__ __forceinline__ void operator()(int s, unsigned d) const {
        const size_t o = (size_t)s * (32 * 3072);
        glds3(gb + o, gv[0], gv[1], gv[2], d);
        glds3(ub + o, gv[0], gv[1], gv[2], d + 6144);
        glds3(gb + o + 16 * 3072, gv[0], gv[1], gv[2], d + 3072);
        glds3(ub + o + 16 * 3072, gv[0], gv[1], gv[2], d + 9216);
    }
};

template <int WS0>
__device__ __forceinline__ void gu_phase(char* smem, const char* X, const char* stats, char* Hh, const char* dep, char* sig, u64 tag,
                                         int L) {
    const int lane = TID() & 63;
    const int w = __builtin_amdgcn_readfirstlane(TID() >> 6);
    int tm, tn;
    gu_tile(tm, tn);
    const unsigned lbase = (unsigned)(uintptr_t)(lds_t)smem;
    STAMP(3, 0);
    if (w >= 6) { idle_barriers<GU_NST + 2>(); return; }
    if (w >= 4) {
        const int lw = w - 4;
        const char* xs = X + (size_t)(tm * 4 + lw * 2) * 40 * 1024;
        const unsigned xv = lane * 16, xv2 = xv + 40 * 1024 - 2048;
        wait_flags(dep + (size_t)tm * FL_GROUP + lw * 2048, 32, tag);
        STAMPT(3, 5, 256);
        ring_loader<GU_NST, X_NS, PDX, 4>([&](int s, int slot) { glds4<true>(xs + s * 2048, xv, xv, xv2, xv2, lbase + slot * X_SLOT + lw * 4096); });
        BAR(); BAR();
        return;
    }
    const int wm = w & 1, wn = w >> 1;
    const int hh = lane >> 5;
    const int xoff = wm * 4096 + lane * 16;
    int woff = WR_OFF + wn * 3072 + lane * 16;
    f32x16 acc[3][2];
#pragma unroll
    for (int i = 0; i < 3; ++i)
#pragma unroll
        for (int j = 0; j < 2; ++j)
#pragma unroll
            for (int r = 0; r < 16; ++r) acc[i][j][r] = 0.f;
    bf16x8 xf[2][2], wf[2][3];
    u32x2 part[8];
    asm volatile("" : "+v"(acc[0][0]), "+v"(acc[0][1]), "+v"(acc[1][0]), "+v"(acc[1][1]), "+v"(acc[2][0]), "+v"(acc[2][1]), "+v"(woff));
    BAR();
    STAMP(3, 1);
#pragma unroll
    for (int ks = 0; ks < 2; ++ks) {
#pragma unroll
        for (int j = 0; j < 2; ++j) xf[ks][j] = ldsr128(smem + xoff + ks * 1024 + j * 2048);
#pragma unroll
        for (int i = 0; i < 3; ++i) wf[ks][i] = ldsr128(smem + WS0 * W_SLOT + woff + ks * 6144 + i * 1024);
    }
    ln_issue(part, stats, tm * 128 + wm * 64);
    LGKM0();
    int xs = X_SLOT, ws = ((WS0 + 1) & (W_NS - 1)) * W_SLOT;
    int xa = xoff, wa = WS0 * W_SLOT + woff;
#define GM(ks, i, j) acc[i][j] = MFMA32(wf[ks][i], xf[ks][j], acc[i][j]); SB()
#define GW(ks, i) wf[ks][i] = ldsr128(smem + wa + ks * 6144 + i * 1024)
#define GX(ks, j) xf[ks][j] = ldsr128(smem + xa + ks * 1024 + j * 2048)
#pragma unroll 1
    for (int s = 0; s < GU_NST; ++s) {
        GM(0, 0, 0); GW(1, 2); GX(1, 1); SB();
        xa = xs + xoff;
        wa = ws + woff;
        asm volatile("" : "+v"(xa), "+v"(wa));
        SB();
        GM(0, 0, 1); BAR(); SB();
        GM(0, 1, 0); GW(0, 0); SB();
        GM(0, 1, 1);
        xs = xs + X_SLOT == X_NS * X_SLOT ? 0 : xs + X_SLOT;
        ws = ws + W_SLOT == W_NS * W_SLOT ? 0 : ws + W_SLOT;
        asm volatile("" : "+s"(xs), "+s"(ws));
        SB();
        GM(0, 2, 0); GW(0, 1); SB();
        GM(0, 2, 1); GX(0, 0); SB();
        GM(1, 0, 0); GW(0, 2); GX(0, 1); SB();
        GM(1, 0, 1);
        GM(1, 1, 0); GW(1, 0); SB();
        GM(1, 1, 1);
        GM(1, 2, 0); GW(1, 1); SB();
        GM(1, 2, 1); GX(1, 0); SB();
    }
#undef GW
#undef GX
#undef GM
    STAMP(3, 2);
    const float* csl = (const float*)(smem + CS_OFF);
    f32x2 st[2];
    split_halves(ln_collect(part), st[0], st[1]);
    char* hb = Hh + (size_t)((tm * 4 + wm * 2) * 96 + tn * 6 + wn * 3) * 1024;
#pragma unroll
    for (int i = 0; i < 3; ++i) {
        const int t = wn * 3 + i;
        f32x4 c4[4];
#pragma unroll
        for (int g = 0; g < 4; ++g) c4[g] = *(const f32x4*)(csl + t * 32 + g * 8 + hh * 4);
#pragma unroll
        for (int j = 0; j < 2; ++j) {
            u32x4 o;
#pragma unroll
            for (int e = 0; e < 4; ++e) {
                const f32x2 cg = {c4[e >> 1][(e & 1) * 2], c4[e >> 1][(e & 1) * 2 + 1]}, cu = {c4[2 + (e >> 1)][(e & 1) * 2], c4[2 + (e >> 1)][(e & 1) * 2 + 1]};
                f32x2 ga = f32x2{acc[i][j][2 * e], acc[i][j][2 * e + 1]} * st[j][0] + cg * st[j][1];
                const f32x2 up = f32x2{acc[i][j][8 + 2 * e], acc[i][j][9 + 2 * e]} * st[j][0] + cu * st[j][1];
                ga[0] = fmaxf(ga[0], 0.f); ga[1] = fmaxf(ga[1], 0.f);
                const f32x2 hv = ga * up;
                o[e] = cvtpk(hv[0], hv[1]);
            }
            gstore4<true>(hb + (j * 96 + i) * 1024, lane * 16, o);
        }
    }
    STAMP(3, 4);
    signal_flag(sig + (size_t)(tm * 2 + wm) * FL_GROUP + (tn * 2 + wn) * 64, tag);
    STAMP(3, 3);
    BAR_L();
}

__device__ __forceinline__ void proj_tile(int& tm, int& tn) {
    const int b = BID(), xcd = b & 7, j0 = b >> 3;
    tm = xcd * 4 + (j0 & 3); tn = j0 >> 2;
}
struct ProjW {
    const char* wb; unsigned lv;
    __device__ __forceinline__ ProjW(const char* WT) { wb = WT; lv = (TID() & 63) * 16; }
    __device__ __forceinline__ void operator()(int s, unsigned d) const {
        glds4<true>(wb + s * 10240, lv, lv, lv, lv, d);
        glds4<true>(wb + s * 10240 + 4096, lv, lv, lv, lv, d + 4096);
        glds2<true>(wb + s * 10240 + 8192, lv, lv, d + 8192);
    }
};

template <int KTOT, int WS0>
__device__ __forceinline__ void proj_phase(char* smem, const char* A, const char* Xold, char* Xnew, char* stats, char* out,
                                           const char* dep, char* sig, u64 tag, int L) {
    constexpr int NST = KTOT / 64, KS16 = KTOT / 16, KID = KTOT == 640 ? 2 : 4;
    const int lane = TID() & 63;
    const int w = __builtin_amdgcn_readfirstlane(TID() >> 6);
    int tm, tn;
    proj_tile(tm, tn);
    const unsigned lbase = (unsigned)(uintptr_t)(lds_t)smem;
    STAMP(KID, 0);
    if (w >= 6) { idle_barriers<NST + 2>(); return; }
    if (w >= 4) {
        const int lw = w - 4;
        const char* as = A + (size_t)((tm * 2 + lw) * KS16) * 1024;
        const unsigned xv = lane * 16;
        wait_flags(dep + (size_t)tm * FL_GROUP + (KTOT == 640 ? lw * 2048 : 0), 32, tag);
        STAMPT(KID, 5, 256);
        ring_loader<NST, X_NS, PDXP, 4>([&](int s, int slot) { glds4<true>(as + s * 4096, xv, xv, xv, xv, lbase + slot * X_SLOT + lw * 4096); });
        BAR(); BAR();
        return;
    }
    const int g = lane >> 4, c16 = lane & 15;
    const int r32 = (w & 1) * 16 + c16;
    const unsigned tilv = (g >> 1) * 512 + r32 * 16 + (g & 1) * 8;
    const size_t tilb = (size_t)((tm * 2 + (w >> 1)) * 40 + tn * 5) * 1024;
    u32x2 res[5];
    {
        const bool rowm = KTOT == 640 && L == 0;
        const char* rb = rowm ? Xold + ((size_t)(tm * 64 + w * 16) * 640 + tn * 80) * 2 : Xold + tilb;
        const unsigned rv = rowm ? c16 * 1280 + g * 8 : tilv;
        const int rs = rowm ? 32 : 1024;
#pragma unroll
        for (int i = 0; i < 5; ++i) res[i] = gload2<true>(rb + i * rs, rv);
    }
    const int aoff = (w >> 1) * 4096 + g * 512 + r32 * 16;
    const int woff = WR_OFF + lane * 16;
    f32x4 acc[5];
#pragma unroll
    for (int i = 0; i < 5; ++i) acc[i] = f32x4{0.f, 0.f, 0.f, 0.f};
    bf16x8 af[2], wf[2][5];
    asm volatile("" : "+v"(acc[0]), "+v"(acc[1]), "+v"(acc[2]), "+v"(acc[3]), "+v"(acc[4]));
    BAR();
    STAMP(KID, 1);
#pragma unroll
    for (int kk = 0; kk < 2; ++kk) {
        af[kk] = ldsr128(smem + aoff + kk * 2048);
#pragma unroll
        for (int i = 0; i < 5; ++i) wf[kk][i] = ldsr128(smem + WS0 * W_SLOT + woff + (kk * 5 + i) * 1024);
    }
    LGKM0();
    int xs = X_SLOT, ws = ((WS0 + 1) & (W_NS - 1)) * W_SLOT;
    int xa = aoff, wa = WS0 * W_SLOT + woff;
#define PM(kk, i) acc[i] = MFMA16(wf[kk][i], af[kk], acc[i]); SB()
#define PW(kk, i) wf[kk][i] = ldsr128(smem + wa + (kk * 5 + i) * 1024)
#pragma unroll 1
    for (int s = 0; s < NST; ++s) {
        PM(0, 0); PW(1, 4); af[1] = ldsr128(smem + xa + 2048); SB();
        xa = xs + aoff; wa = ws + woff;
        asm volatile("" : "+v"(xa), "+v"(wa));
        SB();
        PM(0, 1); BAR(); SB();
        PM(0, 2); PW(0, 0); PW(0, 1); SB();
        PM(0, 3); PW(0, 2); SB();
        PM(0, 4); PW(0, 3); SB();
        PM(1, 0); PW(0, 4); af[0] = ldsr128(smem + xa); SB();
        PM(1, 1); PW(1, 0); SB();
        xs = xs + X_SLOT == X_NS * X_SLOT ? 0 : xs + X_SLOT;
        ws = ws + W_SLOT == W_NS * W_SLOT ? 0 : ws + W_SLOT;
        asm volatile("" : "+s"(xs), "+s"(ws));
        PM(1, 2); PW(1, 1); SB();
        PM(1, 3); PW(1, 2); SB();
        PM(1, 4); PW(1, 3); SB();
    }
#undef PM
#undef PW
    STAMP(KID, 2);
    asm volatile("s_waitcnt vmcnt(0)" : "+v"(res[0]), "+v"(res[1]), "+v"(res[2]), "+v"(res[3]), "+v"(res[4]) :: "memory");
    u32x2 o[5];
#pragma unroll
    for (int i = 0; i < 5; ++i) {
        acc[i] += f32x4{bflo(res[i][0]), bfhi(res[i][0]), bflo(res[i][1]), bfhi(res[i][1])};
        o[i] = u32x2{cvtpk(acc[i][0], acc[i][1]), cvtpk(acc[i][2], acc[i][3])};
    }
    if (KTOT == 1536 && out != nullptr) {
        char* ob = out + ((size_t)(tm * 64 + w * 16) * 640 + tn * 80) * 2;
#pragma unroll
        for (int i = 0; i < 5; ++i) gstore2<false>(ob + i * 32, c16 * 1280 + g * 8, o[i]);
    } else {
#pragma unroll
        for (int i = 0; i < 5; ++i) gstore2<true>(Xnew + tilb + i * 1024, tilv, o[i]);
        f32x4 s1 = acc[0], s2 = acc[0] * acc[0];
#pragma unroll
        for (int i = 1; i < 5; ++i) { s1 += acc[i]; s2 += acc[i] * acc[i]; }
        const float t1 = xsum16_32((s1[0] + s1[1]) + (s1[2] + s1[3])), t2 = xsum16_32((s2[0] + s2[1]) + (s2[2] + s2[3]));
        if (g == 0) gstore2<true>(stats + ((size_t)tn * S_ + tm * 64 + w * 16) * 8, c16 * 8, u32x2{__float_as_uint(t1), __float_as_uint(t2)});
        STAMP(KID, 4);
        signal_flag(sig + (size_t)(tm >> 1) * FL_GROUP + ((tm & 1) * 32 + tn * 4 + w) * 64, tag);
    }
    STAMP(KID, 3);
    BAR_L();
}

constexpr int WS_WO = 2, WS_GU = (WS_WO + 10) & 7, WS_WD = (WS_GU + 20) & 7, WS_QKV = (WS_WD + 24) & 7;

template <int NST, int WS0, class Cur, class Next>
__device__ __forceinline__ void w_phase(unsigned ring, const Cur& cur, const Next& next) {
    int slot = (WS0 + W_PD) & (W_NS - 1);
#pragma unroll 1
    for (int s = 0; s < NST - W_PD; ++s) {
        WAIT_V(40);
        BAR();
        cur(s + W_PD, ring + slot * W_SLOT);
        slot = (slot + 1) & (W_NS - 1);
    }
#pragma unroll 1
    for (int s = 0; s < W_PD; ++s) {
        WAIT_V(40);
        BAR();
        next(s, ring + slot * W_SLOT);
        slot = (slot + 1) & (W_NS - 1);
    }
    BAR(); BAR();
}
template <int NST, int WS0, int D, class Cur>
__device__ __forceinline__ void w_phase_last(unsigned ring, const Cur& cur) {
    int slot = (WS0 + W_PD) & (W_NS - 1);
#pragma unroll 1
    for (int s = 0; s < NST - W_PD; ++s) {
        WAIT_V(40);
        BAR();
        cur(s + W_PD, ring + slot * W_SLOT);
        slot = (slot + 1) & (W_NS - 1);
    }
#pragma unroll
    for (int r = W_PD - 1; r >= 0; --r) {
        WAIT_V(r * D < 40 ? r * D : 40);
        BAR();
    }
    BAR(); BAR();
}

__device__ __forceinline__ void loader_wave(char* smem, const Params& p, u64 tag) {
    const int lane = TID() & 63;
    const unsigned lbase = (unsigned)(uintptr_t)(lds_t)smem, ring = lbase + WR_OFF;
    const unsigned lv = lane * 16;
    int ptm, ptn, gtm, gtn, qtm, qtn, head, qblk;
    const bool hasq = qkv_tile(qtm, qtn);
    const QkvW qk(p.wqkv, qtn);
    if (hasq) {
        ring_loader<QKV_NST, W_NS, 3, 8, false>([&](int s, int slot) { qk(s, ring + slot * W_SLOT); });
        BAR(); BAR(); BAR();
    }
    static_assert(WS_QKV == 0, "first QKV phase starts at slot 0");
    proj_tile(ptm, ptn);
    gu_tile(gtm, gtn);
    attn_tile(head, qblk);
    char* wt = p.ws + WS_WT + (size_t)ptn * 68 * 5120;
    const ProjW wo(wt), wd(wt + 20 * 5120);
    char* gt = p.ws + WS_GT + (size_t)gtn * 20 * 12288;
    const GuW gu(gt);
    unsigned pv[3];
    kv_piece_offs(head, pv);
    const unsigned fr = ((head * 20 + ((lane >> 2) & 1) * 19) >> 6) * 256 + (lane & 3) * 64;
    const unsigned fo = (((lane >> 3) & 1) + 1) * 2560 + fr;
    const unsigned fo0 = lane < 32 ? (lane >> 4) * 8192 + fo : qblk * 8192 + fr;
#pragma unroll 1
    for (int L = 0; L < 2; ++L) {
        char* fl = p.ws + WS_FLAGS + (size_t)L * FL_BYTES;
        const char* kv = p.ws + WS_L0 + (size_t)L * L_BYTES + L_KV;
        __builtin_amdgcn_s_sleep(POLL_PRE);
        wait_flags_at(fl + FL_Q, fo0, lane < 40, tag);
        BAR();
        int slot = 1;
#pragma unroll 1
        for (int c = 1; c < AT_NCH; ++c) {
            BAR();
#pragma unroll
            for (int i = 0; i < 8; ++i) glds3<true>(kv + (size_t)(c * 8 + i) * KV_REC, pv[0], pv[1], pv[2], lbase + slot * AT_SLOT + i * 3072);
            if (c + 1 < AT_NCH) wait_flags_at(fl + FL_Q + (c + 1) * 8192, fo, lane < 16, tag);
            WAIT_V(0);
            slot = slot + 1 == AT_NS ? 0 : slot + 1;
            if (L == 0) {
                constexpr int SCR = WR_OFF + 7 * W_SLOT;
                const int u = ptm + (c - 2) * 32;
                if (c >= 2 && c <= 4 && u < 68) {
                    const bool isd = u >= 20;
                    const int g = lane >> 4;
                    const int q0 = isd ? (g >> 1) * 4 + (g & 1) : g * 2, qd = isd ? 1536 : 768;
                    const char* rp = smem + SCR + (c & 1) * 6144 + q0 * 768 + ((lane >> 2) & 3) * 64 + (lane & 3) * 8;
#pragma unroll
                    for (int i = 0; i < 5; ++i) {
                        const int c2 = (ptn & 1) + i;
                        const char* r2 = rp + (c2 >> 1) * 256 + (c2 & 1) * 32;
                        const bf16x8 f = __builtin_shufflevector(ldstr(r2), ldstr(r2 + qd), 0, 1, 2, 3, 4, 5, 6, 7);
                        gstore4<true>(wt + u * 5120 + i * 1024, lv, __builtin_bit_cast(u32x4, f));
                    }
                }
                const int un = u + 32;
                if (c <= 3 && un < 68) {
                    const char* src = (un >= 20 ? p.wd + (size_t)(un - 20) * 32 * 1280 : p.wo + (size_t)un * 32 * 1280) + ((ptn * 80) & ~31) * 2;
                    unsigned wv[3];
#pragma unroll
                    for (int i = 0; i < 3; ++i) {
                        const int idx = i * 64 + lane, kq = idx / 48, rem = idx - kq * 48;
                        wv[i] = (kq * 4 + ((rem >> 2) & 3)) * 1280 + (rem >> 4) * 64 + (rem & 3) * 16 - i * 1024;
                    }
                    const unsigned d = lbase + SCR + ((c + 1) & 1) * 6144;
                    glds3(src, wv[0], wv[1], wv[2], d);
                    glds3(src + 16 * 1280, wv[0], wv[1], wv[2], d + 3072);
                }
                const int gs = gtm + (c - 2) * 16;
                if ((c == 2 || c == 3) && gs < 20) {
                    const char* rp = smem + WR_OFF + (8 - c) * W_SLOT + ((lane >> 4) & 1) * 6144 + (lane >> 5) * 1536 + ((lane >> 2) & 3) * 64 + (lane & 3) * 8;
#pragma unroll
                    for (int i = 0; i < 12; ++i) {
                        const int ks = i / 6, t = i % 6;
                        const bf16x8 f = ldstr2(rp + ks * 3072 + (t >> 1) * 256 + (t & 1) * 32, 768);
                        gstore4<true>(gt + gs * 12288 + i * 1024, lv, __builtin_bit_cast(u32x4, f));
                    }
                }
                if (c <= 2 && gs + 16 < 20) {
                    const GuNat gn(p.wg, p.wu, gtn);
                    gn(gs + 16, ring + (7 - c) * W_SLOT);
                }
                if (c == 5) {
                    signal_flag(fl + FL_P + (size_t)ptn * FL_GROUP + ptm * 64, tag);
                    signal_flag(fl + FL_T + (size_t)(gtn * 16 + gtm) * 64, tag);
                }
                if (c == 6) wait_flags_at(fl, lane < 32 ? FL_P + ptn * FL_GROUP + lane * 64 : (lane < 48 ? FL_T + (gtn * 16 + (lane & 15)) * 64 :
                                              FL_U + ((lane & 2) * 12 + ((gtn * 96 + (lane & 1) * 95) >> 6)) * 8), lane < 52, tag);
            }
            if (c == 7) {
#pragma unroll 1
                for (int s = 0; s < 3; ++s) wo(s, ring + (WS_WO + s) * W_SLOT);
            }
        }
        BAR();
#pragma unroll 1
        for (int s = 3; s < W_PD; ++s) wo(s, ring + (WS_WO + s) * W_SLOT);
        BAR();
        BAR();
        w_phase<10, WS_WO>(ring, wo, gu);
        glds1<true>(p.ws + WS_CSG + gtn * 768, lv, lbase + CS_OFF);
        w_phase<GU_NST, WS_GU>(ring, gu, wd);
        if (L == 0 && hasq) {
            w_phase<24, WS_WD>(ring, wd, qk);
            w_phase_last<QKV_NST, WS_QKV, 8>(ring, qk);
        } else {
            w_phase_last<24, WS_WD, 10>(ring, wd);
        }
    }
}

__global__ __launch_bounds__(576, 3) void k_main(Params p) {
    __shared__ __attribute__((aligned(1024))) char smem[LDS_BYTES];
    startup(smem, p);
    u64 tag;
    asm volatile("s_load_dwordx2 %0, %1, 0x0\n\ts_waitcnt lgkmcnt(0)" : "=s"(tag) : "s"(p.ws + WS_TAG) : "memory");
    tag = tag * 0x9E3779B97F4A7C15ull + 0x632BE59BD9B4E019ull;
    if (__builtin_amdgcn_readfirstlane(threadIdx.x >> 6) == 8) {
        loader_wave(smem, p, tag);
        if (blockIdx.x == 0 && (threadIdx.x & 63) == 0) *(u64*)(p.ws + WS_TAG) = tag;
        return;
    }
    qkv_phase<0, WS_QKV>(smem, p, nullptr, nullptr, p.ws + WS_L0 + L_Q, p.ws + WS_L0 + L_KV, nullptr, p.ws + WS_FLAGS + FL_Q, tag);
#pragma unroll 1
    for (int L = 0; L < 2; ++L) {
        char* lw = p.ws + WS_L0 + (size_t)L * L_BYTES;
        char* fl = p.ws + WS_FLAGS + (size_t)L * FL_BYTES;
#define OPQ() asm volatile("" : "+s"(lw), "+s"(fl), "+s"(p.ws), "+s"(p.x0))
        OPQ();
        attn_phase(smem, lw + L_Q, lw + L_KV, lw + L_O, fl + FL_A, tag, L);
        OPQ();
        proj_phase<640, WS_WO>(smem, lw + L_O, L ? lw + L_X0 : p.x0, lw + L_X1, lw + L_SB, nullptr, fl + FL_A, fl + FL_W, tag, L);
        OPQ();
        gu_phase<WS_GU>(smem, lw + L_X1, lw + L_SB, lw + L_H, fl + FL_W, fl + FL_G, tag, L);
        OPQ();
        proj_phase<1536, WS_WD>(smem, lw + L_H, lw + L_X1, lw + L_BYTES + L_X0, lw + L_BYTES + L_SA, L ? p.out : nullptr, fl + FL_G,
                                fl + FL_BYTES + FL_D, tag, L);
        if (L) break;
        lw += L_BYTES; fl += FL_BYTES;
        OPQ();
        qkv_phase<1, WS_QKV>(smem, p, lw + L_X0, lw + L_SA, lw + L_Q, lw + L_KV, fl + FL_D, fl + FL_Q, tag);
    }
}

extern "C" void kernel_launch(void* const* d_in, const int* in_sizes, int n_in, void* d_out, int out_size, void* d_ws, size_t ws_size,
                              hipStream_t stream) {
    Params p;
    p.x0 = (const char*)d_in[0];
    p.wqkv = (const char*)d_in[1];
    p.wo = (const char*)d_in[2];
    p.wg = (const char*)d_in[4];
    p.wu = (const char*)d_in[5];
    p.wd = (const char*)d_in[6];
    p.ws = (char*)d_ws;
    p.out = (char*)d_out;
    k_main<<<256, 576, 0, stream>>>(p);
}
```

```cpp
#include <hip/hip_runtime.h>
#include <hip/hip_bf16.h>
#include <stdint.h>

typedef short bf16x8 __attribute__((ext_vector_type(8)));
typedef short s16x4 __attribute__((ext_vector_type(4)));
typedef float f32x32 __attribute__((ext_vector_type(32)));
typedef float f32x16 __attribute__((ext_vector_type(16)));
typedef float f32x4 __attribute__((ext_vector_type(4)));
typedef float f32x2 __attribute__((ext_vector_type(2)));
typedef unsigned u32x4 __attribute__((ext_vector_type(4)));
typedef unsigned u32x2 __attribute__((ext_vector_type(2)));
typedef unsigned short u16;
typedef __attribute__((address_space(3))) char* lds_t;
typedef __attribute__((address_space(1))) unsigned gu32;
typedef unsigned long long u64;
typedef __attribute__((address_space(1))) u64 gu64;

#define CODE_WARM 1
#define MAP 8
constexpr int S_ = 2048, D_ = 640, H_ = 32, F_ = 1536;
constexpr float LN_EPS = 1e-5f;
constexpr float QSCALE = 0.22360679774997896f * 1.4426950408889634f;
constexpr unsigned ONES2 = 0x3f803f80u;

constexpr size_t XT_BYTES = (size_t)S_ * D_ * 2;
constexpr size_t HT_BYTES = (size_t)S_ * F_ * 2;
constexpr int KV_REC = 82048, KV_V = 40960, KV_ONES = 81920;
constexpr size_t KV_BYTES = (size_t)64 * KV_REC;
constexpr size_t ST_BYTES = (size_t)8 * S_ * 8;
constexpr size_t FL_GROUP = 64 * 64;
constexpr size_t FL_A = 0, FL_W = FL_A + 32 * FL_GROUP, FL_G = FL_W + 16 * FL_GROUP, FL_D = FL_G + 32 * FL_GROUP,
                 FL_P = FL_D + 16 * FL_GROUP, FL_Q = FL_P + 8 * FL_GROUP, FL_U = FL_Q + 16 * FL_GROUP, FL_T = FL_U + FL_GROUP, FL_BYTES = FL_T + 4 * FL_GROUP;
constexpr size_t WS_FLAGS = 0;
constexpr size_t WS_TAG = WS_FLAGS + 2 * FL_BYTES;
constexpr size_t WS_CSG = WS_TAG + 4096;
constexpr size_t WS_WT = WS_CSG + 16384;
constexpr size_t WS_GT = WS_WT + (size_t)8 * 68 * 5120;
constexpr size_t WS_L0 = WS_GT + (size_t)16 * 20 * 12288;
constexpr size_t L_X0 = 0, L_SA = L_X0 + XT_BYTES, L_Q = L_SA + ST_BYTES, L_KV = L_Q + XT_BYTES, L_O = L_KV + KV_BYTES, L_X1 = L_O + XT_BYTES,
                 L_SB = L_X1 + XT_BYTES, L_H = L_SB + ST_BYTES, L_BYTES = L_H + HT_BYTES;

constexpr int X_SLOT = 8192, X_NS = 6;
constexpr int WR_OFF = X_NS * X_SLOT;
constexpr int W_SLOT = 12288, W_NS = 8, W_PD = 6;
constexpr int CS_OFF = WR_OFF + 98304, CQ_OFF = CS_OFF + 1024, S2_OFF = CQ_OFF + 512, DUM_OFF = S2_OFF + 2048, STP_OFF = DUM_OFF + 256;

constexpr int LDS_BYTES = STP_OFF;
#define STAMP(kid, idx) do {} while (0)
#define STAMPT(kid, idx, thr) do {} while (0)
#define WAIT_V(n) asm volatile("s_waitcnt vmcnt(%0)" ::"i"(n) : "memory")
#define BAR() asm volatile("s_barrier" ::: "memory")
#define BAR_L() asm volatile("s_waitcnt lgkmcnt(0)\n\ts_barrier" ::: "memory")
#define LGKM0() __builtin_amdgcn_s_waitcnt(0xC07F)
#define SB() __builtin_amdgcn_sched_barrier(0)

__device__ __forceinline__ int TID() { int t = threadIdx.x; asm volatile("" : "+v"(t)); return t; }
__device__ __forceinline__ int BID() { int t = blockIdx.x; asm volatile("" : "+s"(t)); return t; }

#define GL_ "global_load_lds_dwordx4 "
template <bool SC1> __device__ __forceinline__ void glds1(const void* sb, unsigned v, unsigned lds) {
    if constexpr (SC1) asm volatile("s_mov_b32 m0, %2\n\ts_nop 0\n\t" GL_ "%0, %1 sc1" :: "v"(v), "s"(sb), "s"(lds) : "memory");
    else asm volatile("s_mov_b32 m0, %2\n\ts_nop 0\n\t" GL_ "%0, %1" :: "v"(v), "s"(sb), "s"(lds) : "memory");
}
template <bool SC1> __device__ __forceinline__ void glds2(const void* sb, unsigned v0, unsigned v1, unsigned lds) {
    if constexpr (SC1) asm volatile("s_mov_b32 m0, %3\n\ts_nop 0\n\t" GL_ "%0, %2 sc1\n\t" GL_ "%1, %2 offset:1024 sc1" :: "v"(v0), "v"(v1), "s"(sb), "s"(lds) : "memory");
    else asm volatile("s_mov_b32 m0, %3\n\ts_nop 0\n\t" GL_ "%0, %2\n\t" GL_ "%1, %2 offset:1024" :: "v"(v0), "v"(v1), "s"(sb), "s"(lds) : "memory");
}
template <bool SC1 = false> __device__ __forceinline__ void glds3(const void* sb, unsigned v0, unsigned v1, unsigned v2, unsigned lds) {
    if constexpr (SC1) asm volatile("s_mov_b32 m0, %4\n\ts_nop 0\n\t" GL_ "%0, %3 sc1\n\t" GL_ "%1, %3 offset:1024 sc1\n\t" GL_ "%2, %3 offset:2048 sc1"
                 :: "v"(v0), "v"(v1), "v"(v2), "s"(sb), "s"(lds) : "memory");
    else asm volatile("s_mov_b32 m0, %4\n\ts_nop 0\n\t" GL_ "%0, %3\n\t" GL_ "%1, %3 offset:1024\n\t" GL_ "%2, %3 offset:2048"
                 :: "v"(v0), "v"(v1), "v"(v2), "s"(sb), "s"(lds) : "memory");
}
template <bool SC1> __device__ __forceinline__ void glds4(const void* sb, unsigned v0, unsigned v1, unsigned v2, unsigned v3, unsigned lds) {
    if constexpr (SC1) asm volatile("s_mov_b32 m0, %5\n\ts_nop 0\n\t" GL_ "%0, %4 sc1\n\t" GL_ "%1, %4 offset:1024 sc1\n\t" GL_ "%2, %4 offset:2048 sc1\n\t" GL_ "%3, %4 offset:3072 sc1"
                 :: "v"(v0), "v"(v1), "v"(v2), "v"(v3), "s"(sb), "s"(lds) : "memory");
    else asm volatile("s_mov_b32 m0, %5\n\ts_nop 0\n\t" GL_ "%0, %4\n\t" GL_ "%1, %4 offset:1024\n\t" GL_ "%2, %4 offset:2048\n\t" GL_ "%3, %4 offset:3072"
                 :: "v"(v0), "v"(v1), "v"(v2), "v"(v3), "s"(sb), "s"(lds) : "memory");
}
template <bool SC1> __device__ __forceinline__ u32x2 gload2(const void* sb, unsigned v) {
    u32x2 r;
    if constexpr (SC1) asm volatile("global_load_dwordx2 %0, %1, %2 sc1" : "=v"(r) : "v"(v), "s"(sb) : "memory");
    else asm volatile("global_load_dwordx2 %0, %1, %2" : "=v"(r) : "v"(v), "s"(sb) : "memory");
    return r;
}
template <bool SC1> __device__ __forceinline__ void gstore2(void* sb, unsigned v, u32x2 d) {
    if constexpr (SC1) asm volatile("global_store_dwordx2 %0, %1, %2 sc1" :: "v"(v), "v"(d), "s"(sb) : "memory");
    else asm volatile("global_store_dwordx2 %0, %1, %2" :: "v"(v), "v"(d), "s"(sb) : "memory");
}
template <bool SC1> __device__ __forceinline__ void gstore4(void* sb, unsigned v, u32x4 d) {
    if constexpr (SC1) asm volatile("global_store_dwordx4 %0, %1, %2 sc1\n\ts_nop 1" :: "v"(v), "v"(d), "s"(sb) : "memory");
    else asm volatile("global_store_dwordx4 %0, %1, %2\n\ts_nop 1" :: "v"(v), "v"(d), "s"(sb) : "memory");
}

typedef __bf16 bf2_t __attribute__((ext_vector_type(2)));
__device__ __forceinline__ unsigned cvtpk(float lo, float hi) {
    f32x2 v = {lo, hi};
    return __builtin_bit_cast(unsigned, __builtin_convertvector(v, bf2_t));
}
__device__ __forceinline__ float bflo(unsigned u) { return __uint_as_float(u << 16); }
__device__ __forceinline__ float bfhi(unsigned u) { return __uint_as_float(u & 0xffff0000u); }
__device__ __forceinline__ bf16x8 ldsr128(const char* p) { return *(const bf16x8*)p; }
__device__ __forceinline__ s16x4 ldsr64(const char* p) { return *(const s16x4*)p; }
__device__ __forceinline__ s16x4 ldstr(const char* p) {
    return __builtin_bit_cast(s16x4, __builtin_amdgcn_ds_read_tr16_b64_v4i16((__attribute__((address_space(3))) s16x4*)p));
}
__device__ __forceinline__ bf16x8 ldstr2(const char* p, int d) { return __builtin_shufflevector(ldstr(p), ldstr(p + d), 0, 1, 2, 3, 4, 5, 6, 7); }
#define MFMA32(a, b, c) __builtin_amdgcn_mfma_f32_32x32x16_bf16((a), (b), (c), 0, 0, 0)
#define MFMA16(a, b, c) __builtin_amdgcn_mfma_f32_16x16x32_bf16((a), (b), (c), 0, 0, 0)
#define MFMA4(a, b, c) __builtin_amdgcn_mfma_f32_4x4x4bf16_1k((a), (b), (c), 0, 0, 0)
#define MFMA2B(a, b, c) __builtin_amdgcn_mfma_f32_32x32x4bf16_1k((a), (b), (c), 0, 0, 0)
#define CAT16(a, b) __builtin_shufflevector(a, b, 0,1,2,3,4,5,6,7,8,9,10,11,12,13,14,15,16,17,18,19,20,21,22,23,24,25,26,27,28,29,30,31)

__device__ __forceinline__ float xsum32(float a) {
    auto rr = __builtin_amdgcn_permlane32_swap(__float_as_uint(a), __float_as_uint(a), false, false);
    return __uint_as_float(rr[0]) + __uint_as_float(rr[1]);
}
__device__ __forceinline__ float xmax32(float a) {
    auto rr = __builtin_amdgcn_permlane32_swap(__float_as_uint(a), __float_as_uint(a), false, false);
    return fmaxf(__uint_as_float(rr[0]), __uint_as_float(rr[1]));
}
__device__ __forceinline__ float xsum16_32(float x) {
    auto s = __builtin_amdgcn_permlane16_swap(__float_as_uint(x), __float_as_uint(x), false, false);
    return xsum32(__uint_as_float(s[0]) + __uint_as_float(s[1]));
}
__device__ __forceinline__ f32x2 ln_finish(float s1, float s2) {
    const float mean = s1 * (1.f / 640.f);
    const float var = fmaxf(s2 * (1.f / 640.f) - mean * mean, 0.f);
    const float rstd = __builtin_amdgcn_rsqf(var + LN_EPS);
    return f32x2{rstd, -mean * rstd};
}

__device__ __forceinline__ void signal_flag(char* flag, u64 tag) {
    asm volatile("s_waitcnt vmcnt(0)" : "+s"(tag), "+s"(flag) :: "memory");
    if ((TID() & 63) == 0) __hip_atomic_store((gu64*)flag, tag, __ATOMIC_RELAXED, __HIP_MEMORY_SCOPE_AGENT);
}
#define POLL_PRE 8
#define POLL_SLEEP 2
__device__ __forceinline__ void wait_flags_at(const char* base, unsigned off, bool use, u64 tag) {
    const gu64* f = (const gu64*)(base + off);
    bool ok = !use;
#pragma unroll 1
    for (unsigned spins = 0; spins < 100000u; ++spins) {
        if (!ok) ok = __hip_atomic_load(f, __ATOMIC_RELAXED, __HIP_MEMORY_SCOPE_AGENT) == tag;
        if (__all(ok)) break;
        __builtin_amdgcn_s_sleep(POLL_SLEEP);
    }
    asm volatile("" ::: "memory");
}
__device__ __forceinline__ void wait_flags(const char* group, int n, u64 tag) {
    const int lane = TID() & 63;
    __builtin_amdgcn_s_sleep(POLL_PRE);
    wait_flags_at(group, lane * 64, lane < n, tag);
}

template <int NST, int NS, int PD, int D, bool PRE = true, class Issue>
__device__ __forceinline__ void ring_loader(Issue issue) {
    static_assert(PD <= NS - 2 && (PD - 1) * D <= 63 && PD <= NST, "ring geometry");
    if (PRE) {
#pragma unroll 1
        for (int s = 0; s < PD; ++s) issue(s, s);
    }
    int slot = PD;
#pragma unroll 1
    for (int s = 0; s < NST - PD; ++s) {
        WAIT_V((PD - 1) * D);
        BAR();
        issue(s + PD, slot);
        slot = slot + 1 == NS ? 0 : slot + 1;
    }
#pragma unroll
    for (int r = PD - 1; r >= 0; --r) {
        WAIT_V(r * D);
        BAR();
    }
}
template <int N> __device__ __forceinline__ void idle_barriers() {
#pragma unroll 1
    for (int i = 0; i < N; ++i) BAR();
}
#define PDX 3
#define PDXP 4

constexpr int QKV_NST = 20;

__device__ __forceinline__ void ln_issue(u32x2 (&p)[8], const char* stats, int tok0) {
#pragma unroll
    for (int c = 0; c < 8; ++c) p[c] = gload2<true>(stats + ((size_t)c * S_ + tok0) * 8, (TID() & 63) * 8);
}
__device__ __forceinline__ f32x2 ln_collect(u32x2 (&p)[8]) {
    asm volatile("s_waitcnt vmcnt(0)" : "+v"(p[0]), "+v"(p[1]), "+v"(p[2]), "+v"(p[3]), "+v"(p[4]), "+v"(p[5]), "+v"(p[6]), "+v"(p[7]) :: "memory");
    f32x2 a = __builtin_bit_cast(f32x2, p[0]);
#pragma unroll
    for (int c = 1; c < 8; ++c) a += __builtin_bit_cast(f32x2, p[c]);
    return ln_finish(a[0], a[1]);
}
__device__ __forceinline__ void split_halves(f32x2 v, f32x2& lo, f32x2& hi) {
#pragma unroll
    for (int e = 0; e < 2; ++e) {
        auto rr = __builtin_amdgcn_permlane32_swap(__float_as_uint(v[e]), __float_as_uint(v[e]), false, false);
        lo[e] = __uint_as_float(rr[0]); hi[e] = __uint_as_float(rr[1]);
    }
}

template <bool FIRST, int WS0, bool ISV>
__device__ __forceinline__ void qkv_compute(char* smem, const char* stats, char* Qb, char* KVb, char* sig, u64 tag, int tm, int tn, int w, int L) {
    const int lane = TID() & 63;
    const int wm = w & 1, wn = w >> 1;
    const int r32 = lane & 31, hh = lane >> 5;
    int xo[2][2], woff[2];
#pragma unroll
    for (int ks = 0; ks < 2; ++ks)
#pragma unroll
        for (int j = 0; j < 2; ++j) {
            const int tt = wm * 2 + (j ^ wn);
            xo[ks][j] = FIRST ? (tt * 32 + r32) * 128 + (((ks * 2 + hh) ^ ((r32 >> 1) & 7)) * 16) : tt * 2048 + ks * 1024 + lane * 16;
        }
#pragma unroll
    for (int i = 0; i < 2; ++i) woff[i] = WR_OFF + hh * 2048 + (wn * 2 + (i ^ wm)) * 256 + ((lane >> 2) & 3) * 64 + ((lane >> 4) & 1) * 32 + (lane & 3) * 8;
    f32x16 acc[2][2];
#pragma unroll
    for (int i = 0; i < 2; ++i)
#pragma unroll
        for (int j = 0; j < 2; ++j)
#pragma unroll
            for (int r = 0; r < 16; ++r) acc[i][j][r] = 0.f;
    bf16x8 xf[2][2], wf[2][2];
    u32x2 part[8];
    f32x4 s1 = {0.f, 0.f, 0.f, 0.f}, s2 = s1, cs = s1;
    s16x4 ones = {0x3f80, 0x3f80, 0x3f80, 0x3f80};
    asm volatile("" : "+v"(acc[0][0]), "+v"(acc[0][1]), "+v"(acc[1][0]), "+v"(acc[1][1]), "+v"(xo[0][0]), "+v"(xo[0][1]), "+v"(woff[0]), "+v"(woff[1]));
    if (FIRST) asm volatile("" : "+v"(s1), "+v"(s2), "+v"(cs), "+v"(ones), "+v"(xo[1][0]), "+v"(xo[1][1]));
    BAR();
    STAMP(0, 1);
#define XADDR(ks, j, key) (FIRST ? smem + (xo[ks][j] ^ (key)) : smem + (key) + xo[0][j] + (ks) * 1024)
#pragma unroll
    for (int ks = 0; ks < 2; ++ks) {
#pragma unroll
        for (int j = 0; j < 2; ++j) xf[ks][j] = ldsr128(XADDR(ks, j, 0));
#pragma unroll
        for (int i = 0; i < 2; ++i) wf[ks][i] = ldstr2(smem + WS0 * W_SLOT + woff[i] + ks * 4096, 1024);
    }
    if (!FIRST) ln_issue(part, stats, tm * 128 + wm * 64);
    LGKM0();
    int xk = FIRST ? 64 : 8192, ws = ((WS0 + 1) & (W_NS - 1)) * W_SLOT;
    int xc = 0, wa0 = WS0 * W_SLOT + woff[0], wa1 = WS0 * W_SLOT + woff[1];
#define QM(ks, i, j) acc[i][j] = ISV ? MFMA32(xf[ks][j], wf[ks][i], acc[i][j]) : MFMA32(wf[ks][i], xf[ks][j], acc[i][j]); SB()
#define QW(ks, i) wf[ks][i] = ldstr2(smem + (i ? wa1 : wa0) + ks * 4096, 1024)
#define QX(ks, j) xf[ks][j] = ldsr128(XADDR(ks, j, xc))
#define LO4(v) __builtin_shufflevector(v, v, 0, 1, 2, 3)
#define HI4(v) __builtin_shufflevector(v, v, 4, 5, 6, 7)
#define QS0(ks) if (FIRST) { s1 = MFMA4(ones, LO4(xf[ks][0]), s1); s2 = MFMA4(LO4(xf[ks][0]), LO4(xf[ks][0]), s2); SB(); }
#define QS1(ks) if (FIRST) { cs = MFMA4(ones, LO4(wf[ks][0]), cs); s1 = MFMA4(ones, HI4(xf[ks][0]), s1); SB(); }
#define QS2(ks) if (FIRST) { s2 = MFMA4(HI4(xf[ks][0]), HI4(xf[ks][0]), s2); cs = MFMA4(ones, HI4(wf[ks][0]), cs); SB(); }
#pragma unroll 1
    for (int s = 0; s < QKV_NST; ++s) {
        QM(0, 1, 1); QW(1, 0); QX(1, 0); SB();
        xc = xk; wa0 = ws + woff[0]; wa1 = ws + woff[1];
        asm volatile("" : "+v"(wa0), "+v"(wa1));
        SB();
        QS0(0)
        QM(0, 1, 0); BAR(); SB();
        QS1(0)
        QM(0, 0, 1); QW(0, 1); SB();
        QS2(0)
        QM(0, 0, 0); QX(0, 1); SB();
        QM(1, 1, 1); QW(0, 0); QX(0, 0); SB();
        QS0(1)
        QM(1, 1, 0);
        if (FIRST) xk = (xk & 64) ? (xk == 2 * 16384 + 64 ? 0 : xk + 16384 - 64) : xk + 64;
        else xk = xk + 8192 == 6 * 8192 ? 0 : xk + 8192;
        ws = ws + W_SLOT == W_NS * W_SLOT ? 0 : ws + W_SLOT;
        asm volatile("" : "+s"(xk), "+s"(ws));
        SB();
        QS1(1)
        QM(1, 0, 1); QW(1, 1); SB();
        QS2(1)
        QM(1, 0, 0); QX(1, 1); SB();
    }
#undef QW
#undef QX
#undef QS0
#undef QS1
#undef QS2
#undef QM
#undef XADDR
    STAMP(0, 2);
    f32x2 stl;
    if (FIRST) {
        const float q2 = (lane & 2) ? ((lane & 1) ? s2[3] : s2[2]) : ((lane & 1) ? s2[1] : s2[0]);
        const f32x2 st = ln_finish(xsum32(s1[0]), xsum32(q2));
        const float c = xsum32(cs[0]);
        if (lane < 32) {
            *(f32x2*)(smem + CS_OFF + ((wm * 2 + wn) * 32 + lane) * 8) = st;
            *(float*)(smem + CQ_OFF + ((wn * 2 + wm) * 32 + lane) * 4) = c;
        }
        BAR_L();
        stl = *(const f32x2*)(smem + CS_OFF + (wm * 64 + lane) * 8);
    } else {
        stl = ln_collect(part);
    }
    const float* csl = (const float*)(smem + CQ_OFF);
    const int tile0 = tm * 4 + wm * 2;
    if (!ISV) {
        const bool isq = tn < 5;
        f32x2 lo, hi, st[2];
        split_halves(stl * (isq ? QSCALE : 1.f), lo, hi);
        st[0] = wn ? hi : lo; st[1] = wn ? lo : hi;
        const int tstride = isq ? 40960 : KV_REC;
        char* ob = (isq ? Qb : KVb) + (size_t)tile0 * tstride + ((isq ? tn : tn - 5) * 16 + wn * 8) * 512;
#pragma unroll
        for (int q = 0; q < 4; ++q) {
            const int qq = q ^ (wm * 2);
            f32x4 c4[2];
#pragma unroll
            for (int e = 0; e < 2; ++e) c4[e] = *(const f32x4*)(csl + wn * 64 + qq * 16 + e * 8 + hh * 4);
#pragma unroll
            for (int j = 0; j < 2; ++j) {
                const f32x16& a = acc[q >> 1][j];
                unsigned pk[2][2];
#pragma unroll
                for (int e = 0; e < 2; ++e) {
                    const int r = (q & 1) * 8 + e * 4;
                    const f32x2 v0 = f32x2{a[r], a[r + 1]} * st[j][0] + f32x2{c4[e][0], c4[e][1]} * st[j][1];
                    const f32x2 v1 = f32x2{a[r + 2], a[r + 3]} * st[j][0] + f32x2{c4[e][2], c4[e][3]} * st[j][1];
                    pk[e][0] = cvtpk(v0[0], v0[1]); pk[e][1] = cvtpk(v1[0], v1[1]);
                }
                auto r0 = __builtin_amdgcn_permlane32_swap(pk[0][0], pk[1][0], false, false);
                auto r1 = __builtin_amdgcn_permlane32_swap(pk[0][1], pk[1][1], false, false);
                gstore4<true>(ob + (j ^ wn) * tstride + qq * 1024, lane * 16, u32x4{r0[0], r1[0], r0[1], r1[1]});
            }
        }
    } else {
        const char* sl = smem + S2_OFF + w * 512;
        *(f32x2*)(smem + S2_OFF + w * 512 + lane * 8) = stl;
        char* ob = KVb + (size_t)tile0 * KV_REC + KV_V + ((tn - 10) * 128 + wn * 64) * 16;
        const unsigned ov = hh * 10240 + r32 * 16;
        float c1[2];
#pragma unroll
        for (int i = 0; i < 2; ++i) c1[i] = csl[(wn * 2 + (i ^ wm)) * 32 + r32];
#pragma unroll
        for (int j = 0; j < 2; ++j)
#pragma unroll
            for (int s2_ = 0; s2_ < 2; ++s2_) {
                f32x4 sa[2], sb[2];
#pragma unroll
                for (int q = 0; q < 2; ++q) {
                    const char* sp = sl + ((j ^ wn) * 32 + (s2_ * 2 + q) * 8 + hh * 4) * 8;
                    sa[q] = *(const f32x4*)sp; sb[q] = *(const f32x4*)(sp + 16);
                }
#pragma unroll
                for (int i = 0; i < 2; ++i) {
                    float v[8];
#pragma unroll
                    for (int q = 0; q < 2; ++q) {
                        const int r = (s2_ * 2 + q) * 4;
                        v[q * 4 + 0] = acc[i][j][r + 0] * sa[q][0] + c1[i] * sa[q][1];
                        v[q * 4 + 1] = acc[i][j][r + 1] * sa[q][2] + c1[i] * sa[q][3];
                        v[q * 4 + 2] = acc[i][j][r + 2] * sb[q][0] + c1[i] * sb[q][1];
                        v[q * 4 + 3] = acc[i][j][r + 3] * sb[q][2] + c1[i] * sb[q][3];
                    }
                    gstore4<true>(ob + (j ^ wn) * KV_REC + s2_ * 20480 + (i ^ wm) * 512, ov,
                                  u32x4{cvtpk(v[0], v[1]), cvtpk(v[2], v[3]), cvtpk(v[4], v[5]), cvtpk(v[6], v[7])});
                }
            }
        if (lane < 16) {
            unsigned one = ONES2;
            asm volatile("" : "+v"(one));
            gstore4<true>(KVb + (size_t)tile0 * KV_REC + KV_ONES, (lane >> 3) * KV_REC + (lane & 7) * 16, u32x4{one, one, one, one});
        }
    }
    STAMP(0, 4);
    signal_flag(sig + (size_t)(((tm >> 1) * 32 + tn * 2 + wn) * 4 + (tm & 1) * 2 + wm) * 64, tag);
    STAMP(0, 3);
    BAR_L();
}

__device__ __forceinline__ bool qkv_tile(int& tm, int& tn) {
    const int b = BID(), xcd = b & 7, j = b >> 3;
    const int id = (xcd & 3) * 30 + j;
    tm = (xcd >> 2) * 8 + (id & 7); tn = id >> 3;
    return j < 30;
}
struct QkvW {
    const char* wsb; unsigned wv;
    __device__ __forceinline__ QkvW(const char* W, int tn) {
        const int lane = TID() & 63;
        wsb = W + tn * 256;
        wv = ((lane >> 2) & 3) * 3840 + (lane >> 4) * 64 + (lane & 3) * 16;
    }
    __device__ __forceinline__ void operator()(int s, unsigned dst) const {
        constexpr unsigned dv = 4 * 3840 - 1024;
        glds4<false>(wsb + (size_t)s * (32 * 3840), wv, wv + dv, wv + 2 * dv, wv + 3 * dv, dst);
        glds4<false>(wsb + (size_t)s * (32 * 3840) + 16 * 3840, wv, wv + dv, wv + 2 * dv, wv + 3 * dv, dst + 4096);
    }
};

struct Params {
    const char *x0, *wqkv, *wo, *wg, *wu, *wd;
    char *ws, *out;
};
__device__ __forceinline__ void acc8(float (&a)[8], const u32x4& v) {
#pragma unroll
    for (int j = 0; j < 4; ++j) { a[2 * j] += bflo(v[j]); a[2 * j + 1] += bfhi(v[j]); }
}
__device__ __forceinline__ void pre_csg(char* smem, const Params& p, u64 tag) {
    const int t = TID(), b = BID();
    const int sidx = (b & 7) * 2 + ((b >> 3) - 30);
    const int lane = t & 63, w = __builtin_amdgcn_readfirstlane(t >> 6), cw = w % 3, half = w / 3;
    const char* src = (sidx < 8 ? p.wg : p.wu) + (sidx & 7) * 384 + cw * 128 + (lane & 7) * 16 + (size_t)(half * 320 + (lane >> 3)) * 3072;
    float a[8] = {0, 0, 0, 0, 0, 0, 0, 0};
#pragma unroll 1
    for (int o = 0; o < 2; ++o) {
        u32x4 v[20];
#pragma unroll
        for (int i = 0; i < 20; ++i) v[i] = *(const u32x4*)(src + (size_t)(o * 20 + i) * 8 * 3072);
#pragma unroll
        for (int i = 0; i < 20; ++i) acc8(a, v[i]);
    }
#pragma unroll
    for (int j = 0; j < 8; ++j) {
        a[j] += __int_as_float(__builtin_amdgcn_ds_swizzle(__float_as_int(a[j]), 0x201f));
        a[j] = xsum16_32(a[j]);
    }
    volatile float* xch = (volatile float*)(smem + S2_OFF) + cw * 64;
    volatile u64* lf = (volatile u64*)(smem + S2_OFF + 1024) + cw;
    if (half) {
        if (lane < 8) {
#pragma unroll
            for (int j = 0; j < 8; ++j) xch[lane * 8 + j] = a[j];
        }
        asm volatile("s_waitcnt lgkmcnt(0)" ::: "memory");
        if (lane == 0) *lf = tag;
        return;
    }
#pragma unroll 1
    for (unsigned spins = 0; spins < 1000000u && *lf != tag; ++spins) __builtin_amdgcn_s_sleep(2);
    if (lane < 8) {
#pragma unroll
        for (int j = 0; j < 8; ++j) a[j] += xch[lane * 8 + j];
        const int c = (sidx & 7) * 192 + cw * 64 + lane * 8;
        const unsigned o = ((c >> 4) * 32 + (sidx < 8 ? 0 : 16) + (c & 15)) * 4;
        gstore4<true>(p.ws + WS_CSG, o, u32x4{__float_as_uint(a[0]), __float_as_uint(a[1]), __float_as_uint(a[2]), __float_as_uint(a[3])});
        gstore4<true>(p.ws + WS_CSG + 16, o, u32x4{__float_as_uint(a[4]), __float_as_uint(a[5]), __float_as_uint(a[6]), __float_as_uint(a[7])});
    }
    signal_flag(p.ws + WS_FLAGS + FL_U + (size_t)(sidx * 3 + cw) * 8, tag);
}

struct XRow {
    const char* xs; unsigned v0, v1, dst;
    __device__ __forceinline__ XRow(const char* x0, int tm, int lw, unsigned lbase) {
        const int lane = TID() & 63, r8 = lane >> 3;
        xs = x0 + (size_t)(tm * 128 + lw * 64) * 1280;
        v0 = r8 * 1280 + (((lane & 7) ^ (r8 >> 1)) * 16);
        v1 = (r8 + 8) * 1280 - 1024 + (((lane & 7) ^ (4 + (r8 >> 1))) * 16);
        dst = lbase + lw * 8192;
    }
    __device__ __forceinline__ void operator()(int d, int slot) const {
        constexpr unsigned d2 = 16 * 1280 - 2048;
        glds4<false>(xs + d * 128, v0, v1, v0 + d2, v1 + d2, dst + slot * 16384);
        glds4<false>(xs + d * 128 + 32 * 1280, v0, v1, v0 + d2, v1 + d2, dst + slot * 16384 + 4096);
    }
};
__device__ __forceinline__ void startup(char* smem, const Params& p) {
    int tm, tn;
    const bool has = qkv_tile(tm, tn);
    const int w = __builtin_amdgcn_readfirstlane(TID() >> 6);
    const unsigned lbase = (unsigned)(uintptr_t)(lds_t)smem;
    if (w == 8) {
        if (has) {
            const QkvW qk(p.wqkv, tn);
#pragma unroll 1
            for (int s = 0; s < 3; ++s) qk(s, lbase + WR_OFF + s * W_SLOT);
        }
    } else if (w == 7) {
        const char* pc;
        int len;
        asm volatile("s_getpc_b64 %0\n.Lk_code_lo:\n\ts_mov_b32 %1, .Lk_code_hi-.Lk_code_lo" : "=s"(pc), "=s"(len));
        int o = ((BID() >> 3) * 16 + (TID() & 15)) * 128;
        o = o < len ? o : 0;
        asm volatile("s_mov_b32 m0, %2\n\ts_nop 0\n\tglobal_load_lds_dword %0, %1" :: "v"(o), "s"(pc), "s"(lbase + DUM_OFF) : "memory");
    } else if ((w == 4 || w == 5) && has) {
        const XRow xr(p.x0, tm, w - 4, lbase);
        xr(0, 0); xr(1, 1);
    }
}

template <int L, int WS0>
__device__ __forceinline__ void qkv_phase(char* smem, const Params& p, const char* X, const char* stats, char* Qb, char* KVb, const char* dep,
                                          char* sig, u64 tag) {
    int tm, tn;
    const bool has = qkv_tile(tm, tn);
    const int lane = TID() & 63;
    const int w = __builtin_amdgcn_readfirstlane(TID() >> 6);
    const unsigned lbase = (unsigned)(uintptr_t)(lds_t)smem;
    STAMP(0, 0);
    if (!has) {
        if (L == 0 && w < 6) pre_csg(smem, p, tag);
        return;
    }
    if (w >= 6) {
        idle_barriers<QKV_NST + 2>();
        if (L == 0) BAR();
    } else if (w < 4) {
        if (tn >= 10) qkv_compute<L == 0, WS0, true>(smem, stats, Qb, KVb, sig, tag, tm, tn, w, L);
        else qkv_compute<L == 0, WS0, false>(smem, stats, Qb, KVb, sig, tag, tm, tn, w, L);
    } else if (L) {
        const int lw = w - 4;
        const char* xs = X + (size_t)(tm * 4 + lw * 2) * 40 * 1024;
        const unsigned xv = lane * 16, xv2 = xv + 40 * 1024 - 2048;
        wait_flags(dep + (size_t)tm * FL_GROUP + lw * 2048, 32, tag);
        STAMPT(0, 5, 256);
        ring_loader<QKV_NST, X_NS, PDX, 4>([&](int s, int slot) { glds4<true>(xs + s * 2048, xv, xv, xv2, xv2, lbase + slot * X_SLOT + lw * 4096); });
        BAR(); BAR();
    } else {
        const XRow issue(p.x0, tm, w - 4, lbase);
        int slot = 2;
#pragma unroll 1
        for (int d = 0; d < 8; ++d) {
            WAIT_V(8);
            BAR();
            BAR();
            issue(d + 2, slot);
            slot = slot == 2 ? 0 : slot + 1;
        }
        WAIT_V(8);
        BAR(); BAR();
        WAIT_V(0);
        BAR(); BAR();
        BAR(); BAR(); BAR();
    }
}

constexpr int AT_SLOT = 8 * 3072, AT_NS = 3, AT_NCH = 8;

__device__ __forceinline__ float rowmax32(const f32x32& s) {
    float a = fmaxf(fmaxf(s[0], s[1]), s[2]), b = fmaxf(fmaxf(s[3], s[4]), s[5]);
#pragma unroll
    for (int r = 6; r < 30; r += 4) { a = fmaxf(fmaxf(a, s[r]), s[r + 1]); b = fmaxf(fmaxf(b, s[r + 2]), s[r + 3]); }
    a = fmaxf(fmaxf(a, s[30]), s[31]);
    return xmax32(fmaxf(a, b));
}

__device__ __forceinline__ void attn_tile(int& head, int& qblk) {
    const int b = BID(), xcd = b & 7, j = b >> 3;
    head = xcd * 4 + (j & 3); qblk = j >> 2;
}
__device__ __forceinline__ void kv_piece_offs(int head, unsigned (&v)[3]) {
    const int lane = TID() & 63;
    const int c0 = (head * 5) >> 1, odd = head & 1;
    v[0] = (c0 + odd) * 512 + lane * 16;
#pragma unroll
    for (int i = 1; i < 3; ++i) {
        const int idx = i * 64 + lane - 96, sh = idx / 24, row = idx - sh * 24;
        unsigned o = row < 20 ? KV_V + sh * 10240 + (head * 20 + row) * 16 : KV_ONES + (row - 20) * 16;
        if (idx < 0) o = (odd ? c0 : c0 + 2) * 512 + lane * 16;
        v[i] = o - i * 1024;
    }
}

__device__ __forceinline__ void attn_phase(char* smem, const char* Qb, const char* KVb, char* O, char* sig, u64 tag, int L) {
    const int tid = TID(), lane = tid & 63;
    const int w = __builtin_amdgcn_readfirstlane(tid >> 6);
    int head, qblk;
    attn_tile(head, qblk);
    const int r32 = lane & 31, hh = lane >> 5;
    const int qtile = qblk * 8 + w;
    const int c0 = (head * 5) >> 1, odd = head & 1;
    STAMP(1, 0);
    const unsigned lbase = (unsigned)(uintptr_t)(lds_t)smem;
    unsigned pv[3];
    kv_piece_offs(head, pv);
    const unsigned qhv = (odd ? c0 : c0 + 2) * 512 + r32 * 16 + odd * 8;
    const int koff = lane * 16, khoff = hh * 3072 + 1024 + r32 * 16 + odd * 8;
    const int vrow = r32 <= 20 ? r32 : (r32 < 28 ? 20 : 16);
    const int voff = 1536 + hh * 384 + vrow * 16;
    asm volatile("" : "+v"(pv[0]), "+v"(pv[1]), "+v"(pv[2]));
    BAR();
    STAMP(1, 5);
    glds3<true>(KVb + (size_t)w * KV_REC, pv[0], pv[1], pv[2], lbase + w * 3072);
    bf16x8 q0;
    s16x4 q4;
    {
        const char* qp = Qb + (size_t)qtile * 40960;
        asm volatile("global_load_dwordx4 %0, %1, %2 sc1" : "=v"(q0) : "v"(pv[0]), "s"(qp) : "memory");
        asm volatile("global_load_dwordx2 %0, %1, %2 sc1" : "=v"(q4) : "v"(qhv), "s"(qp) : "memory");
    }
    f32x16 zero;
#pragma unroll
    for (int r = 0; r < 16; ++r) zero[r] = 0.f;
    f32x16 o = zero, m16;
    f32x32 SA, SBn;
    asm volatile("s_waitcnt vmcnt(0)" : "+v"(q0), "+v"(q4) :: "memory");
    BAR();
    STAMP(1, 1);
    bf16x8 ka, kb, va0, va1, vb0, vb1;
    s16x4 kh;
    u32x4 pa0, pa1, pb0, pb1 = {0u, 0u, 0u, 0u};
    {
        ka = ldsr128(smem + koff); kb = ldsr128(smem + 3072 + koff); kh = ldsr64(smem + khoff);
        f32x16 n0 = MFMA32(ka, q0, zero), n1 = MFMA32(kb, q0, zero);
        SA = MFMA2B(kh, q4, CAT16(n0, n1));
        const float m = rowmax32(SA);
#pragma unroll
        for (int r = 0; r < 16; ++r) m16[r] = -m;
#pragma unroll
        for (int r = 0; r < 32; ++r) SA[r] -= m;
        ka = ldsr128(smem + 2 * 3072 + koff); kb = ldsr128(smem + 3 * 3072 + koff); kh = ldsr64(smem + 2 * 3072 + khoff);
        va0 = ldsr128(smem + voff); va1 = ldsr128(smem + voff + 768); vb0 = ldsr128(smem + 3072 + voff); vb1 = vb0;
    }
    asm volatile("" : "+v"(m16));
    LGKM0();
    SB();
#define EX(x) x = __builtin_amdgcn_exp2f(x)
#define BF8(x) __builtin_bit_cast(bf16x8, x)
#define G4(S, i, P, jj) EX(S[i]); EX(S[i + 1]); SB(); EX(S[i + 2]); P[jj] = cvtpk(S[i], S[i + 1]); SB(); EX(S[i + 3]); SB();
#define STEP2(S, SN, V1, V2, V3, K4, K5, KH4) do { \
        f32x16 n0, n1; \
        o = MFMA32(vb1, BF8(pb1), o); SB(); vb1 = ldsr128((V1) + 768); SB(); \
        G4(S, 0, pa0, 0) \
        n0 = MFMA32(ka, q0, m16); SB(); ka = ldsr128(K4); pa0[1] = cvtpk(S[2], S[3]); SB(); \
        G4(S, 4, pa0, 2) \
        n1 = MFMA32(kb, q0, m16); SB(); kb = ldsr128(K5); pa0[3] = cvtpk(S[6], S[7]); SB(); \
        G4(S, 8, pa1, 0) \
        o = MFMA32(va0, BF8(pa0), o); SB(); va0 = ldsr128(V2); pa1[1] = cvtpk(S[10], S[11]); SB(); \
        G4(S, 12, pa1, 2) \
        SN = MFMA2B(kh, q4, CAT16(n0, n1)); SB(); kh = ldsr64(KH4); pa1[3] = cvtpk(S[14], S[15]); SB(); \
        G4(S, 16, pb0, 0) \
        pb0[1] = cvtpk(S[18], S[19]); SB(); \
        G4(S, 20, pb0, 2) \
        o = MFMA32(va1, BF8(pa1), o); SB(); va1 = ldsr128((V2) + 768); pb0[3] = cvtpk(S[22], S[23]); SB(); \
        G4(S, 24, pb1, 0) \
        pb1[1] = cvtpk(S[26], S[27]); SB(); \
        o = MFMA32(vb0, BF8(pb0), o); SB(); vb0 = ldsr128(V3); SB(); \
        G4(S, 28, pb1, 2) \
        pb1[3] = cvtpk(S[30], S[31]); SB(); \
    } while (0)
    int slot = 0;
#pragma unroll 1
    for (int c = 0; c < AT_NCH; ++c) {
        const int nslot = slot + 1 == AT_NS ? 0 : slot + 1;
        const char* cv = smem + slot * AT_SLOT + voff;   const char* ck = smem + slot * AT_SLOT + koff;   const char* ch = smem + slot * AT_SLOT + khoff;
        const char* nv = smem + nslot * AT_SLOT + voff;  const char* nk = smem + nslot * AT_SLOT + koff;  const char* nh = smem + nslot * AT_SLOT + khoff;
        STEP2(SA, SBn, cv + 1 * 3072, cv + 2 * 3072, cv + 3 * 3072, ck + 4 * 3072, ck + 5 * 3072, ch + 4 * 3072);
        STEP2(SBn, SA, cv + 3 * 3072, cv + 4 * 3072, cv + 5 * 3072, ck + 6 * 3072, ck + 7 * 3072, ch + 6 * 3072);
        BAR();
        SB();
        STEP2(SA, SBn, cv + 5 * 3072, cv + 6 * 3072, cv + 7 * 3072, nk, nk + 3072, nh);
        STEP2(SBn, SA, cv + 7 * 3072, nv, nv + 3072, nk + 2 * 3072, nk + 3 * 3072, nh + 2 * 3072);
        slot = nslot;
    }
    o = MFMA32(vb1, BF8(pb1), o);
    STAMP(1, 2);
    float l;
    {
        auto rr = __builtin_amdgcn_permlane32_swap(__float_as_uint(o[8]), __float_as_uint(o[8]), false, false);
        l = __uint_as_float(rr[1]);
    }
    float big = fmaxf(fmaxf(fabsf(o[0]), fabsf(o[1])), fabsf(o[2]));
#pragma unroll
    for (int r = 3; r < 9; r += 2) big = fmaxf(fmaxf(big, fabsf(o[r])), fabsf(o[r + 1]));
    float sum = ((o[0] + o[1]) + (o[2] + o[3])) + ((o[4] + o[5]) + (o[6] + o[7])) + o[8];
    if (!hh) { big = fmaxf(fmaxf(big, fabsf(o[9])), fmaxf(fabsf(o[10]), fabsf(o[11]))); sum += (o[9] + o[10]) + o[11]; }
    const bool bad = !(l < 1.8446744073709552e19f) || !(big < 1.0e37f) || !(sum == sum);
    if (__builtin_expect(__any(bad), 0)) {
        float m = -INFINITY;
        o = zero;
        const unsigned gk = (c0 + odd) * 512 + lane * 16, gh = hh * KV_REC + qhv;
        const unsigned gv0 = r32 < 20 ? KV_V + hh * 10240 + (head * 20 + r32) * 16 : KV_ONES, gv1 = r32 < 20 ? gv0 + 20480 : KV_ONES;
#pragma unroll 1
        for (int t = 0; t < 64; t += 2) {
            const char* tp = KVb + (size_t)t * KV_REC;
            bf16x8 fk[2], fv[4];
            s16x4 fh;
            asm volatile("global_load_dwordx4 %0, %7, %11 sc1\n\tglobal_load_dwordx4 %1, %7, %12 sc1\n\tglobal_load_dwordx2 %2, %8, %11 sc1\n\t"
                         "global_load_dwordx4 %3, %9, %11 sc1\n\tglobal_load_dwordx4 %4, %10, %11 sc1\n\t"
                         "global_load_dwordx4 %5, %9, %12 sc1\n\tglobal_load_dwordx4 %6, %10, %12 sc1\n\ts_waitcnt vmcnt(0)"
                         : "=&v"(fk[0]), "=&v"(fk[1]), "=&v"(fh), "=&v"(fv[0]), "=&v"(fv[1]), "=&v"(fv[2]), "=&v"(fv[3])
                         : "v"(gk), "v"(gh), "v"(gv0), "v"(gv1), "s"(tp), "s"(tp + KV_REC) : "memory");
            f32x16 n0 = MFMA32(fk[0], q0, zero), n1 = MFMA32(fk[1], q0, zero);
            f32x32 sc = MFMA2B(fh, q4, CAT16(n0, n1));
            const float mn = fmaxf(m, rowmax32(sc));
            const float alpha = __builtin_amdgcn_exp2f(m - mn);
            m = mn;
#pragma unroll
            for (int r = 0; r < 16; ++r) o[r] *= alpha;
#pragma unroll
            for (int q = 0; q < 4; ++q) {
                u32x4 pp;
#pragma unroll
                for (int e = 0; e < 4; ++e) pp[e] = cvtpk(__builtin_amdgcn_exp2f(sc[q * 8 + 2 * e] - mn), __builtin_amdgcn_exp2f(sc[q * 8 + 2 * e + 1] - mn));
                o = MFMA32(fv[q], BF8(pp), o);
            }
        }
        auto rr = __builtin_amdgcn_permlane32_swap(__float_as_uint(o[8]), __float_as_uint(o[8]), false, false);
        l = __uint_as_float(rr[1]);
    }
    const float inv = __builtin_amdgcn_rcpf(l);
    char* ob = O + (size_t)qtile * 40 * 1024;
#pragma unroll
    for (int g = 0; g < 3; ++g) {
        if (g < 2 || !hh) {
            const int col = head * 20 + g * 8 + hh * 4;
            gstore2<true>(ob, (col >> 3) * 512 + (col & 4) * 2 + r32 * 16,
                           u32x2{cvtpk(o[g * 4] * inv, o[g * 4 + 1] * inv), cvtpk(o[g * 4 + 2] * inv, o[g * 4 + 3] * inv)});
        }
    }
    STAMP(1, 4);
    signal_flag(sig + (size_t)(qtile >> 1) * FL_GROUP + ((qtile & 1) * 32 + head) * 64, tag);
    STAMP(1, 3);
    BAR_L();
}
#undef EX
#undef G4
#undef STEP2

constexpr int GU_NST = 20;
__device__ __forceinline__ void gu_tile(int& tm, int& tn) {
    const int b = BID(), xcd = b & 7, j0 = b >> 3;
    tm = (xcd >> 2) * 8 + (j0 & 7); tn = (xcd & 3) * 4 + (j0 >> 3);
}
struct GuW {
    const char* wb; unsigned lv;
    __device__ __forceinline__ GuW(const char* GT) { wb = GT; lv = (TID() & 63) * 16; }
    __device__ __forceinline__ void operator()(int s, unsigned d) const {
#pragma unroll
        for (int i = 0; i < 3; ++i) glds4<true>(wb + s * 12288 + i * 4096, lv, lv, lv, lv, d + i * 4096);
    }
};
struct GuNat {
    const char *gb, *ub; unsigned gv[3];
    __device__ __forceinline__ GuNat(const char* Wg, const char* Wu, int tn) {
        const int lane = TID() & 63;
#pragma unroll
        for (int i = 0; i < 3; ++i) {
            const int idx = i * 64 + lane, kq = idx / 48, rem = idx - kq * 48, sub = rem >> 4, kr = (rem >> 2) & 3, c = rem & 3;
            gv[i] = (kq * 4 + kr) * 3072 + sub * 64 + c * 16 - i * 1024;
        }
        gb = Wg + tn * 192; ub = Wu + tn * 192;
    }
    __device__ __forceinline__ void operator()(int s, unsigned d) const {
        const size_t o = (size_t)s * (32 * 3072);
        glds3(gb + o, gv[0], gv[1], gv[2], d);
        glds3(ub + o, gv[0], gv[1], gv[2], d + 6144);
        glds3(gb + o + 16 * 3072, gv[0], gv[1], gv[2], d + 3072);
        glds3(ub + o + 16 * 3072, gv[0], gv[1], gv[2], d + 9216);
    }
};

template <int WS0>
__device__ __forceinline__ void gu_phase(char* smem, const char* X, const char* stats, char* Hh, const char* dep, char* sig, u64 tag,
                                         int L) {
    const int lane = TID() & 63;
    const int w = __builtin_amdgcn_readfirstlane(TID() >> 6);
    int tm, tn;
    gu_tile(tm, tn);
    const unsigned lbase = (unsigned)(uintptr_t)(lds_t)smem;
    STAMP(3, 0);
    if (w >= 6) { idle_barriers<GU_NST + 2>(); return; }
    if (w >= 4) {
        const int lw = w - 4;
        const char* xs = X + (size_t)(tm * 4 + lw * 2) * 40 * 1024;
        const unsigned xv = lane * 16, xv2 = xv + 40 * 1024 - 2048;
        wait_flags(dep + (size_t)tm * FL_GROUP + lw * 2048, 32, tag);
        STAMPT(3, 5, 256);
        ring_loader<GU_NST, X_NS, PDX, 4>([&](int s, int slot) { glds4<true>(xs + s * 2048, xv, xv, xv2, xv2, lbase + slot * X_SLOT + lw * 4096); });
        BAR(); BAR();
        return;
    }
    const int wm = w & 1, wn = w >> 1;
    const int hh = lane >> 5;
    const int xoff = wm * 4096 + lane * 16;
    int woff = WR_OFF + wn * 3072 + lane * 16;
    f32x16 acc[3][2];
#pragma unroll
    for (int i = 0; i < 3; ++i)
#pragma unroll
        for (int j = 0; j < 2; ++j)
#pragma unroll
            for (int r = 0; r < 16; ++r) acc[i][j][r] = 0.f;
    bf16x8 xf[2][2], wf[2][3];
    u32x2 part[8];
    asm volatile("" : "+v"(acc[0][0]), "+v"(acc[0][1]), "+v"(acc[1][0]), "+v"(acc[1][1]), "+v"(acc[2][0]), "+v"(acc[2][1]), "+v"(woff));
    BAR();
    STAMP(3, 1);
#pragma unroll
    for (int ks = 0; ks < 2; ++ks) {
#pragma unroll
        for (int j = 0; j < 2; ++j) xf[ks][j] = ldsr128(smem + xoff + ks * 1024 + j * 2048);
#pragma unroll
        for (int i = 0; i < 3; ++i) wf[ks][i] = ldsr128(smem + WS0 * W_SLOT + woff + ks * 6144 + i * 1024);
    }
    ln_issue(part, stats, tm * 128 + wm * 64);
    LGKM0();
    int xs = X_SLOT, ws = ((WS0 + 1) & (W_NS - 1)) * W_SLOT;
    int xa = xoff, wa = WS0 * W_SLOT + woff;
#define GM(ks, i, j) acc[i][j] = MFMA32(wf[ks][i], xf[ks][j], acc[i][j]); SB()
#define GW(ks, i) wf[ks][i] = ldsr128(smem + wa + ks * 6144 + i * 1024)
#define GX(ks, j) xf[ks][j] = ldsr128(smem + xa + ks * 1024 + j * 2048)
#pragma unroll 1
    for (int s = 0; s < GU_NST; ++s) {
        GM(0, 0, 0); GW(1, 2); GX(1, 1); SB();
        xa = xs + xoff;
        wa = ws + woff;
        asm volatile("" : "+v"(xa), "+v"(wa));
        SB();
        GM(0, 0, 1); BAR(); SB();
        GM(0, 1, 0); GW(0, 0); SB();
        GM(0, 1, 1);
        xs = xs + X_SLOT == X_NS * X_SLOT ? 0 : xs + X_SLOT;
        ws = ws + W_SLOT == W_NS * W_SLOT ? 0 : ws + W_SLOT;
        asm volatile("" : "+s"(xs), "+s"(ws));
        SB();
        GM(0, 2, 0); GW(0, 1); SB();
        GM(0, 2, 1); GX(0, 0); SB();
        GM(1, 0, 0); GW(0, 2); GX(0, 1); SB();
        GM(1, 0, 1);
        GM(1, 1, 0); GW(1, 0); SB();
        GM(1, 1, 1);
        GM(1, 2, 0); GW(1, 1); SB();
        GM(1, 2, 1); GX(1, 0); SB();
    }
#undef GW
#undef GX
#undef GM
    STAMP(3, 2);
    const float* csl = (const float*)(smem + CS_OFF);
    f32x2 st[2];
    split_halves(ln_collect(part), st[0], st[1]);
    char* hb = Hh + (size_t)((tm * 4 + wm * 2) * 96 + tn * 6 + wn * 3) * 1024;
#pragma unroll
    for (int i = 0; i < 3; ++i) {
        const int t = wn * 3 + i;
        f32x4 c4[4];
#pragma unroll
        for (int g = 0; g < 4; ++g) c4[g] = *(const f32x4*)(csl + t * 32 + g * 8 + hh * 4);
#pragma unroll
        for (int j = 0; j < 2; ++j) {
            u32x4 o;
#pragma unroll
            for (int e = 0; e < 4; ++e) {
                const f32x2 cg = {c4[e >> 1][(e & 1) * 2], c4[e >> 1][(e & 1) * 2 + 1]}, cu = {c4[2 + (e >> 1)][(e & 1) * 2], c4[2 + (e >> 1)][(e & 1) * 2 + 1]};
                f32x2 ga = f32x2{acc[i][j][2 * e], acc[i][j][2 * e + 1]} * st[j][0] + cg * st[j][1];
                const f32x2 up = f32x2{acc[i][j][8 + 2 * e], acc[i][j][9 + 2 * e]} * st[j][0] + cu * st[j][1];
                ga[0] = fmaxf(ga[0], 0.f); ga[1] = fmaxf(ga[1], 0.f);
                const f32x2 hv = ga * up;
                o[e] = cvtpk(hv[0], hv[1]);
            }
            gstore4<true>(hb + (j * 96 + i) * 1024, lane * 16, o);
        }
    }
    STAMP(3, 4);
    signal_flag(sig + (size_t)(tm * 2 + wm) * FL_GROUP + (tn * 2 + wn) * 64, tag);
    STAMP(3, 3);
    BAR_L();
}

__device__ __forceinline__ void proj_tile(int& tm, int& tn) {
    const int b = BID(), xcd = b & 7, j0 = b >> 3;
    tm = xcd * 4 + (j0 & 3); tn = j0 >> 2;
}
struct ProjW {
    const char* wb; unsigned lv;
    __device__ __forceinline__ ProjW(const char* WT) { wb = WT; lv = (TID() & 63) * 16; }
    __device__ __forceinline__ void operator()(int s, unsigned d) const {
        glds4<true>(wb + s * 10240, lv, lv, lv, lv, d);
        glds4<true>(wb + s * 10240 + 4096, lv, lv, lv, lv, d + 4096);
        glds2<true>(wb + s * 10240 + 8192, lv, lv, d + 8192);
    }
};

template <int KTOT, int WS0>
__device__ __forceinline__ void proj_phase(char* smem, const char* A, const char* Xold, char* Xnew, char* stats, char* out,
                                           const char* dep, char* sig, u64 tag, int L) {
    constexpr int NST = KTOT / 64, KS16 = KTOT / 16, KID = KTOT == 640 ? 2 : 4;
    const int lane = TID() & 63;
    const int w = __builtin_amdgcn_readfirstlane(TID() >> 6);
    int tm, tn;
    proj_tile(tm, tn);
    const unsigned lbase = (unsigned)(uintptr_t)(lds_t)smem;
    STAMP(KID, 0);
    if (w >= 6) { idle_barriers<NST + 2>(); return; }
    if (w >= 4) {
        const int lw = w - 4;
        const char* as = A + (size_t)((tm * 2 + lw) * KS16) * 1024;
        const unsigned xv = lane * 16;
        wait_flags(dep + (size_t)tm * FL_GROUP + (KTOT == 640 ? lw * 2048 : 0), 32, tag);
        STAMPT(KID, 5, 256);
        ring_loader<NST, X_NS, PDXP, 4>([&](int s, int slot) { glds4<true>(as + s * 4096, xv, xv, xv, xv, lbase + slot * X_SLOT + lw * 4096); });
        BAR(); BAR();
        return;
    }
    const int g = lane >> 4, c16 = lane & 15;
    const int r32 = (w & 1) * 16 + c16;
    const unsigned tilv = (g >> 1) * 512 + r32 * 16 + (g & 1) * 8;
    const size_t tilb = (size_t)((tm * 2 + (w >> 1)) * 40 + tn * 5) * 1024;
    u32x2 res[5];
    {
        const bool rowm = KTOT == 640 && L == 0;
        const char* rb = rowm ? Xold + ((size_t)(tm * 64 + w * 16) * 640 + tn * 80) * 2 : Xold + tilb;
        const unsigned rv = rowm ? c16 * 1280 + g * 8 : tilv;
        const int rs = rowm ? 32 : 1024;
#pragma unroll
        for (int i = 0; i < 5; ++i) res[i] = gload2<true>(rb + i * rs, rv);
    }
    const int aoff = (w >> 1) * 4096 + g * 512 + r32 * 16;
    const int woff = WR_OFF + lane * 16;
    f32x4 acc[5];
#pragma unroll
    for (int i = 0; i < 5; ++i) acc[i] = f32x4{0.f, 0.f, 0.f, 0.f};
    bf16x8 af[2], wf[2][5];
    asm volatile("" : "+v"(acc[0]), "+v"(acc[1]), "+v"(acc[2]), "+v"(acc[3]), "+v"(acc[4]));
    BAR();
    STAMP(KID, 1);
#pragma unroll
    for (int kk = 0; kk < 2; ++kk) {
        af[kk] = ldsr128(smem + aoff + kk * 2048);
#pragma unroll
        for (int i = 0; i < 5; ++i) wf[kk][i] = ldsr128(smem + WS0 * W_SLOT + woff + (kk * 5 + i) * 1024);
    }
    LGKM0();
    int xs = X_SLOT, ws = ((WS0 + 1) & (W_NS - 1)) * W_SLOT;
    int xa = aoff, wa = WS0 * W_SLOT + woff;
#define PM(kk, i) acc[i] = MFMA16(wf[kk][i], af[kk], acc[i]); SB()
#define PW(kk, i) wf[kk][i] = ldsr128(smem + wa + (kk * 5 + i) * 1024)
#pragma unroll 1
    for (int s = 0; s < NST; ++s) {
        PM(0, 0); PW(1, 4); af[1] = ldsr128(smem + xa + 2048); SB();
        xa = xs + aoff; wa = ws + woff;
        asm volatile("" : "+v"(xa), "+v"(wa));
        SB();
        PM(0, 1); BAR(); SB();
        PM(0, 2); PW(0, 0); PW(0, 1); SB();
        PM(0, 3); PW(0, 2); SB();
        PM(0, 4); PW(0, 3); SB();
        PM(1, 0); PW(0, 4); af[0] = ldsr128(smem + xa); SB();
        PM(1, 1); PW(1, 0); SB();
        xs = xs + X_SLOT == X_NS * X_SLOT ? 0 : xs + X_SLOT;
        ws = ws + W_SLOT == W_NS * W_SLOT ? 0 : ws + W_SLOT;
        asm volatile("" : "+s"(xs), "+s"(ws));
        PM(1, 2); PW(1, 1); SB();
        PM(1, 3); PW(1, 2); SB();
        PM(1, 4); PW(1, 3); SB();
    }
#undef PM
#undef PW
    STAMP(KID, 2);
    asm volatile("s_waitcnt vmcnt(0)" : "+v"(res[0]), "+v"(res[1]), "+v"(res[2]), "+v"(res[3]), "+v"(res[4]) :: "memory");
    u32x2 o[5];
#pragma unroll
    for (int i = 0; i < 5; ++i) {
        acc[i] += f32x4{bflo(res[i][0]), bfhi(res[i][0]), bflo(res[i][1]), bfhi(res[i][1])};
        o[i] = u32x2{cvtpk(acc[i][0], acc[i][1]), cvtpk(acc[i][2], acc[i][3])};
    }
    if (KTOT == 1536 && out != nullptr) {
        char* ob = out + ((size_t)(tm * 64 + w * 16) * 640 + tn * 80) * 2;
#pragma unroll
        for (int i = 0; i < 5; ++i) gstore2<false>(ob + i * 32, c16 * 1280 + g * 8, o[i]);
    } else {
#pragma unroll
        for (int i = 0; i < 5; ++i) gstore2<true>(Xnew + tilb + i * 1024, tilv, o[i]);
        f32x4 s1 = acc[0], s2 = acc[0] * acc[0];
#pragma unroll
        for (int i = 1; i < 5; ++i) { s1 += acc[i]; s2 += acc[i] * acc[i]; }
        const float t1 = xsum16_32((s1[0] + s1[1]) + (s1[2] + s1[3])), t2 = xsum16_32((s2[0] + s2[1]) + (s2[2] + s2[3]));
        if (g == 0) gstore2<true>(stats + ((size_t)tn * S_ + tm * 64 + w * 16) * 8, c16 * 8, u32x2{__float_as_uint(t1), __float_as_uint(t2)});
        STAMP(KID, 4);
        signal_flag(sig + (size_t)(tm >> 1) * FL_GROUP + ((tm & 1) * 32 + tn * 4 + w) * 64, tag);
    }
    STAMP(KID, 3);
    BAR_L();
}

constexpr int WS_WO = 2, WS_GU = (WS_WO + 10) & 7, WS_WD = (WS_GU + 20) & 7, WS_QKV = (WS_WD + 24) & 7;

template <int NST, int WS0, class Cur, class Next>
__device__ __forceinline__ void w_phase(unsigned ring, const Cur& cur, const Next& next) {
    int slot = (WS0 + W_PD) & (W_NS - 1);
#pragma unroll 1
    for (int s = 0; s < NST - W_PD; ++s) {
        WAIT_V(40);
        BAR();
        cur(s + W_PD, ring + slot * W_SLOT);
        slot = (slot + 1) & (W_NS - 1);
    }
#pragma unroll 1
    for (int s = 0; s < W_PD; ++s) {
        WAIT_V(40);
        BAR();
        next(s, ring + slot * W_SLOT);
        slot = (slot + 1) & (W_NS - 1);
    }
    BAR(); BAR();
}
template <int NST, int WS0, int D, class Cur>
__device__ __forceinline__ void w_phase_last(unsigned ring, const Cur& cur) {
    int slot = (WS0 + W_PD) & (W_NS - 1);
#pragma unroll 1
    for (int s = 0; s < NST - W_PD; ++s) {
        WAIT_V(40);
        BAR();
        cur(s + W_PD, ring + slot * W_SLOT);
        slot = (slot + 1) & (W_NS - 1);
    }
#pragma unroll
    for (int r = W_PD - 1; r >= 0; --r) {
        WAIT_V(r * D < 40 ? r * D : 40);
        BAR();
    }
    BAR(); BAR();
}

__device__ __forceinline__ void loader_wave(char* smem, const Params& p, u64 tag) {
    const int lane = TID() & 63;
    const unsigned lbase = (unsigned)(uintptr_t)(lds_t)smem, ring = lbase + WR_OFF;
    const unsigned lv = lane * 16;
    int ptm, ptn, gtm, gtn, qtm, qtn, head, qblk;
    const bool hasq = qkv_tile(qtm, qtn);
    const QkvW qk(p.wqkv, qtn);
    if (hasq) {
        ring_loader<QKV_NST, W_NS, 3, 8, false>([&](int s, int slot) { qk(s, ring + slot * W_SLOT); });
        BAR(); BAR(); BAR();
    }
    static_assert(WS_QKV == 0, "first QKV phase starts at slot 0");
    proj_tile(ptm, ptn);
    gu_tile(gtm, gtn);
    attn_tile(head, qblk);
    char* wt = p.ws + WS_WT + (size_t)ptn * 68 * 5120;
    const ProjW wo(wt), wd(wt + 20 * 5120);
    char* gt = p.ws + WS_GT + (size_t)gtn * 20 * 12288;
    const GuW gu(gt);
    unsigned pv[3];
    kv_piece_offs(head, pv);
    const unsigned fr = ((head * 20 + ((lane >> 2) & 1) * 19) >> 6) * 256 + (lane & 3) * 64;
    const unsigned fo = (((lane >> 3) & 1) + 1) * 2560 + fr;
    const unsigned fo0 = lane < 32 ? (lane >> 4) * 8192 + fo : qblk * 8192 + fr;
#pragma unroll 1
    for (int L = 0; L < 2; ++L) {
        char* fl = p.ws + WS_FLAGS + (size_t)L * FL_BYTES;
        const char* kv = p.ws + WS_L0 + (size_t)L * L_BYTES + L_KV;
        __builtin_amdgcn_s_sleep(POLL_PRE);
        wait_flags_at(fl + FL_Q, fo0, lane < 40, tag);
        BAR();
        int slot = 1;
#pragma unroll 1
        for (int c = 1; c < AT_NCH; ++c) {
            BAR();
#pragma unroll
            for (int i = 0; i < 8; ++i) glds3<true>(kv + (size_t)(c * 8 + i) * KV_REC, pv[0], pv[1], pv[2], lbase + slot * AT_SLOT + i * 3072);
            if (c + 1 < AT_NCH) wait_flags_at(fl + FL_Q + (c + 1) * 8192, fo, lane < 16, tag);
            WAIT_V(0);
            slot = slot + 1 == AT_NS ? 0 : slot + 1;
            if (L == 0) {
                constexpr int SCR = WR_OFF + 7 * W_SLOT;
                const int u = ptm + (c - 2) * 32;
                if (c >= 2 && c <= 4 && u < 68) {
                    const bool isd = u >= 20;
                    const int g = lane >> 4;
                    const int q0 = isd ? (g >> 1) * 4 + (g & 1) : g * 2, qd = isd ? 1536 : 768;
                    const char* rp = smem + SCR + (c & 1) * 6144 + q0 * 768 + ((lane >> 2) & 3) * 64 + (lane & 3) * 8;
#pragma unroll
                    for (int i = 0; i < 5; ++i) {
                        const int c2 = (ptn & 1) + i;
                        const char* r2 = rp + (c2 >> 1) * 256 + (c2 & 1) * 32;
                        const bf16x8 f = __builtin_shufflevector(ldstr(r2), ldstr(r2 + qd), 0, 1, 2, 3, 4, 5, 6, 7);
                        gstore4<true>(wt + u * 5120 + i * 1024, lv, __builtin_bit_cast(u32x4, f));
                    }
                }
                const int un = u + 32;
                if (c <= 3 && un < 68) {
                    const char* src = (un >= 20 ? p.wd + (size_t)(un - 20) * 32 * 1280 : p.wo + (size_t)un * 32 * 1280) + ((ptn * 80) & ~31) * 2;
                    unsigned wv[3];
#pragma unroll
                    for (int i = 0; i < 3; ++i) {
                        const int idx = i * 64 + lane, kq = idx / 48, rem = idx - kq * 48;
                        wv[i] = (kq * 4 + ((rem >> 2) & 3)) * 1280 + (rem >> 4) * 64 + (rem & 3) * 16 - i * 1024;
                    }
                    const unsigned d = lbase + SCR + ((c + 1) & 1) * 6144;
                    glds3(src, wv[0], wv[1], wv[2], d);
                    glds3(src + 16 * 1280, wv[0], wv[1], wv[2], d + 3072);
                }
                const int gs = gtm + (c - 2) * 16;
                if ((c == 2 || c == 3) && gs < 20) {
                    const char* rp = smem + WR_OFF + (8 - c) * W_SLOT + ((lane >> 4) & 1) * 6144 + (lane >> 5) * 1536 + ((lane >> 2) & 3) * 64 + (lane & 3) * 8;
#pragma unroll
                    for (int i = 0; i < 12; ++i) {
                        const int ks = i / 6, t = i % 6;
                        const bf16x8 f = ldstr2(rp + ks * 3072 + (t >> 1) * 256 + (t & 1) * 32, 768);
                        gstore4<true>(gt + gs * 12288 + i * 1024, lv, __builtin_bit_cast(u32x4, f));
                    }
                }
                if (c <= 2 && gs + 16 < 20) {
                    const GuNat gn(p.wg, p.wu, gtn);
                    gn(gs + 16, ring + (7 - c) * W_SLOT);
                }
                if (c == 5) {
                    signal_flag(fl + FL_P + (size_t)ptn * FL_GROUP + ptm * 64, tag);
                    signal_flag(fl + FL_T + (size_t)(gtn * 16 + gtm) * 64, tag);
                }
                if (c == 6) wait_flags_at(fl, lane < 32 ? FL_P + ptn * FL_GROUP + lane * 64 : (lane < 48 ? FL_T + (gtn * 16 + (lane & 15)) * 64 :
                                              FL_U + ((lane & 2) * 12 + ((gtn * 96 + (lane & 1) * 95) >> 6)) * 8), lane < 52, tag);
            }
            if (c == 7) {
#pragma unroll 1
                for (int s = 0; s < 3; ++s) wo(s, ring + (WS_WO + s) * W_SLOT);
            }
        }
        BAR();
#pragma unroll 1
        for (int s = 3; s < W_PD; ++s) wo(s, ring + (WS_WO + s) * W_SLOT);
        BAR();
        BAR();
        w_phase<10, WS_WO>(ring, wo, gu);
        glds1<true>(p.ws + WS_CSG + gtn * 768, lv, lbase + CS_OFF);
        w_phase<GU_NST, WS_GU>(ring, gu, wd);
        if (L == 0 && hasq) {
            w_phase<24, WS_WD>(ring, wd, qk);
            w_phase_last<QKV_NST, WS_QKV, 8>(ring, qk);
        } else {
            w_phase_last<24, WS_WD, 10>(ring, wd);
        }
    }
}

__global__ __launch_bounds__(576, 3) void k_main(Params p) {
    __shared__ __attribute__((aligned(1024))) char smem[LDS_BYTES];
    startup(smem, p);
    u64 tag;
    asm volatile("s_load_dwordx2 %0, %1, 0x0\n\ts_waitcnt lgkmcnt(0)" : "=s"(tag) : "s"(p.ws + WS_TAG) : "memory");
    tag = tag * 0x9E3779B97F4A7C15ull + 0x632BE59BD9B4E019ull;
    if (__builtin_amdgcn_readfirstlane(threadIdx.x >> 6) == 8) {
        loader_wave(smem, p, tag);
        asm volatile(".Lk_code_hi:");
        if (blockIdx.x == 0 && (threadIdx.x & 63) == 0) *(u64*)(p.ws + WS_TAG) = tag;
        return;
    }
    qkv_phase<0, WS_QKV>(smem, p, nullptr, nullptr, p.ws + WS_L0 + L_Q, p.ws + WS_L0 + L_KV, nullptr, p.ws + WS_FLAGS + FL_Q, tag);
#pragma unroll 1
    for (int L = 0; L < 2; ++L) {
        char* lw = p.ws + WS_L0 + (size_t)L * L_BYTES;
        char* fl = p.ws + WS_FLAGS + (size_t)L * FL_BYTES;
#define OPQ() asm volatile("" : "+s"(lw), "+s"(fl), "+s"(p.ws), "+s"(p.x0))
        OPQ();
        attn_phase(smem, lw + L_Q, lw + L_KV, lw + L_O, fl + FL_A, tag, L);
        OPQ();
        proj_phase<640, WS_WO>(smem, lw + L_O, L ? lw + L_X0 : p.x0, lw + L_X1, lw + L_SB, nullptr, fl + FL_A, fl + FL_W, tag, L);
        OPQ();
        gu_phase<WS_GU>(smem, lw + L_X1, lw + L_SB, lw + L_H, fl + FL_W, fl + FL_G, tag, L);
        OPQ();
        proj_phase<1536, WS_WD>(smem, lw + L_H, lw + L_X1, lw + L_BYTES + L_X0, lw + L_BYTES + L_SA, L ? p.out : nullptr, fl + FL_G,
                                fl + FL_BYTES + FL_D, tag, L);
        if (L) break;
        lw += L_BYTES; fl += FL_BYTES;
        OPQ();
        qkv_phase<1, WS_QKV>(smem, p, lw + L_X0, lw + L_SA, lw + L_Q, lw + L_KV, fl + FL_D, fl + FL_Q, tag);
    }
}

extern "C" void kernel_launch(void* const* d_in, const int* in_sizes, int n_in, void* d_out, int out_size, void* d_ws, size_t ws_size,
                              hipStream_t stream) {
    Params p;
    p.x0 = (const char*)d_in[0];
    p.wqkv = (const char*)d_in[1];
    p.wo = (const char*)d_in[2];
    p.wg = (const char*)d_in[4];
    p.wu = (const char*)d_in[5];
    p.wd = (const char*)d_in[6];
    p.ws = (char*)d_ws;
    p.out = (char*)d_out;
    k_main<<<256, 576, 0, stream>>>(p);
}
```

```cpp
#include <hip/hip_runtime.h>
#include <hip/hip_bf16.h>
#include <stdint.h>

typedef short bf16x8 __attribute__((ext_vector_type(8)));
typedef short s16x4 __attribute__((ext_vector_type(4)));
typedef float f32x32 __attribute__((ext_vector_type(32)));
typedef float f32x16 __attribute__((ext_vector_type(16)));
typedef float f32x4 __attribute__((ext_vector_type(4)));
typedef float f32x2 __attribute__((ext_vector_type(2)));
typedef unsigned u32x4 __attribute__((ext_vector_type(4)));
typedef unsigned u32x2 __attribute__((ext_vector_type(2)));
typedef unsigned short u16;
typedef __attribute__((address_space(3))) char* lds_t;
typedef __attribute__((address_space(1))) unsigned gu32;
typedef unsigned long long u64;
typedef __attribute__((address_space(1))) u64 gu64;

#define CODE_WARM 1
#define TLB_WARM 32768
#define LPRIO 3
#define APRIO 1
#define MAP 8
constexpr int S_ = 2048, D_ = 640, H_ = 32, F_ = 1536;
constexpr float LN_EPS = 1e-5f;
constexpr float QSCALE = 0.22360679774997896f * 1.4426950408889634f;
constexpr unsigned ONES2 = 0x3f803f80u;

constexpr size_t XT_BYTES = (size_t)S_ * D_ * 2;
constexpr size_t HT_BYTES = (size_t)S_ * F_ * 2;
constexpr int KV_REC = 82048, KV_V = 40960, KV_ONES = 81920;
constexpr size_t KV_BYTES = (size_t)64 * KV_REC;
constexpr size_t ST_BYTES = (size_t)8 * S_ * 8;
constexpr size_t FL_GROUP = 64 * 64;
constexpr size_t FL_A = 0, FL_W = FL_A + 32 * FL_GROUP, FL_G = FL_W + 16 * FL_GROUP, FL_D = FL_G + 32 * FL_GROUP,
                 FL_P = FL_D + 16 * FL_GROUP, FL_Q = FL_P + 8 * FL_GROUP, FL_U = FL_Q + 16 * FL_GROUP, FL_T = FL_U + FL_GROUP, FL_BYTES = FL_T + 4 * FL_GROUP;
constexpr size_t WS_FLAGS = 0;
constexpr size_t WS_TAG = WS_FLAGS + 2 * FL_BYTES;
constexpr size_t WS_CSG = WS_TAG + 4096;
constexpr size_t WS_WT = WS_CSG + 16384;
constexpr size_t WS_GT = WS_WT + (size_t)8 * 68 * 5120;
constexpr size_t WS_L0 = WS_GT + (size_t)16 * 20 * 12288;
constexpr size_t L_X0 = 0, L_SA = L_X0 + XT_BYTES, L_Q = L_SA + ST_BYTES, L_KV = L_Q + XT_BYTES, L_O = L_KV + KV_BYTES, L_X1 = L_O + XT_BYTES,
                 L_SB = L_X1 + XT_BYTES, L_H = L_SB + ST_BYTES, L_BYTES = L_H + HT_BYTES;

constexpr int X_SLOT = 8192, X_NS = 6;
constexpr int WR_OFF = X_NS * X_SLOT;
constexpr int W_SLOT = 12288, W_NS = 8, W_PD = 6;
constexpr int CS_OFF = WR_OFF + 98304, CQ_OFF = CS_OFF + 1024, S2_OFF = CQ_OFF + 512, DUM_OFF = S2_OFF + 2048, STP_OFF = DUM_OFF + 256;

constexpr int LDS_BYTES = STP_OFF;
#define STAMP(kid, idx) do {} while (0)
#define STAMPT(kid, idx, thr) do {} while (0)
#define WAIT_V(n) asm volatile("s_waitcnt vmcnt(%0)" ::"i"(n) : "memory")
#define BAR() asm volatile("s_barrier" ::: "memory")
#define BAR_L() asm volatile("s_waitcnt lgkmcnt(0)\n\ts_barrier" ::: "memory")
#define LGKM0() __builtin_amdgcn_s_waitcnt(0xC07F)
#define SB() __builtin_amdgcn_sched_barrier(0)

__device__ __forceinline__ int TID() { int t = threadIdx.x; asm volatile("" : "+v"(t)); return t; }
__device__ __forceinline__ int BID() { int t = blockIdx.x; asm volatile("" : "+s"(t)); return t; }

#define GL_ "global_load_lds_dwordx4 "
template <bool SC1> __device__ __forceinline__ void glds1(const void* sb, unsigned v, unsigned lds) {
    if constexpr (SC1) asm volatile("s_mov_b32 m0, %2\n\ts_nop 0\n\t" GL_ "%0, %1 sc1" :: "v"(v), "s"(sb), "s"(lds) : "memory");
    else asm volatile("s_mov_b32 m0, %2\n\ts_nop 0\n\t" GL_ "%0, %1" :: "v"(v), "s"(sb), "s"(lds) : "memory");
}
template <bool SC1> __device__ __forceinline__ void glds2(const void* sb, unsigned v0, unsigned v1, unsigned lds) {
    if constexpr (SC1) asm volatile("s_mov_b32 m0, %3\n\ts_nop 0\n\t" GL_ "%0, %2 sc1\n\t" GL_ "%1, %2 offset:1024 sc1" :: "v"(v0), "v"(v1), "s"(sb), "s"(lds) : "memory");
    else asm volatile("s_mov_b32 m0, %3\n\ts_nop 0\n\t" GL_ "%0, %2\n\t" GL_ "%1, %2 offset:1024" :: "v"(v0), "v"(v1), "s"(sb), "s"(lds) : "memory");
}
template <bool SC1 = false> __device__ __forceinline__ void glds3(const void* sb, unsigned v0, unsigned v1, unsigned v2, unsigned lds) {
    if constexpr (SC1) asm volatile("s_mov_b32 m0, %4\n\ts_nop 0\n\t" GL_ "%0, %3 sc1\n\t" GL_ "%1, %3 offset:1024 sc1\n\t" GL_ "%2, %3 offset:2048 sc1"
                 :: "v"(v0), "v"(v1), "v"(v2), "s"(sb), "s"(lds) : "memory");
    else asm volatile("s_mov_b32 m0, %4\n\ts_nop 0\n\t" GL_ "%0, %3\n\t" GL_ "%1, %3 offset:1024\n\t" GL_ "%2, %3 offset:2048"
                 :: "v"(v0), "v"(v1), "v"(v2), "s"(sb), "s"(lds) : "memory");
}
template <bool SC1> __device__ __forceinline__ void glds4(const void* sb, unsigned v0, unsigned v1, unsigned v2, unsigned v3, unsigned lds) {
    if constexpr (SC1) asm volatile("s_mov_b32 m0, %5\n\ts_nop 0\n\t" GL_ "%0, %4 sc1\n\t" GL_ "%1, %4 offset:1024 sc1\n\t" GL_ "%2, %4 offset:2048 sc1\n\t" GL_ "%3, %4 offset:3072 sc1"
                 :: "v"(v0), "v"(v1), "v"(v2), "v"(v3), "s"(sb), "s"(lds) : "memory");
    else asm volatile("s_mov_b32 m0, %5\n\ts_nop 0\n\t" GL_ "%0, %4\n\t" GL_ "%1, %4 offset:1024\n\t" GL_ "%2, %4 offset:2048\n\t" GL_ "%3, %4 offset:3072"
                 :: "v"(v0), "v"(v1), "v"(v2), "v"(v3), "s"(sb), "s"(lds) : "memory");
}
template <bool SC1> __device__ __forceinline__ u32x2 gload2(const void* sb, unsigned v) {
    u32x2 r;
    if constexpr (SC1) asm volatile("global_load_dwordx2 %0, %1, %2 sc1" : "=v"(r) : "v"(v), "s"(sb) : "memory");
    else asm volatile("global_load_dwordx2 %0, %1, %2" : "=v"(r) : "v"(v), "s"(sb) : "memory");
    return r;
}
template <bool SC1> __device__ __forceinline__ void gstore2(void* sb, unsigned v, u32x2 d) {
    if constexpr (SC1) asm volatile("global_store_dwordx2 %0, %1, %2 sc1" :: "v"(v), "v"(d), "s"(sb) : "memory");
    else asm volatile("global_store_dwordx2 %0, %1, %2" :: "v"(v), "v"(d), "s"(sb) : "memory");
}
template <bool SC1> __device__ __forceinline__ void gstore4(void* sb, unsigned v, u32x4 d) {
    if constexpr (SC1) asm volatile("global_store_dwordx4 %0, %1, %2 sc1\n\ts_nop 1" :: "v"(v), "v"(d), "s"(sb) : "memory");
    else asm volatile("global_store_dwordx4 %0, %1, %2\n\ts_nop 1" :: "v"(v), "v"(d), "s"(sb) : "memory");
}

typedef __bf16 bf2_t __attribute__((ext_vector_type(2)));
__device__ __forceinline__ unsigned cvtpk(float lo, float hi) {
    f32x2 v = {lo, hi};
    return __builtin_bit_cast(unsigned, __builtin_convertvector(v, bf2_t));
}
__device__ __forceinline__ float bflo(unsigned u) { return __uint_as_float(u << 16); }
__device__ __forceinline__ float bfhi(unsigned u) { return __uint_as_float(u & 0xffff0000u); }
__device__ __forceinline__ bf16x8 ldsr128(const char* p) { return *(const bf16x8*)p; }
__device__ __forceinline__ s16x4 ldsr64(const char* p) { return *(const s16x4*)p; }
__device__ __forceinline__ s16x4 ldstr(const char* p) {
    return __builtin_bit_cast(s16x4, __builtin_amdgcn_ds_read_tr16_b64_v4i16((__attribute__((address_space(3))) s16x4*)p));
}
__device__ __forceinline__ bf16x8 ldstr2(const char* p, int d) { return __builtin_shufflevector(ldstr(p), ldstr(p + d), 0, 1, 2, 3, 4, 5, 6, 7); }
#define MFMA32(a, b, c) __builtin_amdgcn_mfma_f32_32x32x16_bf16((a), (b), (c), 0, 0, 0)
#define MFMA16(a, b, c) __builtin_amdgcn_mfma_f32_16x16x32_bf16((a), (b), (c), 0, 0, 0)
#define MFMA4(a, b, c) __builtin_amdgcn_mfma_f32_4x4x4bf16_1k((a), (b), (c), 0, 0, 0)
#define MFMA2B(a, b, c) __builtin_amdgcn_mfma_f32_32x32x4bf16_1k((a), (b), (c), 0, 0, 0)
#define CAT16(a, b) __builtin_shufflevector(a, b, 0,1,2,3,4,5,6,7,8,9,10,11,12,13,14,15,16,17,18,19,20,21,22,23,24,25,26,27,28,29,30,31)

__device__ __forceinline__ float xsum32(float a) {
    auto rr = __builtin_amdgcn_permlane32_swap(__float_as_uint(a), __float_as_uint(a), false, false);
    return __uint_as_float(rr[0]) + __uint_as_float(rr[1]);
}
__device__ __forceinline__ float xmax32(float a) {
    auto rr = __builtin_amdgcn_permlane32_swap(__float_as_uint(a), __float_as_uint(a), false, false);
    return fmaxf(__uint_as_float(rr[0]), __uint_as_float(rr[1]));
}
__device__ __forceinline__ float xsum16_32(float x) {
    auto s = __builtin_amdgcn_permlane16_swap(__float_as_uint(x), __float_as_uint(x), false, false);
    return xsum32(__uint_as_float(s[0]) + __uint_as_float(s[1]));
}
__device__ __forceinline__ f32x2 ln_finish(float s1, float s2) {
    const float mean = s1 * (1.f / 640.f);
    const float var = fmaxf(s2 * (1.f / 640.f) - mean * mean, 0.f);
    const float rstd = __builtin_amdgcn_rsqf(var + LN_EPS);
    return f32x2{rstd, -mean * rstd};
}

__device__ __forceinline__ void signal_flag(char* flag, u64 tag) {
    asm volatile("" : "+s"(tag), "+s"(flag));
    unsigned zero = 0;
    asm volatile("" : "+v"(zero));
    asm volatile("s_waitcnt vmcnt(0)\n\tglobal_store_dwordx2 %0, %1, %2 sc1" :: "v"(zero), "v"(tag), "s"(flag) : "memory");
}
#define POLL_PRE 8
#define POLL_SLEEP 2
__device__ __forceinline__ void wait_flags_at(const char* base, unsigned off, bool use, u64 tag) {
    const gu64* f = (const gu64*)(base + off);
    bool ok = !use;
#pragma unroll 1
    for (unsigned spins = 0; spins < 100000u; ++spins) {
        if (!ok) ok = __hip_atomic_load(f, __ATOMIC_RELAXED, __HIP_MEMORY_SCOPE_AGENT) == tag;
        if (__all(ok)) break;
        __builtin_amdgcn_s_sleep(POLL_SLEEP);
    }
    asm volatile("" ::: "memory");
}
__device__ __forceinline__ void wait_flags(const char* group, int n, u64 tag) {
    const int lane = TID() & 63;
    __builtin_amdgcn_s_sleep(POLL_PRE);
    wait_flags_at(group, lane * 64, lane < n, tag);
}

template <int NST, int NS, int PD, int D, bool PRE = true, class Issue>
__device__ __forceinline__ void ring_loader(Issue issue) {
    static_assert(PD <= NS - 2 && (PD - 1) * D <= 63 && PD <= NST, "ring geometry");
    if (PRE) {
#pragma unroll 1
        for (int s = 0; s < PD; ++s) issue(s, s);
    }
    int slot = PD;
#pragma unroll 1
    for (int s = 0; s < NST - PD; ++s) {
        WAIT_V((PD - 1) * D);
        BAR();
        issue(s + PD, slot);
        slot = slot + 1 == NS ? 0 : slot + 1;
    }
#pragma unroll
    for (int r = PD - 1; r >= 0; --r) {
        WAIT_V(r * D);
        BAR();
    }
}
template <int N> __device__ __forceinline__ void idle_barriers() {
#pragma unroll 1
    for (int i = 0; i < N; ++i) BAR();
}
#define PDX 3
#define PDXP 4

constexpr int QKV_NST = 20;

__device__ __forceinline__ void ln_issue(u32x2 (&p)[8], const char* stats, int tok0) {
#pragma unroll
    for (int c = 0; c < 8; ++c) p[c] = gload2<true>(stats + ((size_t)c * S_ + tok0) * 8, (TID() & 63) * 8);
}
template <bool SQ = false>
__device__ __forceinline__ f32x2 ln_collect(u32x2 (&p)[8]) {
    asm volatile("s_waitcnt vmcnt(0)" : "+v"(p[0]), "+v"(p[1]), "+v"(p[2]), "+v"(p[3]), "+v"(p[4]), "+v"(p[5]), "+v"(p[6]), "+v"(p[7]) :: "memory");
    f32x2 a = __builtin_bit_cast(f32x2, p[0]);
#pragma unroll
    for (int c = 1; c < 8; ++c) a += __builtin_bit_cast(f32x2, p[c]);
    if (SQ) {
        const float mean = a[0] * (1.f / 640.f);
        const float var = fmaxf(a[1] * (1.f / 640.f) - mean * mean, 0.f);
        return f32x2{__builtin_amdgcn_rcpf(var + LN_EPS), -mean};
    }
    return ln_finish(a[0], a[1]);
}
__device__ __forceinline__ void split_halves(f32x2 v, f32x2& lo, f32x2& hi) {
#pragma unroll
    for (int e = 0; e < 2; ++e) {
        auto rr = __builtin_amdgcn_permlane32_swap(__float_as_uint(v[e]), __float_as_uint(v[e]), false, false);
        lo[e] = __uint_as_float(rr[0]); hi[e] = __uint_as_float(rr[1]);
    }
}

template <bool FIRST, int WS0, bool ISV>
__device__ __forceinline__ void qkv_compute(char* smem, const char* stats, char* Qb, char* KVb, char* sig, u64 tag, int tm, int tn, int w, int L) {
    const int lane = TID() & 63;
    const int wm = w & 1, wn = w >> 1;
    const int r32 = lane & 31, hh = lane >> 5;
    int xo[2][2], woff[2];
#pragma unroll
    for (int ks = 0; ks < 2; ++ks)
#pragma unroll
        for (int j = 0; j < 2; ++j) {
            const int tt = wm * 2 + (j ^ wn);
            xo[ks][j] = FIRST ? (tt * 32 + r32) * 128 + (((ks * 2 + hh) ^ ((r32 >> 1) & 7)) * 16) : tt * 2048 + ks * 1024 + lane * 16;
        }
#pragma unroll
    for (int i = 0; i < 2; ++i) woff[i] = WR_OFF + hh * 2048 + (wn * 2 + (i ^ wm)) * 256 + ((lane >> 2) & 3) * 64 + ((lane >> 4) & 1) * 32 + (lane & 3) * 8;
    f32x16 acc[2][2];
#pragma unroll
    for (int i = 0; i < 2; ++i)
#pragma unroll
        for (int j = 0; j < 2; ++j)
#pragma unroll
            for (int r = 0; r < 16; ++r) acc[i][j][r] = 0.f;
    bf16x8 xf[2][2], wf[2][2];
    u32x2 part[8];
    f32x4 s1 = {0.f, 0.f, 0.f, 0.f}, s2 = s1, cs = s1;
    s16x4 ones = {0x3f80, 0x3f80, 0x3f80, 0x3f80};
    asm volatile("" : "+v"(acc[0][0]), "+v"(acc[0][1]), "+v"(acc[1][0]), "+v"(acc[1][1]), "+v"(xo[0][0]), "+v"(xo[0][1]), "+v"(woff[0]), "+v"(woff[1]));
    if (FIRST) asm volatile("" : "+v"(s1), "+v"(s2), "+v"(cs), "+v"(ones), "+v"(xo[1][0]), "+v"(xo[1][1]));
    BAR();
    STAMP(0, 1);
#define XADDR(ks, j, key) (FIRST ? smem + (xo[ks][j] ^ (key)) : smem + (key) + xo[0][j] + (ks) * 1024)
#pragma unroll
    for (int ks = 0; ks < 2; ++ks) {
#pragma unroll
        for (int j = 0; j < 2; ++j) xf[ks][j] = ldsr128(XADDR(ks, j, 0));
#pragma unroll
        for (int i = 0; i < 2; ++i) wf[ks][i] = ldstr2(smem + WS0 * W_SLOT + woff[i] + ks * 4096, 1024);
    }
    if (!FIRST) ln_issue(part, stats, tm * 128 + wm * 64);
    LGKM0();
    int xk = FIRST ? 64 : 8192, ws = ((WS0 + 1) & (W_NS - 1)) * W_SLOT;
    int xc = 0, wa0 = WS0 * W_SLOT + woff[0], wa1 = WS0 * W_SLOT + woff[1];
#define QM(ks, i, j) acc[i][j] = ISV ? MFMA32(xf[ks][j], wf[ks][i], acc[i][j]) : MFMA32(wf[ks][i], xf[ks][j], acc[i][j]); SB()
#define QW(ks, i) wf[ks][i] = ldstr2(smem + (i ? wa1 : wa0) + ks * 4096, 1024)
#define QX(ks, j) xf[ks][j] = ldsr128(XADDR(ks, j, xc))
#define LO4(v) __builtin_shufflevector(v, v, 0, 1, 2, 3)
#define HI4(v) __builtin_shufflevector(v, v, 4, 5, 6, 7)
#define QS0(ks) if (FIRST) { s1 = MFMA4(ones, LO4(xf[ks][0]), s1); s2 = MFMA4(LO4(xf[ks][0]), LO4(xf[ks][0]), s2); SB(); }
#define QS1(ks) if (FIRST) { cs = MFMA4(ones, LO4(wf[ks][0]), cs); s1 = MFMA4(ones, HI4(xf[ks][0]), s1); SB(); }
#define QS2(ks) if (FIRST) { s2 = MFMA4(HI4(xf[ks][0]), HI4(xf[ks][0]), s2); cs = MFMA4(ones, HI4(wf[ks][0]), cs); SB(); }
#pragma unroll 1
    for (int s = 0; s < QKV_NST; ++s) {
        QM(0, 1, 1); QW(1, 0); QX(1, 0); SB();
        xc = xk; wa0 = ws + woff[0]; wa1 = ws + woff[1];
        asm volatile("" : "+v"(wa0), "+v"(wa1));
        SB();
        QS0(0)
        QM(0, 1, 0); BAR(); SB();
        QS1(0)
        QM(0, 0, 1); QW(0, 1); SB();
        QS2(0)
        QM(0, 0, 0); QX(0, 1); SB();
        QM(1, 1, 1); QW(0, 0); QX(0, 0); SB();
        QS0(1)
        QM(1, 1, 0);
        if (FIRST) xk = (xk & 64) ? (xk == 2 * 16384 + 64 ? 0 : xk + 16384 - 64) : xk + 64;
        else xk = xk + 8192 == 6 * 8192 ? 0 : xk + 8192;
        ws = ws + W_SLOT == W_NS * W_SLOT ? 0 : ws + W_SLOT;
        asm volatile("" : "+s"(xk), "+s"(ws));
        SB();
        QS1(1)
        QM(1, 0, 1); QW(1, 1); SB();
        QS2(1)
        QM(1, 0, 0); QX(1, 1); SB();
    }
#undef QW
#undef QX
#undef QS0
#undef QS1
#undef QS2
#undef QM
#undef XADDR
    STAMP(0, 2);
    f32x2 stl;
    if (FIRST) {
        const float q2 = (lane & 2) ? ((lane & 1) ? s2[3] : s2[2]) : ((lane & 1) ? s2[1] : s2[0]);
        const f32x2 st = ln_finish(xsum32(s1[0]), xsum32(q2));
        const float c = xsum32(cs[0]);
        if (lane < 32) {
            *(f32x2*)(smem + CS_OFF + ((wm * 2 + wn) * 32 + lane) * 8) = st;
            *(float*)(smem + CQ_OFF + ((wn * 2 + wm) * 32 + lane) * 4) = c;
        }
        BAR_L();
        stl = *(const f32x2*)(smem + CS_OFF + (wm * 64 + lane) * 8);
    } else {
        stl = ln_collect(part);
    }
    const float* csl = (const float*)(smem + CQ_OFF);
    const int tile0 = tm * 4 + wm * 2;
    if (!ISV) {
        const bool isq = tn < 5;
        f32x2 lo, hi, st[2];
        split_halves(stl * (isq ? QSCALE : 1.f), lo, hi);
        st[0] = wn ? hi : lo; st[1] = wn ? lo : hi;
        const int tstride = isq ? 40960 : KV_REC;
        char* ob = (isq ? Qb : KVb) + (size_t)tile0 * tstride + ((isq ? tn : tn - 5) * 16 + wn * 8) * 512;
#pragma unroll
        for (int q = 0; q < 4; ++q) {
            const int qq = q ^ (wm * 2);
            f32x4 c4[2];
#pragma unroll
            for (int e = 0; e < 2; ++e) c4[e] = *(const f32x4*)(csl + wn * 64 + qq * 16 + e * 8 + hh * 4);
#pragma unroll
            for (int j = 0; j < 2; ++j) {
                const f32x16& a = acc[q >> 1][j];
                unsigned pk[2][2];
#pragma unroll
                for (int e = 0; e < 2; ++e) {
                    const int r = (q & 1) * 8 + e * 4;
                    const f32x2 v0 = f32x2{a[r], a[r + 1]} * st[j][0] + f32x2{c4[e][0], c4[e][1]} * st[j][1];
                    const f32x2 v1 = f32x2{a[r + 2], a[r + 3]} * st[j][0] + f32x2{c4[e][2], c4[e][3]} * st[j][1];
                    pk[e][0] = cvtpk(v0[0], v0[1]); pk[e][1] = cvtpk(v1[0], v1[1]);
                }
                auto r0 = __builtin_amdgcn_permlane32_swap(pk[0][0], pk[1][0], false, false);
                auto r1 = __builtin_amdgcn_permlane32_swap(pk[0][1], pk[1][1], false, false);
                gstore4<true>(ob + (j ^ wn) * tstride + qq * 1024, lane * 16, u32x4{r0[0], r1[0], r0[1], r1[1]});
            }
        }
    } else {
        const char* sl = smem + S2_OFF + w * 512;
        *(f32x2*)(smem + S2_OFF + w * 512 + lane * 8) = stl;
        char* ob = KVb + (size_t)tile0 * KV_REC + KV_V + ((tn - 10) * 128 + wn * 64) * 16;
        const unsigned ov = hh * 10240 + r32 * 16;
        float c1[2];
#pragma unroll
        for (int i = 0; i < 2; ++i) c1[i] = csl[(wn * 2 + (i ^ wm)) * 32 + r32];
#pragma unroll
        for (int j = 0; j < 2; ++j)
#pragma unroll
            for (int s2_ = 0; s2_ < 2; ++s2_) {
                f32x4 sa[2], sb[2];
#pragma unroll
                for (int q = 0; q < 2; ++q) {
                    const char* sp = sl + ((j ^ wn) * 32 + (s2_ * 2 + q) * 8 + hh * 4) * 8;
                    sa[q] = *(const f32x4*)sp; sb[q] = *(const f32x4*)(sp + 16);
                }
#pragma unroll
                for (int i = 0; i < 2; ++i) {
                    float v[8];
#pragma unroll
                    for (int q = 0; q < 2; ++q) {
                        const int r = (s2_ * 2 + q) * 4;
                        v[q * 4 + 0] = acc[i][j][r + 0] * sa[q][0] + c1[i] * sa[q][1];
                        v[q * 4 + 1] = acc[i][j][r + 1] * sa[q][2] + c1[i] * sa[q][3];
                        v[q * 4 + 2] = acc[i][j][r + 2] * sb[q][0] + c1[i] * sb[q][1];
                        v[q * 4 + 3] = acc[i][j][r + 3] * sb[q][2] + c1[i] * sb[q][3];
                    }
                    gstore4<true>(ob + (j ^ wn) * KV_REC + s2_ * 20480 + (i ^ wm) * 512, ov,
                                  u32x4{cvtpk(v[0], v[1]), cvtpk(v[2], v[3]), cvtpk(v[4], v[5]), cvtpk(v[6], v[7])});
                }
            }
        if (lane < 16) {
            unsigned one = ONES2;
            asm volatile("" : "+v"(one));
            gstore4<true>(KVb + (size_t)tile0 * KV_REC + KV_ONES, (lane >> 3) * KV_REC + (lane & 7) * 16, u32x4{one, one, one, one});
        }
    }
    STAMP(0, 4);
    signal_flag(sig + (size_t)(((tm >> 1) * 32 + tn * 2 + wn) * 4 + (tm & 1) * 2 + wm) * 64, tag);
    STAMP(0, 3);
    BAR_L();
}

__device__ __forceinline__ bool qkv_tile(int& tm, int& tn) {
    const int b = BID(), xcd = b & 7, j = b >> 3;
    const int id = (xcd & 3) * 30 + j;
    tm = (xcd >> 2) * 8 + (id & 7); tn = id >> 3;
    return j < 30;
}
struct QkvW {
    const char* wsb; unsigned wv;
    __device__ __forceinline__ QkvW(const char* W, int tn) {
        const int lane = TID() & 63;
        wsb = W + tn * 256;
        wv = ((lane >> 2) & 3) * 3840 + (lane >> 4) * 64 + (lane & 3) * 16;
    }
    __device__ __forceinline__ void operator()(int s, unsigned dst) const {
        constexpr unsigned dv = 4 * 3840 - 1024;
        glds4<false>(wsb + (size_t)s * (32 * 3840), wv, wv + dv, wv + 2 * dv, wv + 3 * dv, dst);
        glds4<false>(wsb + (size_t)s * (32 * 3840) + 16 * 3840, wv, wv + dv, wv + 2 * dv, wv + 3 * dv, dst + 4096);
    }
};

struct Params {
    const char *x0, *wqkv, *wo, *wg, *wu, *wd;
    char *ws, *out;
};
__device__ __forceinline__ void acc8(float (&a)[8], const u32x4& v) {
#pragma unroll
    for (int j = 0; j < 4; ++j) { a[2 * j] += bflo(v[j]); a[2 * j + 1] += bfhi(v[j]); }
}
__device__ __forceinline__ void pre_csg(char* smem, const Params& p, u64 tag) {
    const int t = TID(), b = BID();
    const int sidx = (b & 7) * 2 + ((b >> 3) - 30);
    const int lane = t & 63, w = __builtin_amdgcn_readfirstlane(t >> 6), cw = w % 3, half = w / 3;
    const char* src = (sidx < 8 ? p.wg : p.wu) + (sidx & 7) * 384 + cw * 128 + (lane & 7) * 16 + (size_t)(half * 320 + (lane >> 3)) * 3072;
    float a[8] = {0, 0, 0, 0, 0, 0, 0, 0};
#pragma unroll 1
    for (int o = 0; o < 2; ++o) {
        u32x4 v[20];
#pragma unroll
        for (int i = 0; i < 20; ++i) v[i] = *(const u32x4*)(src + (size_t)(o * 20 + i) * 8 * 3072);
#pragma unroll
        for (int i = 0; i < 20; ++i) acc8(a, v[i]);
    }
#pragma unroll
    for (int j = 0; j < 8; ++j) {
        a[j] += __int_as_float(__builtin_amdgcn_ds_swizzle(__float_as_int(a[j]), 0x201f));
        a[j] = xsum16_32(a[j]);
    }
    volatile float* xch = (volatile float*)(smem + S2_OFF) + cw * 64;
    volatile u64* lf = (volatile u64*)(smem + S2_OFF + 1024) + cw;
    if (half) {
        if (lane < 8) {
#pragma unroll
            for (int j = 0; j < 8; ++j) xch[lane * 8 + j] = a[j];
        }
        asm volatile("s_waitcnt lgkmcnt(0)" ::: "memory");
        if (lane == 0) *lf = tag;
        return;
    }
#pragma unroll 1
    for (unsigned spins = 0; spins < 1000000u && *lf != tag; ++spins) __builtin_amdgcn_s_sleep(2);
    if (lane < 8) {
#pragma unroll
        for (int j = 0; j < 8; ++j) a[j] += xch[lane * 8 + j];
        const int c = (sidx & 7) * 192 + cw * 64 + lane * 8;
        const unsigned o = ((c >> 4) * 32 + (sidx < 8 ? 0 : 16) + (c & 15)) * 4;
        gstore4<true>(p.ws + WS_CSG, o, u32x4{__float_as_uint(a[0]), __float_as_uint(a[1]), __float_as_uint(a[2]), __float_as_uint(a[3])});
        gstore4<true>(p.ws + WS_CSG + 16, o, u32x4{__float_as_uint(a[4]), __float_as_uint(a[5]), __float_as_uint(a[6]), __float_as_uint(a[7])});
    }
    signal_flag(p.ws + WS_FLAGS + FL_U + (size_t)(sidx * 3 + cw) * 8, tag);
}

struct XRow {
    const char* xs; unsigned v0, v1, dst;
    __device__ __forceinline__ XRow(const char* x0, int tm, int lw, unsigned lbase) {
        const int lane = TID() & 63, r8 = lane >> 3;
        xs = x0 + (size_t)(tm * 128 + lw * 64) * 1280;
        v0 = r8 * 1280 + (((lane & 7) ^ (r8 >> 1)) * 16);
        v1 = (r8 + 8) * 1280 - 1024 + (((lane & 7) ^ (4 + (r8 >> 1))) * 16);
        dst = lbase + lw * 8192;
    }
    __device__ __forceinline__ void operator()(int d, int slot) const {
        constexpr unsigned d2 = 16 * 1280 - 2048;
        glds4<false>(xs + d * 128, v0, v1, v0 + d2, v1 + d2, dst + slot * 16384);
        glds4<false>(xs + d * 128 + 32 * 1280, v0, v1, v0 + d2, v1 + d2, dst + slot * 16384 + 4096);
    }
};
__device__ __forceinline__ void startup(char* smem, const Params& p) {
    int tm, tn;
    const bool has = qkv_tile(tm, tn);
    const int w = __builtin_amdgcn_readfirstlane(TID() >> 6);
    const unsigned lbase = (unsigned)(uintptr_t)(lds_t)smem;
    if (w == 8) {
        if (has) {
            const QkvW qk(p.wqkv, tn);
#pragma unroll 1
            for (int s = 0; s < 3; ++s) qk(s, lbase + WR_OFF + s * W_SLOT);
        }
    } else if (w == 7) {
        const char* pc;
        int len;
        asm volatile("s_getpc_b64 %0\n.Lk_code_lo:\n\ts_mov_b32 %1, .Lk_code_hi-.Lk_code_lo" : "=s"(pc), "=s"(len));
        int o = ((BID() >> 3) * 16 + (TID() & 15)) * 128;
        o = o < len ? o : 0;
        asm volatile("s_mov_b32 m0, %2\n\ts_nop 0\n\tglobal_load_lds_dword %0, %1" :: "v"(o), "s"(pc), "s"(lbase + DUM_OFF) : "memory");
    } else if (w == 6) {
        constexpr unsigned USED = (unsigned)(WS_L0 + 2 * L_BYTES);
        unsigned o = ((BID() >> 3) * 64 + (TID() & 63)) * TLB_WARM;
        o = o < USED ? o : 0;
        asm volatile("s_mov_b32 m0, %2\n\ts_nop 0\n\tglobal_load_lds_dword %0, %1 sc1" :: "v"(o), "s"(p.ws), "s"(lbase + DUM_OFF) : "memory");
    } else if ((w == 4 || w == 5) && has) {
        const XRow xr(p.x0, tm, w - 4, lbase);
        xr(0, 0); xr(1, 1);
    }
}

template <int L, int WS0>
__device__ __forceinline__ void qkv_phase(char* smem, const Params& p, const char* X, const char* stats, char* Qb, char* KVb, const char* dep,
                                          char* sig, u64 tag) {
    int tm, tn;
    const bool has = qkv_tile(tm, tn);
    const int lane = TID() & 63;
    const int w = __builtin_amdgcn_readfirstlane(TID() >> 6);
    const unsigned lbase = (unsigned)(uintptr_t)(lds_t)smem;
    STAMP(0, 0);
    if (!has) {
        if (L == 0 && w < 6) pre_csg(smem, p, tag);
        return;
    }
    if (w >= 6) {
        idle_barriers<QKV_NST + 2>();
        if (L == 0) BAR();
    } else if (w < 4) {
        if (tn >= 10) qkv_compute<L == 0, WS0, true>(smem, stats, Qb, KVb, sig, tag, tm, tn, w, L);
        else qkv_compute<L == 0, WS0, false>(smem, stats, Qb, KVb, sig, tag, tm, tn, w, L);
    } else if (L) {
        const int lw = w - 4;
        const char* xs = X + (size_t)(tm * 4 + lw * 2) * 40 * 1024;
        const unsigned xv = lane * 16, xv2 = xv + 40 * 1024 - 2048;
        wait_flags(dep + (size_t)tm * FL_GROUP + lw * 2048, 32, tag);
        STAMPT(0, 5, 256);
        ring_loader<QKV_NST, X_NS, PDX, 4>([&](int s, int slot) { glds4<true>(xs + s * 2048, xv, xv, xv2, xv2, lbase + slot * X_SLOT + lw * 4096); });
        BAR(); BAR();
    } else {
        const XRow issue(p.x0, tm, w - 4, lbase);
        int slot = 2;
#pragma unroll 1
        for (int d = 0; d < 8; ++d) {
            WAIT_V(8);
            BAR();
            BAR();
            issue(d + 2, slot);
            slot = slot == 2 ? 0 : slot + 1;
        }
        WAIT_V(8);
        BAR(); BAR();
        WAIT_V(0);
        BAR(); BAR();
        BAR(); BAR(); BAR();
    }
}

constexpr int AT_SLOT = 8 * 3072, AT_NS = 3, AT_NCH = 8;

__device__ __forceinline__ float rowmax32(const f32x32& s) {
    float a = fmaxf(fmaxf(s[0], s[1]), s[2]), b = fmaxf(fmaxf(s[3], s[4]), s[5]);
#pragma unroll
    for (int r = 6; r < 30; r += 4) { a = fmaxf(fmaxf(a, s[r]), s[r + 1]); b = fmaxf(fmaxf(b, s[r + 2]), s[r + 3]); }
    a = fmaxf(fmaxf(a, s[30]), s[31]);
    return xmax32(fmaxf(a, b));
}

__device__ __forceinline__ void attn_tile(int& head, int& qblk) {
    const int b = BID(), xcd = b & 7, j = b >> 3;
    head = xcd * 4 + (j & 3); qblk = j >> 2;
}
__device__ __forceinline__ void kv_piece_offs(int head, unsigned (&v)[3]) {
    const int lane = TID() & 63;
    const int c0 = (head * 5) >> 1, odd = head & 1;
    v[0] = (c0 + odd) * 512 + lane * 16;
#pragma unroll
    for (int i = 1; i < 3; ++i) {
        const int idx = i * 64 + lane - 96, sh = idx / 24, row = idx - sh * 24;
        unsigned o = row < 20 ? KV_V + sh * 10240 + (head * 20 + row) * 16 : KV_ONES + (row - 20) * 16;
        if (idx < 0) o = (odd ? c0 : c0 + 2) * 512 + lane * 16;
        v[i] = o - i * 1024;
    }
}

__device__ __forceinline__ void attn_phase(char* smem, const char* Qb, const char* KVb, char* O, char* sig, u64 tag, int L) {
    const int tid = TID(), lane = tid & 63;
    const int w = __builtin_amdgcn_readfirstlane(tid >> 6);
    int head, qblk;
    attn_tile(head, qblk);
    const int r32 = lane & 31, hh = lane >> 5;
    const int qtile = qblk * 8 + w;
    const int c0 = (head * 5) >> 1, odd = head & 1;
    STAMP(1, 0);
    const unsigned lbase = (unsigned)(uintptr_t)(lds_t)smem;
    unsigned pv[3];
    kv_piece_offs(head, pv);
    const unsigned qhv = (odd ? c0 : c0 + 2) * 512 + r32 * 16 + odd * 8;
    const int koff = lane * 16, khoff = hh * 3072 + 1024 + r32 * 16 + odd * 8;
    const int vrow = r32 <= 20 ? r32 : (r32 < 28 ? 20 : 16);
    const int voff = 1536 + hh * 384 + vrow * 16;
    asm volatile("" : "+v"(pv[0]), "+v"(pv[1]), "+v"(pv[2]));
    BAR();
    STAMP(1, 5);
    glds3<true>(KVb + (size_t)w * KV_REC, pv[0], pv[1], pv[2], lbase + w * 3072);
    bf16x8 q0;
    s16x4 q4;
    {
        const char* qp = Qb + (size_t)qtile * 40960;
        asm volatile("global_load_dwordx4 %0, %1, %2 sc1" : "=v"(q0) : "v"(pv[0]), "s"(qp) : "memory");
        asm volatile("global_load_dwordx2 %0, %1, %2 sc1" : "=v"(q4) : "v"(qhv), "s"(qp) : "memory");
    }
    f32x16 zero;
#pragma unroll
    for (int r = 0; r < 16; ++r) zero[r] = 0.f;
    f32x16 o = zero, m16;
    f32x32 SA, SBn;
    asm volatile("s_waitcnt vmcnt(0)" : "+v"(q0), "+v"(q4) :: "memory");
    BAR();
    STAMP(1, 1);
    bf16x8 ka, kb, va0, va1, vb0, vb1;
    s16x4 kh;
    u32x4 pa0, pa1, pb0, pb1 = {0u, 0u, 0u, 0u};
    {
        ka = ldsr128(smem + koff); kb = ldsr128(smem + 3072 + koff); kh = ldsr64(smem + khoff);
        f32x16 n0 = MFMA32(ka, q0, zero), n1 = MFMA32(kb, q0, zero);
        SA = MFMA2B(kh, q4, CAT16(n0, n1));
        const float m = rowmax32(SA);
#pragma unroll
        for (int r = 0; r < 16; ++r) m16[r] = -m;
#pragma unroll
        for (int r = 0; r < 32; ++r) SA[r] -= m;
        ka = ldsr128(smem + 2 * 3072 + koff); kb = ldsr128(smem + 3 * 3072 + koff); kh = ldsr64(smem + 2 * 3072 + khoff);
        va0 = ldsr128(smem + voff); va1 = ldsr128(smem + voff + 768); vb0 = ldsr128(smem + 3072 + voff); vb1 = vb0;
    }
    asm volatile("" : "+v"(m16));
    LGKM0();
    SB();
#define EX(x) x = __builtin_amdgcn_exp2f(x)
#define BF8(x) __builtin_bit_cast(bf16x8, x)
#define G4(S, i, P, jj) EX(S[i]); EX(S[i + 1]); SB(); EX(S[i + 2]); P[jj] = cvtpk(S[i], S[i + 1]); SB(); EX(S[i + 3]); SB();
#define STEP2(S, SN, V1, V2, V3, K4, K5, KH4) do { \
        f32x16 n0, n1; \
        o = MFMA32(vb1, BF8(pb1), o); SB(); vb1 = ldsr128((V1) + 768); SB(); \
        G4(S, 0, pa0, 0) \
        n0 = MFMA32(ka, q0, m16); SB(); ka = ldsr128(K4); pa0[1] = cvtpk(S[2], S[3]); SB(); \
        G4(S, 4, pa0, 2) \
        n1 = MFMA32(kb, q0, m16); SB(); kb = ldsr128(K5); pa0[3] = cvtpk(S[6], S[7]); SB(); \
        G4(S, 8, pa1, 0) \
        o = MFMA32(va0, BF8(pa0), o); SB(); va0 = ldsr128(V2); pa1[1] = cvtpk(S[10], S[11]); SB(); \
        G4(S, 12, pa1, 2) \
        SN = MFMA2B(kh, q4, CAT16(n0, n1)); SB(); kh = ldsr64(KH4); pa1[3] = cvtpk(S[14], S[15]); SB(); \
        G4(S, 16, pb0, 0) \
        pb0[1] = cvtpk(S[18], S[19]); SB(); \
        G4(S, 20, pb0, 2) \
        o = MFMA32(va1, BF8(pa1), o); SB(); va1 = ldsr128((V2) + 768); pb0[3] = cvtpk(S[22], S[23]); SB(); \
        G4(S, 24, pb1, 0) \
        pb1[1] = cvtpk(S[26], S[27]); SB(); \
        o = MFMA32(vb0, BF8(pb0), o); SB(); vb0 = ldsr128(V3); SB(); \
        G4(S, 28, pb1, 2) \
        pb1[3] = cvtpk(S[30], S[31]); SB(); \
    } while (0)
    if (w >= 4) __builtin_amdgcn_s_setprio(APRIO);
    int slot = 0;
#pragma unroll 1
    for (int c = 0; c < AT_NCH; ++c) {
        const int nslot = slot + 1 == AT_NS ? 0 : slot + 1;
        const char* cv = smem + slot * AT_SLOT + voff;   const char* ck = smem + slot * AT_SLOT + koff;   const char* ch = smem + slot * AT_SLOT + khoff;
        const char* nv = smem + nslot * AT_SLOT + voff;  const char* nk = smem + nslot * AT_SLOT + koff;  const char* nh = smem + nslot * AT_SLOT + khoff;
        STEP2(SA, SBn, cv + 1 * 3072, cv + 2 * 3072, cv + 3 * 3072, ck + 4 * 3072, ck + 5 * 3072, ch + 4 * 3072);
        STEP2(SBn, SA, cv + 3 * 3072, cv + 4 * 3072, cv + 5 * 3072, ck + 6 * 3072, ck + 7 * 3072, ch + 6 * 3072);
        BAR();
        SB();
        STEP2(SA, SBn, cv + 5 * 3072, cv + 6 * 3072, cv + 7 * 3072, nk, nk + 3072, nh);
        STEP2(SBn, SA, cv + 7 * 3072, nv, nv + 3072, nk + 2 * 3072, nk + 3 * 3072, nh + 2 * 3072);
        slot = nslot;
    }
    o = MFMA32(vb1, BF8(pb1), o);
    __builtin_amdgcn_s_setprio(0);
    STAMP(1, 2);
    STAMPT(1, 6, 448);
    float l;
    {
        auto rr = __builtin_amdgcn_permlane32_swap(__float_as_uint(o[8]), __float_as_uint(o[8]), false, false);
        l = __uint_as_float(rr[1]);
    }
    float big = fmaxf(fmaxf(fabsf(o[0]), fabsf(o[1])), fabsf(o[2]));
#pragma unroll
    for (int r = 3; r < 9; r += 2) big = fmaxf(fmaxf(big, fabsf(o[r])), fabsf(o[r + 1]));
    float sum = ((o[0] + o[1]) + (o[2] + o[3])) + ((o[4] + o[5]) + (o[6] + o[7])) + o[8];
    if (!hh) { big = fmaxf(fmaxf(big, fabsf(o[9])), fmaxf(fabsf(o[10]), fabsf(o[11]))); sum += (o[9] + o[10]) + o[11]; }
    const bool bad = !(l < 1.8446744073709552e19f) || !(big < 1.0e37f) || !(sum == sum);
    if (__builtin_expect(__any(bad), 0)) {
        float m = -INFINITY;
        o = zero;
        const unsigned gk = (c0 + odd) * 512 + lane * 16, gh = hh * KV_REC + qhv;
        const unsigned gv0 = r32 < 20 ? KV_V + hh * 10240 + (head * 20 + r32) * 16 : KV_ONES, gv1 = r32 < 20 ? gv0 + 20480 : KV_ONES;
#pragma unroll 1
        for (int t = 0; t < 64; t += 2) {
            const char* tp = KVb + (size_t)t * KV_REC;
            bf16x8 fk[2], fv[4];
            s16x4 fh;
            asm volatile("global_load_dwordx4 %0, %7, %11 sc1\n\tglobal_load_dwordx4 %1, %7, %12 sc1\n\tglobal_load_dwordx2 %2, %8, %11 sc1\n\t"
                         "global_load_dwordx4 %3, %9, %11 sc1\n\tglobal_load_dwordx4 %4, %10, %11 sc1\n\t"
                         "global_load_dwordx4 %5, %9, %12 sc1\n\tglobal_load_dwordx4 %6, %10, %12 sc1\n\ts_waitcnt vmcnt(0)"
                         : "=&v"(fk[0]), "=&v"(fk[1]), "=&v"(fh), "=&v"(fv[0]), "=&v"(fv[1]), "=&v"(fv[2]), "=&v"(fv[3])
                         : "v"(gk), "v"(gh), "v"(gv0), "v"(gv1), "s"(tp), "s"(tp + KV_REC) : "memory");
            f32x16 n0 = MFMA32(fk[0], q0, zero), n1 = MFMA32(fk[1], q0, zero);
            f32x32 sc = MFMA2B(fh, q4, CAT16(n0, n1));
            const float mn = fmaxf(m, rowmax32(sc));
            const float alpha = __builtin_amdgcn_exp2f(m - mn);
            m = mn;
#pragma unroll
            for (int r = 0; r < 16; ++r) o[r] *= alpha;
#pragma unroll
            for (int q = 0; q < 4; ++q) {
                u32x4 pp;
#pragma unroll
                for (int e = 0; e < 4; ++e) pp[e] = cvtpk(__builtin_amdgcn_exp2f(sc[q * 8 + 2 * e] - mn), __builtin_amdgcn_exp2f(sc[q * 8 + 2 * e + 1] - mn));
                o = MFMA32(fv[q], BF8(pp), o);
            }
        }
        auto rr = __builtin_amdgcn_permlane32_swap(__float_as_uint(o[8]), __float_as_uint(o[8]), false, false);
        l = __uint_as_float(rr[1]);
    }
    const float inv = __builtin_amdgcn_rcpf(l);
    char* ob = O + (size_t)qtile * 40 * 1024;
#pragma unroll
    for (int g = 0; g < 3; ++g) {
        if (g < 2 || !hh) {
            const int col = head * 20 + g * 8 + hh * 4;
            gstore2<true>(ob, (col >> 3) * 512 + (col & 4) * 2 + r32 * 16,
                           u32x2{cvtpk(o[g * 4] * inv, o[g * 4 + 1] * inv), cvtpk(o[g * 4 + 2] * inv, o[g * 4 + 3] * inv)});
        }
    }
    STAMP(1, 4);
    signal_flag(sig + (size_t)(qtile >> 1) * FL_GROUP + ((qtile & 1) * 32 + head) * 64, tag);
    STAMP(1, 3);
    BAR_L();
}
#undef EX
#undef G4
#undef STEP2

constexpr int GU_NST = 20;
__device__ __forceinline__ void gu_tile(int& tm, int& tn) {
    const int b = BID(), xcd = b & 7, j0 = b >> 3;
    tm = (xcd >> 2) * 8 + (j0 & 7); tn = (xcd & 3) * 4 + (j0 >> 3);
}
struct GuW {
    const char* wb; unsigned lv;
    __device__ __forceinline__ GuW(const char* GT) { wb = GT; lv = (TID() & 63) * 16; }
    __device__ __forceinline__ void operator()(int s, unsigned d) const {
#pragma unroll
        for (int i = 0; i < 3; ++i) glds4<true>(wb + s * 12288 + i * 4096, lv, lv, lv, lv, d + i * 4096);
    }
};
struct GuNat {
    const char *gb, *ub; unsigned gv[3];
    __device__ __forceinline__ GuNat(const char* Wg, const char* Wu, int tn) {
        const int lane = TID() & 63;
#pragma unroll
        for (int i = 0; i < 3; ++i) {
            const int idx = i * 64 + lane, kq = idx / 48, rem = idx - kq * 48, sub = rem >> 4, kr = (rem >> 2) & 3, c = rem & 3;
            gv[i] = (kq * 4 + kr) * 3072 + sub * 64 + c * 16 - i * 1024;
        }
        gb = Wg + tn * 192; ub = Wu + tn * 192;
    }
    __device__ __forceinline__ void operator()(int s, unsigned d) const {
        const size_t o = (size_t)s * (32 * 3072);
        glds3(gb + o, gv[0], gv[1], gv[2], d);
        glds3(ub + o, gv[0], gv[1], gv[2], d + 6144);
        glds3(gb + o + 16 * 3072, gv[0], gv[1], gv[2], d + 3072);
        glds3(ub + o + 16 * 3072, gv[0], gv[1], gv[2], d + 9216);
    }
};

template <int WS0>
__device__ __forceinline__ void gu_phase(char* smem, const char* X, const char* stats, char* Hh, const char* dep, char* sig, u64 tag,
                                         int L) {
    const int lane = TID() & 63;
    const int w = __builtin_amdgcn_readfirstlane(TID() >> 6);
    int tm, tn;
    gu_tile(tm, tn);
    const unsigned lbase = (unsigned)(uintptr_t)(lds_t)smem;
    STAMP(3, 0);
    if (w >= 6) { idle_barriers<GU_NST + 2>(); return; }
    if (w >= 4) {
        const int lw = w - 4;
        const char* xs = X + (size_t)(tm * 4 + lw * 2) * 40 * 1024;
        const unsigned xv = lane * 16, xv2 = xv + 40 * 1024 - 2048;
        wait_flags(dep + (size_t)tm * FL_GROUP + lw * 2048, 32, tag);
        STAMPT(3, 5, 256);
        ring_loader<GU_NST, X_NS, PDX, 4>([&](int s, int slot) { glds4<true>(xs + s * 2048, xv, xv, xv2, xv2, lbase + slot * X_SLOT + lw * 4096); });
        BAR(); BAR();
        return;
    }
    const int wm = w & 1, wn = w >> 1;
    const int hh = lane >> 5;
    const int xoff = wm * 4096 + lane * 16;
    int woff = WR_OFF + wn * 3072 + lane * 16;
    f32x16 acc[3][2];
#pragma unroll
    for (int i = 0; i < 3; ++i)
#pragma unroll
        for (int j = 0; j < 2; ++j)
#pragma unroll
            for (int r = 0; r < 16; ++r) acc[i][j][r] = 0.f;
    bf16x8 xf[2][2], wf[2][3];
    u32x2 part[8];
    asm volatile("" : "+v"(acc[0][0]), "+v"(acc[0][1]), "+v"(acc[1][0]), "+v"(acc[1][1]), "+v"(acc[2][0]), "+v"(acc[2][1]), "+v"(woff));
    BAR();
    STAMP(3, 1);
#pragma unroll
    for (int ks = 0; ks < 2; ++ks) {
#pragma unroll
        for (int j = 0; j < 2; ++j) xf[ks][j] = ldsr128(smem + xoff + ks * 1024 + j * 2048);
#pragma unroll
        for (int i = 0; i < 3; ++i) wf[ks][i] = ldsr128(smem + WS0 * W_SLOT + woff + ks * 6144 + i * 1024);
    }
    ln_issue(part, stats, tm * 128 + wm * 64);
    LGKM0();
    int xs = X_SLOT, ws = ((WS0 + 1) & (W_NS - 1)) * W_SLOT;
    int xa = xoff, wa = WS0 * W_SLOT + woff;
#define GM(ks, i, j) acc[i][j] = MFMA32(wf[ks][i], xf[ks][j], acc[i][j]); SB()
#define GW(ks, i) wf[ks][i] = ldsr128(smem + wa + ks * 6144 + i * 1024)
#define GX(ks, j) xf[ks][j] = ldsr128(smem + xa + ks * 1024 + j * 2048)
#pragma unroll 1
    for (int s = 0; s < GU_NST; ++s) {
        GM(0, 0, 0); GW(1, 2); GX(1, 1); SB();
        xa = xs + xoff;
        wa = ws + woff;
        asm volatile("" : "+v"(xa), "+v"(wa));
        SB();
        GM(0, 0, 1); BAR(); SB();
        GM(0, 1, 0); GW(0, 0); SB();
        GM(0, 1, 1);
        xs = xs + X_SLOT == X_NS * X_SLOT ? 0 : xs + X_SLOT;
        ws = ws + W_SLOT == W_NS * W_SLOT ? 0 : ws + W_SLOT;
        asm volatile("" : "+s"(xs), "+s"(ws));
        SB();
        GM(0, 2, 0); GW(0, 1); SB();
        GM(0, 2, 1); GX(0, 0); SB();
        GM(1, 0, 0); GW(0, 2); GX(0, 1); SB();
        GM(1, 0, 1);
        GM(1, 1, 0); GW(1, 0); SB();
        GM(1, 1, 1);
        GM(1, 2, 0); GW(1, 1); SB();
        GM(1, 2, 1); GX(1, 0); SB();
    }
#undef GW
#undef GX
#undef GM
    STAMP(3, 2);
    const float* csl = (const float*)(smem + CS_OFF);
    f32x2 st[2];
    {
        const f32x2 t = ln_collect<true>(part);
        split_halves(t, st[0], st[1]);
    }
    char* hb = Hh + (size_t)((tm * 4 + wm * 2) * 96 + tn * 6 + wn * 3) * 1024;
#pragma unroll
    for (int i = 0; i < 3; ++i) {
        const int t = wn * 3 + i;
        f32x4 c4[4];
#pragma unroll
        for (int g = 0; g < 4; ++g) c4[g] = *(const f32x4*)(csl + t * 32 + g * 8 + hh * 4);
#pragma unroll
        for (int j = 0; j < 2; ++j) {
            u32x4 o;
#pragma unroll
            for (int e = 0; e < 4; ++e) {
                const f32x2 cg = {c4[e >> 1][(e & 1) * 2], c4[e >> 1][(e & 1) * 2 + 1]}, cu = {c4[2 + (e >> 1)][(e & 1) * 2], c4[2 + (e >> 1)][(e & 1) * 2 + 1]};
                f32x2 ga = cg * st[j][1] + f32x2{acc[i][j][2 * e], acc[i][j][2 * e + 1]};
                const f32x2 up = (cu * st[j][1] + f32x2{acc[i][j][8 + 2 * e], acc[i][j][9 + 2 * e]}) * st[j][0];
                ga[0] = fmaxf(ga[0], 0.f); ga[1] = fmaxf(ga[1], 0.f);
                const f32x2 hv = ga * up;
                o[e] = cvtpk(hv[0], hv[1]);
            }
            gstore4<true>(hb + (j * 96 + i) * 1024, lane * 16, o);
        }
    }
    STAMP(3, 4);
    signal_flag(sig + (size_t)(tm * 2 + wm) * FL_GROUP + (tn * 2 + wn) * 64, tag);
    STAMP(3, 3);
    BAR_L();
}

__device__ __forceinline__ void proj_tile(int& tm, int& tn) {
    const int b = BID(), xcd = b & 7, j0 = b >> 3;
    tm = xcd * 4 + (j0 & 3); tn = j0 >> 2;
}
struct ProjW {
    const char* wb; unsigned lv;
    __device__ __forceinline__ ProjW(const char* WT) { wb = WT; lv = (TID() & 63) * 16; }
    __device__ __forceinline__ void operator()(int s, unsigned d) const {
        glds4<true>(wb + s * 10240, lv, lv, lv, lv, d);
        glds4<true>(wb + s * 10240 + 4096, lv, lv, lv, lv, d + 4096);
        glds2<true>(wb + s * 10240 + 8192, lv, lv, d + 8192);
    }
};

template <int KTOT, int WS0>
__device__ __forceinline__ void proj_phase(char* smem, const char* A, const char* Xold, char* Xnew, char* stats, char* out,
                                           const char* dep, char* sig, u64 tag, int L) {
    constexpr int NST = KTOT / 64, KS16 = KTOT / 16, KID = KTOT == 640 ? 2 : 4;
    const int lane = TID() & 63;
    const int w = __builtin_amdgcn_readfirstlane(TID() >> 6);
    int tm, tn;
    proj_tile(tm, tn);
    const unsigned lbase = (unsigned)(uintptr_t)(lds_t)smem;
    STAMP(KID, 0);
    if (w >= 6) { idle_barriers<NST + 2>(); return; }
    if (w >= 4) {
        const int lw = w - 4;
        const char* as = A + (size_t)((tm * 2 + lw) * KS16) * 1024;
        const unsigned xv = lane * 16;
        wait_flags(dep + (size_t)tm * FL_GROUP + (KTOT == 640 ? lw * 2048 : 0), 32, tag);
        STAMPT(KID, 5, 256);
        ring_loader<NST, X_NS, PDXP, 4>([&](int s, int slot) { glds4<true>(as + s * 4096, xv, xv, xv, xv, lbase + slot * X_SLOT + lw * 4096); });
        BAR(); BAR();
        return;
    }
    const int g = lane >> 4, c16 = lane & 15;
    const int r32 = (w & 1) * 16 + c16;
    const unsigned tilv = (g >> 1) * 512 + r32 * 16 + (g & 1) * 8;
    const size_t tilb = (size_t)((tm * 2 + (w >> 1)) * 40 + tn * 5) * 1024;
    u32x2 res[5];
    {
        const bool rowm = KTOT == 640 && L == 0;
        const char* rb = rowm ? Xold + ((size_t)(tm * 64 + w * 16) * 640 + tn * 80) * 2 : Xold + tilb;
        const unsigned rv = rowm ? c16 * 1280 + g * 8 : tilv;
        const int rs = rowm ? 32 : 1024;
#pragma unroll
        for (int i = 0; i < 5; ++i) res[i] = gload2<true>(rb + i * rs, rv);
    }
    const int aoff = (w >> 1) * 4096 + g * 512 + r32 * 16;
    const int woff = WR_OFF + lane * 16;
    char* sigp = sig + (size_t)(tm >> 1) * FL_GROUP + ((tm & 1) * 32 + tn * 4 + w) * 64;
    char* stp = stats + ((size_t)tn * S_ + tm * 64 + w * 16) * 8;
    char* xnp = Xnew + tilb;
    asm volatile("s_waitcnt vmcnt(0)" : "+v"(res[0]), "+v"(res[1]), "+v"(res[2]), "+v"(res[3]), "+v"(res[4]) :: "memory");
    asm volatile("" : "+s"(sigp), "+s"(stp), "+s"(xnp));
    f32x4 acc[5];
#pragma unroll
    for (int i = 0; i < 5; ++i) acc[i] = f32x4{bflo(res[i][0]), bfhi(res[i][0]), bflo(res[i][1]), bfhi(res[i][1])};
    bf16x8 af[2], wf[2][5];
    asm volatile("" : "+v"(acc[0]), "+v"(acc[1]), "+v"(acc[2]), "+v"(acc[3]), "+v"(acc[4]));
    BAR();
    STAMP(KID, 1);
#pragma unroll
    for (int kk = 0; kk < 2; ++kk) {
        af[kk] = ldsr128(smem + aoff + kk * 2048);
#pragma unroll
        for (int i = 0; i < 5; ++i) wf[kk][i] = ldsr128(smem + WS0 * W_SLOT + woff + (kk * 5 + i) * 1024);
    }
    LGKM0();
    int xs = X_SLOT, ws = ((WS0 + 1) & (W_NS - 1)) * W_SLOT;
    int xa = aoff, wa = WS0 * W_SLOT + woff;
#define PM(kk, i) acc[i] = MFMA16(wf[kk][i], af[kk], acc[i]); SB()
#define PW(kk, i) wf[kk][i] = ldsr128(smem + wa + (kk * 5 + i) * 1024)
#pragma unroll 1
    for (int s = 0; s < NST; ++s) {
        PM(0, 0); PW(1, 4); af[1] = ldsr128(smem + xa + 2048); SB();
        xa = xs + aoff; wa = ws + woff;
        asm volatile("" : "+v"(xa), "+v"(wa));
        SB();
        PM(0, 1); BAR(); SB();
        PM(0, 2); PW(0, 0); PW(0, 1); SB();
        PM(0, 3); PW(0, 2); SB();
        PM(0, 4); PW(0, 3); SB();
        PM(1, 0); PW(0, 4); af[0] = ldsr128(smem + xa); SB();
        PM(1, 1); PW(1, 0); SB();
        xs = xs + X_SLOT == X_NS * X_SLOT ? 0 : xs + X_SLOT;
        ws = ws + W_SLOT == W_NS * W_SLOT ? 0 : ws + W_SLOT;
        asm volatile("" : "+s"(xs), "+s"(ws));
        PM(1, 2); PW(1, 1); SB();
        PM(1, 3); PW(1, 2); SB();
        PM(1, 4); PW(1, 3); SB();
    }
#undef PM
#undef PW
    STAMP(KID, 2);
    u32x2 o[5];
#pragma unroll
    for (int i = 0; i < 5; ++i) o[i] = u32x2{cvtpk(acc[i][0], acc[i][1]), cvtpk(acc[i][2], acc[i][3])};
    if (KTOT == 1536 && out != nullptr) {
        char* ob = out + ((size_t)(tm * 64 + w * 16) * 640 + tn * 80) * 2;
#pragma unroll
        for (int i = 0; i < 5; ++i) gstore2<false>(ob + i * 32, c16 * 1280 + g * 8, o[i]);
    } else {
#pragma unroll
        for (int i = 0; i < 5; ++i) gstore2<true>(xnp + i * 1024, tilv, o[i]);
        SB();
        f32x4 s1 = acc[0], s2 = acc[0] * acc[0];
#pragma unroll
        for (int i = 1; i < 5; ++i) { s1 += acc[i]; s2 += acc[i] * acc[i]; }
        const float t1 = xsum16_32((s1[0] + s1[1]) + (s1[2] + s1[3])), t2 = xsum16_32((s2[0] + s2[1]) + (s2[2] + s2[3]));
        gstore2<true>(stp, c16 * 8, u32x2{__float_as_uint(t1), __float_as_uint(t2)});
        STAMP(KID, 4);
        signal_flag(sigp, tag);
    }
    STAMP(KID, 3);
    BAR_L();
}

constexpr int WS_WO = 2, WS_GU = (WS_WO + 10) & 7, WS_WD = (WS_GU + 20) & 7, WS_QKV = (WS_WD + 24) & 7;

template <int NST, int WS0, class Cur, class Next>
__device__ __forceinline__ void w_phase(unsigned ring, const Cur& cur, const Next& next) {
    int slot = (WS0 + W_PD) & (W_NS - 1);
#pragma unroll 1
    for (int s = 0; s < NST - W_PD; ++s) {
        WAIT_V(40);
        BAR();
        cur(s + W_PD, ring + slot * W_SLOT);
        slot = (slot + 1) & (W_NS - 1);
    }
#pragma unroll 1
    for (int s = 0; s < W_PD; ++s) {
        WAIT_V(40);
        BAR();
        next(s, ring + slot * W_SLOT);
        slot = (slot + 1) & (W_NS - 1);
    }
    BAR(); BAR();
}
template <int NST, int WS0, int D, class Cur>
__device__ __forceinline__ void w_phase_last(unsigned ring, const Cur& cur) {
    int slot = (WS0 + W_PD) & (W_NS - 1);
#pragma unroll 1
    for (int s = 0; s < NST - W_PD; ++s) {
        WAIT_V(40);
        BAR();
        cur(s + W_PD, ring + slot * W_SLOT);
        slot = (slot + 1) & (W_NS - 1);
    }
#pragma unroll
    for (int r = W_PD - 1; r >= 0; --r) {
        WAIT_V(r * D < 40 ? r * D : 40);
        BAR();
    }
    BAR(); BAR();
}

__device__ __forceinline__ void loader_wave(char* smem, const Params& p, u64 tag) {
    const int lane = TID() & 63;
    const unsigned lbase = (unsigned)(uintptr_t)(lds_t)smem, ring = lbase + WR_OFF;
    const unsigned lv = lane * 16;
    __builtin_amdgcn_s_setprio(LPRIO);
    int ptm, ptn, gtm, gtn, qtm, qtn, head, qblk;
    const bool hasq = qkv_tile(qtm, qtn);
    const QkvW qk(p.wqkv, qtn);
    if (hasq) {
        ring_loader<QKV_NST, W_NS, 3, 8, false>([&](int s, int slot) { qk(s, ring + slot * W_SLOT); });
        BAR(); BAR(); BAR();
    }
    static_assert(WS_QKV == 0, "first QKV phase starts at slot 0");
    proj_tile(ptm, ptn);
    gu_tile(gtm, gtn);
    attn_tile(head, qblk);
    char* wt = p.ws + WS_WT + (size_t)ptn * 68 * 5120;
    const ProjW wo(wt), wd(wt + 20 * 5120);
    char* gt = p.ws + WS_GT + (size_t)gtn * 20 * 12288;
    const GuW gu(gt);
    unsigned pv[3];
    kv_piece_offs(head, pv);
    const unsigned fr = ((head * 20 + ((lane >> 2) & 1) * 19) >> 6) * 256 + (lane & 3) * 64;
    const unsigned fo = (((lane >> 3) & 1) + 1) * 2560 + fr;
    const unsigned fo0 = lane < 32 ? (lane >> 4) * 8192 + fo : qblk * 8192 + fr;
#pragma unroll 1
    for (int L = 0; L < 2; ++L) {
        char* fl = p.ws + WS_FLAGS + (size_t)L * FL_BYTES;
        const char* kv = p.ws + WS_L0 + (size_t)L * L_BYTES + L_KV;
        __builtin_amdgcn_s_sleep(POLL_PRE);
        wait_flags_at(fl + FL_Q, fo0, lane < 40, tag);
        BAR();
        int slot = 1;
#pragma unroll 1
        for (int c = 1; c < AT_NCH; ++c) {
            BAR();
#pragma unroll
            for (int i = 0; i < 8; ++i) glds3<true>(kv + (size_t)(c * 8 + i) * KV_REC, pv[0], pv[1], pv[2], lbase + slot * AT_SLOT + i * 3072);
            if (c + 1 < AT_NCH) wait_flags_at(fl + FL_Q + (c + 1) * 8192, fo, lane < 16, tag);
            WAIT_V(0);
            slot = slot + 1 == AT_NS ? 0 : slot + 1;
            if (L == 0) {
                constexpr int SCR = WR_OFF + 7 * W_SLOT;
                const int u = ptm + (c - 2) * 32;
                if (c >= 2 && c <= 4 && u < 68) {
                    const bool isd = u >= 20;
                    const int g = lane >> 4;
                    const int q0 = isd ? (g >> 1) * 4 + (g & 1) : g * 2, qd = isd ? 1536 : 768;
                    const char* rp = smem + SCR + (c & 1) * 6144 + q0 * 768 + ((lane >> 2) & 3) * 64 + (lane & 3) * 8;
#pragma unroll
                    for (int i = 0; i < 5; ++i) {
                        const int c2 = (ptn & 1) + i;
                        const char* r2 = rp + (c2 >> 1) * 256 + (c2 & 1) * 32;
                        const bf16x8 f = __builtin_shufflevector(ldstr(r2), ldstr(r2 + qd), 0, 1, 2, 3, 4, 5, 6, 7);
                        gstore4<true>(wt + u * 5120 + i * 1024, lv, __builtin_bit_cast(u32x4, f));
                    }
                }
                const int un = u + 32;
                if (c <= 3 && un < 68) {
                    const char* src = (un >= 20 ? p.wd + (size_t)(un - 20) * 32 * 1280 : p.wo + (size_t)un * 32 * 1280) + ((ptn * 80) & ~31) * 2;
                    unsigned wv[3];
#pragma unroll
                    for (int i = 0; i < 3; ++i) {
                        const int idx = i * 64 + lane, kq = idx / 48, rem = idx - kq * 48;
                        wv[i] = (kq * 4 + ((rem >> 2) & 3)) * 1280 + (rem >> 4) * 64 + (rem & 3) * 16 - i * 1024;
                    }
                    const unsigned d = lbase + SCR + ((c + 1) & 1) * 6144;
                    glds3(src, wv[0], wv[1], wv[2], d);
                    glds3(src + 16 * 1280, wv[0], wv[1], wv[2], d + 3072);
                }
                const int gs = gtm + (c - 2) * 16;
                if ((c == 2 || c == 3) && gs < 20) {
                    const char* rp = smem + WR_OFF + (8 - c) * W_SLOT + ((lane >> 4) & 1) * 6144 + (lane >> 5) * 1536 + ((lane >> 2) & 3) * 64 + (lane & 3) * 8;
#pragma unroll
                    for (int i = 0; i < 12; ++i) {
                        const int ks = i / 6, t = i % 6;
                        const bf16x8 f = ldstr2(rp + ks * 3072 + (t >> 1) * 256 + (t & 1) * 32, 768);
                        gstore4<true>(gt + gs * 12288 + i * 1024, lv, __builtin_bit_cast(u32x4, f));
                    }
                }
                if (c <= 2 && gs + 16 < 20) {
                    const GuNat gn(p.wg, p.wu, gtn);
                    gn(gs + 16, ring + (7 - c) * W_SLOT);
                }
                if (c == 5) {
                    signal_flag(fl + FL_P + (size_t)ptn * FL_GROUP + ptm * 64, tag);
                    signal_flag(fl + FL_T + (size_t)(gtn * 16 + gtm) * 64, tag);
                }
                if (c == 6) wait_flags_at(fl, lane < 32 ? FL_P + ptn * FL_GROUP + lane * 64 : (lane < 48 ? FL_T + (gtn * 16 + (lane & 15)) * 64 :
                                              FL_U + ((lane & 2) * 12 + ((gtn * 96 + (lane & 1) * 95) >> 6)) * 8), lane < 52, tag);
            }
            if (c == 7) {
#pragma unroll 1
                for (int s = 0; s < 3; ++s) wo(s, ring + (WS_WO + s) * W_SLOT);
            }
        }
        BAR();
#pragma unroll 1
        for (int s = 3; s < W_PD; ++s) wo(s, ring + (WS_WO + s) * W_SLOT);
        BAR();
        BAR();
        w_phase<10, WS_WO>(ring, wo, gu);
        glds1<true>(p.ws + WS_CSG + gtn * 768, lv, lbase + CS_OFF);
        w_phase<GU_NST, WS_GU>(ring, gu, wd);
        if (L == 0 && hasq) {
            w_phase<24, WS_WD>(ring, wd, qk);
            w_phase_last<QKV_NST, WS_QKV, 8>(ring, qk);
        } else {
            w_phase_last<24, WS_WD, 10>(ring, wd);
        }
    }
}

__global__ __launch_bounds__(576, 3) void k_main(Params p) {
    __shared__ __attribute__((aligned(1024))) char smem[LDS_BYTES];
    startup(smem, p);
    u64 tag;
    asm volatile("s_load_dwordx2 %0, %1, 0x0\n\ts_waitcnt lgkmcnt(0)" : "=s"(tag) : "s"(p.ws + WS_TAG) : "memory");
    tag = tag * 0x9E3779B97F4A7C15ull + 0x632BE59BD9B4E019ull;
    if (__builtin_amdgcn_readfirstlane(threadIdx.x >> 6) == 8) {
        loader_wave(smem, p, tag);
        asm volatile(".Lk_code_hi:");
        if (blockIdx.x == 0 && (threadIdx.x & 63) == 0) *(u64*)(p.ws + WS_TAG) = tag;
        return;
    }
    qkv_phase<0, WS_QKV>(smem, p, nullptr, nullptr, p.ws + WS_L0 + L_Q, p.ws + WS_L0 + L_KV, nullptr, p.ws + WS_FLAGS + FL_Q, tag);
#pragma unroll 1
    for (int L = 0; L < 2; ++L) {
        char* lw = p.ws + WS_L0 + (size_t)L * L_BYTES;
        char* fl = p.ws + WS_FLAGS + (size_t)L * FL_BYTES;
#define OPQ() asm volatile("" : "+s"(lw), "+s"(fl), "+s"(p.ws), "+s"(p.x0))
        OPQ();
        attn_phase(smem, lw + L_Q, lw + L_KV, lw + L_O, fl + FL_A, tag, L);
        OPQ();
        proj_phase<640, WS_WO>(smem, lw + L_O, L ? lw + L_X0 : p.x0, lw + L_X1, lw + L_SB, nullptr, fl + FL_A, fl + FL_W, tag, L);
        OPQ();
        gu_phase<WS_GU>(smem, lw + L_X1, lw + L_SB, lw + L_H, fl + FL_W, fl + FL_G, tag, L);
        OPQ();
        proj_phase<1536, WS_WD>(smem, lw + L_H, lw + L_X1, lw + L_BYTES + L_X0, lw + L_BYTES + L_SA, L ? p.out : nullptr, fl + FL_G,
                                fl + FL_BYTES + FL_D, tag, L);
        if (L) break;
        lw += L_BYTES; fl += FL_BYTES;
        OPQ();
        qkv_phase<1, WS_QKV>(smem, p, lw + L_X0, lw + L_SA, lw + L_Q, lw + L_KV, fl + FL_D, fl + FL_Q, tag);
    }
}

extern "C" void kernel_launch(void* const* d_in, const int* in_sizes, int n_in, void* d_out, int out_size, void* d_ws, size_t ws_size,
                              hipStream_t stream) {
    Params p;
    p.x0 = (const char*)d_in[0];
    p.wqkv = (const char*)d_in[1];
    p.wo = (const char*)d_in[2];
    p.wg = (const char*)d_in[4];
    p.wu = (const char*)d_in[5];
    p.wd = (const char*)d_in[6];
    p.ws = (char*)d_ws;
    p.out = (char*)d_out;
    k_main<<<256, 576, 0, stream>>>(p);
}
```

```cpp
#include <hip/hip_runtime.h>
#include <hip/hip_bf16.h>
#include <stdint.h>

typedef short bf16x8 __attribute__((ext_vector_type(8)));
typedef short s16x4 __attribute__((ext_vector_type(4)));
typedef float f32x32 __attribute__((ext_vector_type(32)));
typedef float f32x16 __attribute__((ext_vector_type(16)));
typedef float f32x4 __attribute__((ext_vector_type(4)));
typedef float f32x2 __attribute__((ext_vector_type(2)));
typedef unsigned u32x4 __attribute__((ext_vector_type(4)));
typedef unsigned u32x2 __attribute__((ext_vector_type(2)));
typedef unsigned short u16;
typedef __attribute__((address_space(3))) char* lds_t;
typedef __attribute__((address_space(1))) unsigned gu32;
typedef unsigned long long u64;
typedef __attribute__((address_space(1))) u64 gu64;

#define CODE_WARM 1
#define LPRIO 3
#define APRIO 1
#define MAP 8
constexpr int S_ = 2048, D_ = 640, H_ = 32, F_ = 1536;
constexpr float LN_EPS = 1e-5f;
constexpr float QSCALE = 0.22360679774997896f * 1.4426950408889634f;
constexpr unsigned ONES2 = 0x3f803f80u;

constexpr size_t XT_BYTES = (size_t)S_ * D_ * 2;
constexpr size_t HT_BYTES = (size_t)S_ * F_ * 2;
constexpr int KV_REC = 82048, KV_V = 40960, KV_ONES = 81920;
constexpr size_t KV_BYTES = (size_t)64 * KV_REC;
constexpr size_t ST_BYTES = (size_t)8 * S_ * 8;
constexpr size_t FL_GROUP = 64 * 64;
constexpr size_t FL_A = 0, FL_W = FL_A + 32 * FL_GROUP, FL_G = FL_W + 16 * FL_GROUP, FL_D = FL_G + 32 * FL_GROUP,
                 FL_P = FL_D + 16 * FL_GROUP, FL_Q = FL_P + 8 * FL_GROUP, FL_U = FL_Q + 16 * FL_GROUP, FL_T = FL_U + FL_GROUP, FL_BYTES = FL_T + 4 * FL_GROUP;
constexpr size_t WS_FLAGS = 0;
constexpr size_t WS_TAG = WS_FLAGS + 2 * FL_BYTES;
constexpr size_t WS_CSG = WS_TAG + 4096;
constexpr size_t WS_WT = WS_CSG + 16384;
constexpr size_t WS_GT = WS_WT + (size_t)8 * 68 * 5120;
constexpr size_t WS_L0 = WS_GT + (size_t)16 * 20 * 12288;
constexpr size_t L_X0 = 0, L_SA = L_X0 + XT_BYTES, L_Q = L_SA + ST_BYTES, L_KV = L_Q + XT_BYTES, L_O = L_KV + KV_BYTES, L_X1 = L_O + XT_BYTES,
                 L_SB = L_X1 + XT_BYTES, L_H = L_SB + ST_BYTES, L_BYTES = L_H + HT_BYTES;

constexpr int X_SLOT = 8192, X_NS = 6;
constexpr int WR_OFF = X_NS * X_SLOT;
constexpr int W_SLOT = 12288, W_NS = 8, W_PD = 6;
constexpr int CS_OFF = WR_OFF + 98304, CQ_OFF = CS_OFF + 1024, S2_OFF = CQ_OFF + 512, DUM_OFF = S2_OFF + 2048, STP_OFF = DUM_OFF + 256;

constexpr int LDS_BYTES = STP_OFF;
#define STAMP(kid, idx) do {} while (0)
#define STAMPT(kid, idx, thr) do {} while (0)
#define WAIT_V(n) asm volatile("s_waitcnt vmcnt(%0)" ::"i"(n) : "memory")
#define BAR() asm volatile("s_barrier" ::: "memory")
#define BAR_L() asm volatile("s_waitcnt lgkmcnt(0)\n\ts_barrier" ::: "memory")
#define LGKM0() __builtin_amdgcn_s_waitcnt(0xC07F)
#define SB() __builtin_amdgcn_sched_barrier(0)

__device__ __forceinline__ int TID() { int t = threadIdx.x; asm volatile("" : "+v"(t)); return t; }
__device__ __forceinline__ int BID() { int t = blockIdx.x; asm volatile("" : "+s"(t)); return t; }

#define GL_ "global_load_lds_dwordx4 "
template <bool SC1> __device__ __forceinline__ void glds1(const void* sb, unsigned v, unsigned lds) {
    if constexpr (SC1) asm volatile("s_mov_b32 m0, %2\n\ts_nop 0\n\t" GL_ "%0, %1 sc1" :: "v"(v), "s"(sb), "s"(lds) : "memory");
    else asm volatile("s_mov_b32 m0, %2\n\ts_nop 0\n\t" GL_ "%0, %1" :: "v"(v), "s"(sb), "s"(lds) : "memory");
}
template <bool SC1> __device__ __forceinline__ void glds2(const void* sb, unsigned v0, unsigned v1, unsigned lds) {
    if constexpr (SC1) asm volatile("s_mov_b32 m0, %3\n\ts_nop 0\n\t" GL_ "%0, %2 sc1\n\t" GL_ "%1, %2 offset:1024 sc1" :: "v"(v0), "v"(v1), "s"(sb), "s"(lds) : "memory");
    else asm volatile("s_mov_b32 m0, %3\n\ts_nop 0\n\t" GL_ "%0, %2\n\t" GL_ "%1, %2 offset:1024" :: "v"(v0), "v"(v1), "s"(sb), "s"(lds) : "memory");
}
template <bool SC1 = false> __device__ __forceinline__ void glds3(const void* sb, unsigned v0, unsigned v1, unsigned v2, unsigned lds) {
    if constexpr (SC1) asm volatile("s_mov_b32 m0, %4\n\ts_nop 0\n\t" GL_ "%0, %3 sc1\n\t" GL_ "%1, %3 offset:1024 sc1\n\t" GL_ "%2, %3 offset:2048 sc1"
                 :: "v"(v0), "v"(v1), "v"(v2), "s"(sb), "s"(lds) : "memory");
    else asm volatile("s_mov_b32 m0, %4\n\ts_nop 0\n\t" GL_ "%0, %3\n\t" GL_ "%1, %3 offset:1024\n\t" GL_ "%2, %3 offset:2048"
                 :: "v"(v0), "v"(v1), "v"(v2), "s"(sb), "s"(lds) : "memory");
}
template <bool SC1> __device__ __forceinline__ void glds4(const void* sb, unsigned v0, unsigned v1, unsigned v2, unsigned v3, unsigned lds) {
    if constexpr (SC1) asm volatile("s_mov_b32 m0, %5\n\ts_nop 0\n\t" GL_ "%0, %4 sc1\n\t" GL_ "%1, %4 offset:1024 sc1\n\t" GL_ "%2, %4 offset:2048 sc1\n\t" GL_ "%3, %4 offset:3072 sc1"
                 :: "v"(v0), "v"(v1), "v"(v2), "v"(v3), "s"(sb), "s"(lds) : "memory");
    else asm volatile("s_mov_b32 m0, %5\n\ts_nop 0\n\t" GL_ "%0, %4\n\t" GL_ "%1, %4 offset:1024\n\t" GL_ "%2, %4 offset:2048\n\t" GL_ "%3, %4 offset:3072"
                 :: "v"(v0), "v"(v1), "v"(v2), "v"(v3), "s"(sb), "s"(lds) : "memory");
}
template <bool SC1> __device__ __forceinline__ u32x2 gload2(const void* sb, unsigned v) {
    u32x2 r;
    if constexpr (SC1) asm volatile("global_load_dwordx2 %0, %1, %2 sc1" : "=v"(r) : "v"(v), "s"(sb) : "memory");
    else asm volatile("global_load_dwordx2 %0, %1, %2" : "=v"(r) : "v"(v), "s"(sb) : "memory");
    return r;
}
template <bool SC1> __device__ __forceinline__ void gstore2(void* sb, unsigned v, u32x2 d) {
    if constexpr (SC1) asm volatile("global_store_dwordx2 %0, %1, %2 sc1" :: "v"(v), "v"(d), "s"(sb) : "memory");
    else asm volatile("global_store_dwordx2 %0, %1, %2" :: "v"(v), "v"(d), "s"(sb) : "memory");
}
template <bool SC1> __device__ __forceinline__ void gstore4(void* sb, unsigned v, u32x4 d) {
    if constexpr (SC1) asm volatile("global_store_dwordx4 %0, %1, %2 sc1\n\ts_nop 1" :: "v"(v), "v"(d), "s"(sb) : "memory");
    else asm volatile("global_store_dwordx4 %0, %1, %2\n\ts_nop 1" :: "v"(v), "v"(d), "s"(sb) : "memory");
}

typedef __bf16 bf2_t __attribute__((ext_vector_type(2)));
__device__ __forceinline__ unsigned cvtpk(float lo, float hi) {
    f32x2 v = {lo, hi};
    return __builtin_bit_cast(unsigned, __builtin_convertvector(v, bf2_t));
}
__device__ __forceinline__ float bflo(unsigned u) { return __uint_as_float(u << 16); }
__device__ __forceinline__ float bfhi(unsigned u) { return __uint_as_float(u & 0xffff0000u); }
__device__ __forceinline__ bf16x8 ldsr128(const char* p) { return *(const bf16x8*)p; }
__device__ __forceinline__ s16x4 ldsr64(const char* p) { return *(const s16x4*)p; }
__device__ __forceinline__ s16x4 ldstr(const char* p) {
    return __builtin_bit_cast(s16x4, __builtin_amdgcn_ds_read_tr16_b64_v4i16((__attribute__((address_space(3))) s16x4*)p));
}
__device__ __forceinline__ bf16x8 ldstr2(const char* p, int d) { return __builtin_shufflevector(ldstr(p), ldstr(p + d), 0, 1, 2, 3, 4, 5, 6, 7); }
#define MFMA32(a, b, c) __builtin_amdgcn_mfma_f32_32x32x16_bf16((a), (b), (c), 0, 0, 0)
#define MFMA16(a, b, c) __builtin_amdgcn_mfma_f32_16x16x32_bf16((a), (b), (c), 0, 0, 0)
#define MFMA4(a, b, c) __builtin_amdgcn_mfma_f32_4x4x4bf16_1k((a), (b), (c), 0, 0, 0)
#define MFMA2B(a, b, c) __builtin_amdgcn_mfma_f32_32x32x4bf16_1k((a), (b), (c), 0, 0, 0)
#define CAT16(a, b) __builtin_shufflevector(a, b, 0,1,2,3,4,5,6,7,8,9,10,11,12,13,14,15,16,17,18,19,20,21,22,23,24,25,26,27,28,29,30,31)

__device__ __forceinline__ float xsum32(float a) {
    auto rr = __builtin_amdgcn_permlane32_swap(__float_as_uint(a), __float_as_uint(a), false, false);
    return __uint_as_float(rr[0]) + __uint_as_float(rr[1]);
}
__device__ __forceinline__ float xmax32(float a) {
    auto rr = __builtin_amdgcn_permlane32_swap(__float_as_uint(a), __float_as_uint(a), false, false);
    return fmaxf(__uint_as_float(rr[0]), __uint_as_float(rr[1]));
}
__device__ __forceinline__ float xsum16_32(float x) {
    auto s = __builtin_amdgcn_permlane16_swap(__float_as_uint(x), __float_as_uint(x), false, false);
    return xsum32(__uint_as_float(s[0]) + __uint_as_float(s[1]));
}
__device__ __forceinline__ f32x2 ln_finish(float s1, float s2) {
    const float mean = s1 * (1.f / 640.f);
    const float var = fmaxf(s2 * (1.f / 640.f) - mean * mean, 0.f);
    const float rstd = __builtin_amdgcn_rsqf(var + LN_EPS);
    return f32x2{rstd, -mean * rstd};
}

__device__ __forceinline__ void signal_flag(char* flag, u64 tag) {
    asm volatile("" : "+s"(tag), "+s"(flag));
    unsigned zero = 0;
    asm volatile("" : "+v"(zero));
    asm volatile("s_waitcnt vmcnt(0)\n\tglobal_store_dwordx2 %0, %1, %2 sc1" :: "v"(zero), "v"(tag), "s"(flag) : "memory");
}
#define POLL_PRE 8
#define POLL_SLEEP 2
__device__ __forceinline__ void wait_flags_at(const char* base, unsigned off, bool use, u64 tag) {
    const gu64* f = (const gu64*)(base + off);
    bool ok = !use;
#pragma unroll 1
    for (unsigned spins = 0; spins < 100000u; ++spins) {
        if (!ok) ok = __hip_atomic_load(f, __ATOMIC_RELAXED, __HIP_MEMORY_SCOPE_AGENT) == tag;
        if (__all(ok)) break;
        __builtin_amdgcn_s_sleep(POLL_SLEEP);
    }
    asm volatile("" ::: "memory");
}
__device__ __forceinline__ void wait_flags(const char* group, int n, u64 tag) {
    const int lane = TID() & 63;
    __builtin_amdgcn_s_sleep(POLL_PRE);
    wait_flags_at(group, lane * 64, lane < n, tag);
}

template <int NST, int NS, int PD, int D, bool PRE = true, class Issue>
__device__ __forceinline__ void ring_loader(Issue issue) {
    static_assert(PD <= NS - 2 && (PD - 1) * D <= 63 && PD <= NST, "ring geometry");
    if (PRE) {
#pragma unroll 1
        for (int s = 0; s < PD; ++s) issue(s, s);
    }
    int slot = PD;
#pragma unroll 1
    for (int s = 0; s < NST - PD; ++s) {
        WAIT_V((PD - 1) * D);
        BAR();
        issue(s + PD, slot);
        slot = slot + 1 == NS ? 0 : slot + 1;
    }
#pragma unroll
    for (int r = PD - 1; r >= 0; --r) {
        WAIT_V(r * D);
        BAR();
    }
}
template <int N> __device__ __forceinline__ void idle_barriers() {
#pragma unroll 1
    for (int i = 0; i < N; ++i) BAR();
}
#define PDX 3
#define PDXP 4

constexpr int QKV_NST = 20;

__device__ __forceinline__ void ln_issue(u32x2 (&p)[8], const char* stats, int tok0) {
#pragma unroll
    for (int c = 0; c < 8; ++c) p[c] = gload2<true>(stats + ((size_t)c * S_ + tok0) * 8, (TID() & 63) * 8);
}
template <bool SQ = false>
__device__ __forceinline__ f32x2 ln_collect(u32x2 (&p)[8]) {
    asm volatile("s_waitcnt vmcnt(0)" : "+v"(p[0]), "+v"(p[1]), "+v"(p[2]), "+v"(p[3]), "+v"(p[4]), "+v"(p[5]), "+v"(p[6]), "+v"(p[7]) :: "memory");
    f32x2 a = __builtin_bit_cast(f32x2, p[0]);
#pragma unroll
    for (int c = 1; c < 8; ++c) a += __builtin_bit_cast(f32x2, p[c]);
    if (SQ) {
        const float mean = a[0] * (1.f / 640.f);
        const float var = fmaxf(a[1] * (1.f / 640.f) - mean * mean, 0.f);
        return f32x2{__builtin_amdgcn_rcpf(var + LN_EPS), -mean};
    }
    return ln_finish(a[0], a[1]);
}
__device__ __forceinline__ void split_halves(f32x2 v, f32x2& lo, f32x2& hi) {
#pragma unroll
    for (int e = 0; e < 2; ++e) {
        auto rr = __builtin_amdgcn_permlane32_swap(__float_as_uint(v[e]), __float_as_uint(v[e]), false, false);
        lo[e] = __uint_as_float(rr[0]); hi[e] = __uint_as_float(rr[1]);
    }
}

template <bool FIRST, int WS0, bool ISV>
__device__ __forceinline__ void qkv_compute(char* smem, const char* stats, char* Qb, char* KVb, char* sig, u64 tag, int tm, int tn, int w, int L) {
    const int lane = TID() & 63;
    const int wm = w & 1, wn = w >> 1;
    const int r32 = lane & 31, hh = lane >> 5;
    int xo[2][2], woff[2];
#pragma unroll
    for (int ks = 0; ks < 2; ++ks)
#pragma unroll
        for (int j = 0; j < 2; ++j) {
            const int tt = wm * 2 + (j ^ wn);
            xo[ks][j] = FIRST ? (tt * 32 + r32) * 128 + (((ks * 2 + hh) ^ ((r32 >> 1) & 7)) * 16) : tt * 2048 + ks * 1024 + lane * 16;
        }
#pragma unroll
    for (int i = 0; i < 2; ++i) woff[i] = WR_OFF + hh * 2048 + (wn * 2 + (i ^ wm)) * 256 + ((lane >> 2) & 3) * 64 + ((lane >> 4) & 1) * 32 + (lane & 3) * 8;
    f32x16 acc[2][2];
#pragma unroll
    for (int i = 0; i < 2; ++i)
#pragma unroll
        for (int j = 0; j < 2; ++j)
#pragma unroll
            for (int r = 0; r < 16; ++r) acc[i][j][r] = 0.f;
    bf16x8 xf[2][2], wf[2][2];
    u32x2 part[8];
    f32x4 s1 = {0.f, 0.f, 0.f, 0.f}, s2 = s1, cs = s1;
    s16x4 ones = {0x3f80, 0x3f80, 0x3f80, 0x3f80};
    asm volatile("" : "+v"(acc[0][0]), "+v"(acc[0][1]), "+v"(acc[1][0]), "+v"(acc[1][1]), "+v"(xo[0][0]), "+v"(xo[0][1]), "+v"(woff[0]), "+v"(woff[1]));
    if (FIRST) asm volatile("" : "+v"(s1), "+v"(s2), "+v"(cs), "+v"(ones), "+v"(xo[1][0]), "+v"(xo[1][1]));
    BAR();
    STAMP(0, 1);
#define XADDR(ks, j, key) (FIRST ? smem + (xo[ks][j] ^ (key)) : smem + (key) + xo[0][j] + (ks) * 1024)
#pragma unroll
    for (int ks = 0; ks < 2; ++ks) {
#pragma unroll
        for (int j = 0; j < 2; ++j) xf[ks][j] = ldsr128(XADDR(ks, j, 0));
#pragma unroll
        for (int i = 0; i < 2; ++i) wf[ks][i] = ldstr2(smem + WS0 * W_SLOT + woff[i] + ks * 4096, 1024);
    }
    if (!FIRST) ln_issue(part, stats, tm * 128 + wm * 64);
    LGKM0();
    int xk = FIRST ? 64 : 8192, ws = ((WS0 + 1) & (W_NS - 1)) * W_SLOT;
    int xc = 0, wa0 = WS0 * W_SLOT + woff[0], wa1 = WS0 * W_SLOT + woff[1];
#define QM(ks, i, j) acc[i][j] = ISV ? MFMA32(xf[ks][j], wf[ks][i], acc[i][j]) : MFMA32(wf[ks][i], xf[ks][j], acc[i][j]); SB()
#define QW(ks, i) wf[ks][i] = ldstr2(smem + (i ? wa1 : wa0) + ks * 4096, 1024)
#define QX(ks, j) xf[ks][j] = ldsr128(XADDR(ks, j, xc))
#define LO4(v) __builtin_shufflevector(v, v, 0, 1, 2, 3)
#define HI4(v) __builtin_shufflevector(v, v, 4, 5, 6, 7)
#define QS0(ks) if (FIRST) { s1 = MFMA4(ones, LO4(xf[ks][0]), s1); s2 = MFMA4(LO4(xf[ks][0]), LO4(xf[ks][0]), s2); SB(); }
#define QS1(ks) if (FIRST) { cs = MFMA4(ones, LO4(wf[ks][0]), cs); s1 = MFMA4(ones, HI4(xf[ks][0]), s1); SB(); }
#define QS2(ks) if (FIRST) { s2 = MFMA4(HI4(xf[ks][0]), HI4(xf[ks][0]), s2); cs = MFMA4(ones, HI4(wf[ks][0]), cs); SB(); }
#pragma unroll 1
    for (int s = 0; s < QKV_NST; ++s) {
        QM(0, 1, 1); QW(1, 0); QX(1, 0); SB();
        xc = xk; wa0 = ws + woff[0]; wa1 = ws + woff[1];
        asm volatile("" : "+v"(wa0), "+v"(wa1));
        SB();
        QS0(0)
        QM(0, 1, 0); BAR(); SB();
        QS1(0)
        QM(0, 0, 1); QW(0, 1); SB();
        QS2(0)
        QM(0, 0, 0); QX(0, 1); SB();
        QM(1, 1, 1); QW(0, 0); QX(0, 0); SB();
        QS0(1)
        QM(1, 1, 0);
        if (FIRST) xk = (xk & 64) ? (xk == 2 * 16384 + 64 ? 0 : xk + 16384 - 64) : xk + 64;
        else xk = xk + 8192 == 6 * 8192 ? 0 : xk + 8192;
        ws = ws + W_SLOT == W_NS * W_SLOT ? 0 : ws + W_SLOT;
        asm volatile("" : "+s"(xk), "+s"(ws));
        SB();
        QS1(1)
        QM(1, 0, 1); QW(1, 1); SB();
        QS2(1)
        QM(1, 0, 0); QX(1, 1); SB();
    }
#undef QW
#undef QX
#undef QS0
#undef QS1
#undef QS2
#undef QM
#undef XADDR
    STAMP(0, 2);
    f32x2 stl;
    if (FIRST) {
        const float q2 = (lane & 2) ? ((lane & 1) ? s2[3] : s2[2]) : ((lane & 1) ? s2[1] : s2[0]);
        const f32x2 st = ln_finish(xsum32(s1[0]), xsum32(q2));
        const float c = xsum32(cs[0]);
        if (lane < 32) {
            *(f32x2*)(smem + CS_OFF + ((wm * 2 + wn) * 32 + lane) * 8) = st;
            *(float*)(smem + CQ_OFF + ((wn * 2 + wm) * 32 + lane) * 4) = c;
        }
        BAR_L();
        stl = *(const f32x2*)(smem + CS_OFF + (wm * 64 + lane) * 8);
    } else {
        stl = ln_collect(part);
    }
    const float* csl = (const float*)(smem + CQ_OFF);
    const int tile0 = tm * 4 + wm * 2;
    if (!ISV) {
        const bool isq = tn < 5;
        f32x2 lo, hi, st[2];
        split_halves(stl * (isq ? QSCALE : 1.f), lo, hi);
        st[0] = wn ? hi : lo; st[1] = wn ? lo : hi;
        const int tstride = isq ? 40960 : KV_REC;
        char* ob = (isq ? Qb : KVb) + (size_t)tile0 * tstride + ((isq ? tn : tn - 5) * 16 + wn * 8) * 512;
#pragma unroll
        for (int q = 0; q < 4; ++q) {
            const int qq = q ^ (wm * 2);
            f32x4 c4[2];
#pragma unroll
            for (int e = 0; e < 2; ++e) c4[e] = *(const f32x4*)(csl + wn * 64 + qq * 16 + e * 8 + hh * 4);
#pragma unroll
            for (int j = 0; j < 2; ++j) {
                const f32x16& a = acc[q >> 1][j];
                unsigned pk[2][2];
#pragma unroll
                for (int e = 0; e < 2; ++e) {
                    const int r = (q & 1) * 8 + e * 4;
                    const f32x2 v0 = f32x2{a[r], a[r + 1]} * st[j][0] + f32x2{c4[e][0], c4[e][1]} * st[j][1];
                    const f32x2 v1 = f32x2{a[r + 2], a[r + 3]} * st[j][0] + f32x2{c4[e][2], c4[e][3]} * st[j][1];
                    pk[e][0] = cvtpk(v0[0], v0[1]); pk[e][1] = cvtpk(v1[0], v1[1]);
                }
                auto r0 = __builtin_amdgcn_permlane32_swap(pk[0][0], pk[1][0], false, false);
                auto r1 = __builtin_amdgcn_permlane32_swap(pk[0][1], pk[1][1], false, false);
                gstore4<true>(ob + (j ^ wn) * tstride + qq * 1024, lane * 16, u32x4{r0[0], r1[0], r0[1], r1[1]});
            }
        }
    } else {
        const char* sl = smem + S2_OFF + w * 512;
        *(f32x2*)(smem + S2_OFF + w * 512 + lane * 8) = stl;
        char* ob = KVb + (size_t)tile0 * KV_REC + KV_V + ((tn - 10) * 128 + wn * 64) * 16;
        const unsigned ov = hh * 10240 + r32 * 16;
        float c1[2];
#pragma unroll
        for (int i = 0; i < 2; ++i) c1[i] = csl[(wn * 2 + (i ^ wm)) * 32 + r32];
#pragma unroll
        for (int j = 0; j < 2; ++j)
#pragma unroll
            for (int s2_ = 0; s2_ < 2; ++s2_) {
                f32x4 sa[2], sb[2];
#pragma unroll
                for (int q = 0; q < 2; ++q) {
                    const char* sp = sl + ((j ^ wn) * 32 + (s2_ * 2 + q) * 8 + hh * 4) * 8;
                    sa[q] = *(const f32x4*)sp; sb[q] = *(const f32x4*)(sp + 16);
                }
#pragma unroll
                for (int i = 0; i < 2; ++i) {
                    float v[8];
#pragma unroll
                    for (int q = 0; q < 2; ++q) {
                        const int r = (s2_ * 2 + q) * 4;
                        v[q * 4 + 0] = acc[i][j][r + 0] * sa[q][0] + c1[i] * sa[q][1];
                        v[q * 4 + 1] = acc[i][j][r + 1] * sa[q][2] + c1[i] * sa[q][3];
                        v[q * 4 + 2] = acc[i][j][r + 2] * sb[q][0] + c1[i] * sb[q][1];
                        v[q * 4 + 3] = acc[i][j][r + 3] * sb[q][2] + c1[i] * sb[q][3];
                    }
                    gstore4<true>(ob + (j ^ wn) * KV_REC + s2_ * 20480 + (i ^ wm) * 512, ov,
                                  u32x4{cvtpk(v[0], v[1]), cvtpk(v[2], v[3]), cvtpk(v[4], v[5]), cvtpk(v[6], v[7])});
                }
            }
        if (lane < 16) {
            unsigned one = ONES2;
            asm volatile("" : "+v"(one));
            gstore4<true>(KVb + (size_t)tile0 * KV_REC + KV_ONES, (lane >> 3) * KV_REC + (lane & 7) * 16, u32x4{one, one, one, one});
        }
    }
    STAMP(0, 4);
    signal_flag(sig + (size_t)(((tm >> 1) * 32 + tn * 2 + wn) * 4 + (tm & 1) * 2 + wm) * 64, tag);
    STAMP(0, 3);
    BAR_L();
}

__device__ __forceinline__ bool qkv_tile(int& tm, int& tn) {
    const int b = BID(), xcd = b & 7, j = b >> 3;
    const int id = (xcd & 3) * 30 + j;
    tm = (xcd >> 2) * 8 + (id & 7); tn = id >> 3;
    return j < 30;
}
struct QkvW {
    const char* wsb; unsigned wv;
    __device__ __forceinline__ QkvW(const char* W, int tn) {
        const int lane = TID() & 63;
        wsb = W + tn * 256;
        wv = ((lane >> 2) & 3) * 3840 + (lane >> 4) * 64 + (lane & 3) * 16;
    }
    __device__ __forceinline__ void operator()(int s, unsigned dst) const {
        constexpr unsigned dv = 4 * 3840 - 1024;
        glds4<false>(wsb + (size_t)s * (32 * 3840), wv, wv + dv, wv + 2 * dv, wv + 3 * dv, dst);
        glds4<false>(wsb + (size_t)s * (32 * 3840) + 16 * 3840, wv, wv + dv, wv + 2 * dv, wv + 3 * dv, dst + 4096);
    }
};

struct Params {
    const char *x0, *wqkv, *wo, *wg, *wu, *wd;
    char *ws, *out;
};
__device__ __forceinline__ void acc8(float (&a)[8], const u32x4& v) {
#pragma unroll
    for (int j = 0; j < 4; ++j) { a[2 * j] += bflo(v[j]); a[2 * j + 1] += bfhi(v[j]); }
}
__device__ __forceinline__ void pre_csg(char* smem, const Params& p, u64 tag) {
    const int t = TID(), b = BID();
    const int sidx = (b & 7) * 2 + ((b >> 3) - 30);
    const int lane = t & 63, w = __builtin_amdgcn_readfirstlane(t >> 6), cw = w % 3, half = w / 3;
    const char* src = (sidx < 8 ? p.wg : p.wu) + (sidx & 7) * 384 + cw * 128 + (lane & 7) * 16 + (size_t)(half * 320 + (lane >> 3)) * 3072;
    float a[8] = {0, 0, 0, 0, 0, 0, 0, 0};
#pragma unroll 1
    for (int o = 0; o < 2; ++o) {
        u32x4 v[20];
#pragma unroll
        for (int i = 0; i < 20; ++i) v[i] = *(const u32x4*)(src + (size_t)(o * 20 + i) * 8 * 3072);
#pragma unroll
        for (int i = 0; i < 20; ++i) acc8(a, v[i]);
    }
#pragma unroll
    for (int j = 0; j < 8; ++j) {
        a[j] += __int_as_float(__builtin_amdgcn_ds_swizzle(__float_as_int(a[j]), 0x201f));
        a[j] = xsum16_32(a[j]);
    }
    volatile float* xch = (volatile float*)(smem + S2_OFF) + cw * 64;
    volatile u64* lf = (volatile u64*)(smem + S2_OFF + 1024) + cw;
    if (half) {
        if (lane < 8) {
#pragma unroll
            for (int j = 0; j < 8; ++j) xch[lane * 8 + j] = a[j];
        }
        asm volatile("s_waitcnt lgkmcnt(0)" ::: "memory");
        if (lane == 0) *lf = tag;
        return;
    }
#pragma unroll 1
    for (unsigned spins = 0; spins < 1000000u && *lf != tag; ++spins) __builtin_amdgcn_s_sleep(2);
    if (lane < 8) {
#pragma unroll
        for (int j = 0; j < 8; ++j) a[j] += xch[lane * 8 + j];
        const int c = (sidx & 7) * 192 + cw * 64 + lane * 8;
        const unsigned o = ((c >> 4) * 32 + (sidx < 8 ? 0 : 16) + (c & 15)) * 4;
        gstore4<true>(p.ws + WS_CSG, o, u32x4{__float_as_uint(a[0]), __float_as_uint(a[1]), __float_as_uint(a[2]), __float_as_uint(a[3])});
        gstore4<true>(p.ws + WS_CSG + 16, o, u32x4{__float_as_uint(a[4]), __float_as_uint(a[5]), __float_as_uint(a[6]), __float_as_uint(a[7])});
    }
    signal_flag(p.ws + WS_FLAGS + FL_U + (size_t)(sidx * 3 + cw) * 8, tag);
}

struct XRow {
    const char* xs; unsigned v0, v1, dst;
    __device__ __forceinline__ XRow(const char* x0, int tm, int lw, unsigned lbase) {
        const int lane = TID() & 63, r8 = lane >> 3;
        xs = x0 + (size_t)(tm * 128 + lw * 64) * 1280;
        v0 = r8 * 1280 + (((lane & 7) ^ (r8 >> 1)) * 16);
        v1 = (r8 + 8) * 1280 - 1024 + (((lane & 7) ^ (4 + (r8 >> 1))) * 16);
        dst = lbase + lw * 8192;
    }
    __device__ __forceinline__ void operator()(int d, int slot) const {
        constexpr unsigned d2 = 16 * 1280 - 2048;
        glds4<false>(xs + d * 128, v0, v1, v0 + d2, v1 + d2, dst + slot * 16384);
        glds4<false>(xs + d * 128 + 32 * 1280, v0, v1, v0 + d2, v1 + d2, dst + slot * 16384 + 4096);
    }
};
__device__ __forceinline__ void startup(char* smem, const Params& p) {
    int tm, tn;
    const bool has = qkv_tile(tm, tn);
    const int w = __builtin_amdgcn_readfirstlane(TID() >> 6);
    const unsigned lbase = (unsigned)(uintptr_t)(lds_t)smem;
    if (w == 8) {
        if (has) {
            const QkvW qk(p.wqkv, tn);
#pragma unroll 1
            for (int s = 0; s < 3; ++s) qk(s, lbase + WR_OFF + s * W_SLOT);
        }
    } else if (w == 7) {
        const char* pc;
        int len;
        asm volatile("s_getpc_b64 %0\n.Lk_code_lo:\n\ts_mov_b32 %1, .Lk_code_hi-.Lk_code_lo" : "=s"(pc), "=s"(len));
        int o = ((BID() >> 3) * 16 + (TID() & 15)) * 128;
        o = o < len ? o : 0;
        asm volatile("s_mov_b32 m0, %2\n\ts_nop 0\n\tglobal_load_lds_dword %0, %1" :: "v"(o), "s"(pc), "s"(lbase + DUM_OFF) : "memory");
    } else if ((w == 4 || w == 5) && has) {
        const XRow xr(p.x0, tm, w - 4, lbase);
        xr(0, 0); xr(1, 1);
    }
}

template <int L, int WS0>
__device__ __forceinline__ void qkv_phase(char* smem, const Params& p, const char* X, const char* stats, char* Qb, char* KVb, const char* dep,
                                          char* sig, u64 tag) {
    int tm, tn;
    const bool has = qkv_tile(tm, tn);
    const int lane = TID() & 63;
    const int w = __builtin_amdgcn_readfirstlane(TID() >> 6);
    const unsigned lbase = (unsigned)(uintptr_t)(lds_t)smem;
    STAMP(0, 0);
    if (!has) {
        if (L == 0 && w < 6) pre_csg(smem, p, tag);
        return;
    }
    if (w >= 6) {
        idle_barriers<QKV_NST + 2>();
        if (L == 0) BAR();
    } else if (w < 4) {
        if (tn >= 10) qkv_compute<L == 0, WS0, true>(smem, stats, Qb, KVb, sig, tag, tm, tn, w, L);
        else qkv_compute<L == 0, WS0, false>(smem, stats, Qb, KVb, sig, tag, tm, tn, w, L);
    } else if (L) {
        const int lw = w - 4;
        const char* xs = X + (size_t)(tm * 4 + lw * 2) * 40 * 1024;
        const unsigned xv = lane * 16, xv2 = xv + 40 * 1024 - 2048;
        wait_flags(dep + (size_t)tm * FL_GROUP + lw * 2048, 32, tag);
        STAMPT(0, 5, 256);
        ring_loader<QKV_NST, X_NS, PDX, 4>([&](int s, int slot) { glds4<true>(xs + s * 2048, xv, xv, xv2, xv2, lbase + slot * X_SLOT + lw * 4096); });
        BAR(); BAR();
    } else {
        const XRow issue(p.x0, tm, w - 4, lbase);
        int slot = 2;
#pragma unroll 1
        for (int d = 0; d < 8; ++d) {
            WAIT_V(8);
            BAR();
            BAR();
            issue(d + 2, slot);
            slot = slot == 2 ? 0 : slot + 1;
        }
        WAIT_V(8);
        BAR(); BAR();
        WAIT_V(0);
        BAR(); BAR();
        BAR(); BAR(); BAR();
    }
}

constexpr int AT_SLOT = 8 * 3072, AT_NS = 3, AT_NCH = 8;

__device__ __forceinline__ float rowmax32(const f32x32& s) {
    float a = fmaxf(fmaxf(s[0], s[1]), s[2]), b = fmaxf(fmaxf(s[3], s[4]), s[5]);
#pragma unroll
    for (int r = 6; r < 30; r += 4) { a = fmaxf(fmaxf(a, s[r]), s[r + 1]); b = fmaxf(fmaxf(b, s[r + 2]), s[r + 3]); }
    a = fmaxf(fmaxf(a, s[30]), s[31]);
    return xmax32(fmaxf(a, b));
}

__device__ __forceinline__ void attn_tile(int& head, int& qblk) {
    const int b = BID(), xcd = b & 7, j = b >> 3;
    head = xcd * 4 + (j & 3); qblk = j >> 2;
}
__device__ __forceinline__ void kv_piece_offs(int head, unsigned (&v)[3]) {
    const int lane = TID() & 63;
    const int c0 = (head * 5) >> 1, odd = head & 1;
    v[0] = (c0 + odd) * 512 + lane * 16;
#pragma unroll
    for (int i = 1; i < 3; ++i) {
        const int idx = i * 64 + lane - 96, sh = idx / 24, row = idx - sh * 24;
        unsigned o = row < 20 ? KV_V + sh * 10240 + (head * 20 + row) * 16 : KV_ONES + (row - 20) * 16;
        if (idx < 0) o = (odd ? c0 : c0 + 2) * 512 + lane * 16;
        v[i] = o - i * 1024;
    }
}

__device__ __forceinline__ void attn_phase(char* smem, const char* Qb, const char* KVb, char* O, char* sig, u64 tag, int L) {
    const int tid = TID(), lane = tid & 63;
    const int w = __builtin_amdgcn_readfirstlane(tid >> 6);
    int head, qblk;
    attn_tile(head, qblk);
    const int r32 = lane & 31, hh = lane >> 5;
    const int qtile = qblk * 8 + w;
    const int c0 = (head * 5) >> 1, odd = head & 1;
    STAMP(1, 0);
    const unsigned lbase = (unsigned)(uintptr_t)(lds_t)smem;
    unsigned pv[3];
    kv_piece_offs(head, pv);
    const unsigned qhv = (odd ? c0 : c0 + 2) * 512 + r32 * 16 + odd * 8;
    const int koff = lane * 16, khoff = hh * 3072 + 1024 + r32 * 16 + odd * 8;
    const int vrow = r32 <= 20 ? r32 : (r32 < 28 ? 20 : 16);
    const int voff = 1536 + hh * 384 + vrow * 16;
    asm volatile("" : "+v"(pv[0]), "+v"(pv[1]), "+v"(pv[2]));
    BAR();
    STAMP(1, 5);
    glds3<true>(KVb + (size_t)w * KV_REC, pv[0], pv[1], pv[2], lbase + w * 3072);
    bf16x8 q0;
    s16x4 q4;
    {
        const char* qp = Qb + (size_t)qtile * 40960;
        asm volatile("global_load_dwordx4 %0, %1, %2 sc1" : "=v"(q0) : "v"(pv[0]), "s"(qp) : "memory");
        asm volatile("global_load_dwordx2 %0, %1, %2 sc1" : "=v"(q4) : "v"(qhv), "s"(qp) : "memory");
    }
    f32x16 zero;
#pragma unroll
    for (int r = 0; r < 16; ++r) zero[r] = 0.f;
    f32x16 o = zero, m16;
    f32x32 SA, SBn;
    asm volatile("s_waitcnt vmcnt(0)" : "+v"(q0), "+v"(q4) :: "memory");
    BAR();
    STAMP(1, 1);
    bf16x8 ka, kb, va0, va1, vb0, vb1;
    s16x4 kh;
    u32x4 pa0, pa1, pb0, pb1 = {0u, 0u, 0u, 0u};
    {
        ka = ldsr128(smem + koff); kb = ldsr128(smem + 3072 + koff); kh = ldsr64(smem + khoff);
        f32x16 n0 = MFMA32(ka, q0, zero), n1 = MFMA32(kb, q0, zero);
        SA = MFMA2B(kh, q4, CAT16(n0, n1));
        const float m = rowmax32(SA);
#pragma unroll
        for (int r = 0; r < 16; ++r) m16[r] = -m;
#pragma unroll
        for (int r = 0; r < 32; ++r) SA[r] -= m;
        ka = ldsr128(smem + 2 * 3072 + koff); kb = ldsr128(smem + 3 * 3072 + koff); kh = ldsr64(smem + 2 * 3072 + khoff);
        va0 = ldsr128(smem + voff); va1 = ldsr128(smem + voff + 768); vb0 = ldsr128(smem + 3072 + voff); vb1 = vb0;
    }
    asm volatile("" : "+v"(m16));
    LGKM0();
    SB();
#define EX(x) x = __builtin_amdgcn_exp2f(x)
#define BF8(x) __builtin_bit_cast(bf16x8, x)
#define G4(S, i, P, jj) EX(S[i]); EX(S[i + 1]); SB(); EX(S[i + 2]); P[jj] = cvtpk(S[i], S[i + 1]); SB(); EX(S[i + 3]); SB();
#define STEP2(S, SN, V1, V2, V3, K4, K5, KH4) do { \
        f32x16 n0, n1; \
        o = MFMA32(vb1, BF8(pb1), o); SB(); vb1 = ldsr128((V1) + 768); SB(); \
        G4(S, 0, pa0, 0) \
        n0 = MFMA32(ka, q0, m16); SB(); ka = ldsr128(K4); pa0[1] = cvtpk(S[2], S[3]); SB(); \
        G4(S, 4, pa0, 2) \
        n1 = MFMA32(kb, q0, m16); SB(); kb = ldsr128(K5); pa0[3] = cvtpk(S[6], S[7]); SB(); \
        G4(S, 8, pa1, 0) \
        o = MFMA32(va0, BF8(pa0), o); SB(); va0 = ldsr128(V2); pa1[1] = cvtpk(S[10], S[11]); SB(); \
        G4(S, 12, pa1, 2) \
        SN = MFMA2B(kh, q4, CAT16(n0, n1)); SB(); kh = ldsr64(KH4); pa1[3] = cvtpk(S[14], S[15]); SB(); \
        G4(S, 16, pb0, 0) \
        pb0[1] = cvtpk(S[18], S[19]); SB(); \
        G4(S, 20, pb0, 2) \
        o = MFMA32(va1, BF8(pa1), o); SB(); va1 = ldsr128((V2) + 768); pb0[3] = cvtpk(S[22], S[23]); SB(); \
        G4(S, 24, pb1, 0) \
        pb1[1] = cvtpk(S[26], S[27]); SB(); \
        o = MFMA32(vb0, BF8(pb0), o); SB(); vb0 = ldsr128(V3); SB(); \
        G4(S, 28, pb1, 2) \
        pb1[3] = cvtpk(S[30], S[31]); SB(); \
    } while (0)
    if (w >= 4) __builtin_amdgcn_s_setprio(APRIO);
    int slot = 0;
#pragma unroll 1
    for (int c = 0; c < AT_NCH; ++c) {
        const int nslot = slot + 1 == AT_NS ? 0 : slot + 1;
        const char* cv = smem + slot * AT_SLOT + voff;   const char* ck = smem + slot * AT_SLOT + koff;   const char* ch = smem + slot * AT_SLOT + khoff;
        const char* nv = smem + nslot * AT_SLOT + voff;  const char* nk = smem + nslot * AT_SLOT + koff;  const char* nh = smem + nslot * AT_SLOT + khoff;
        STEP2(SA, SBn, cv + 1 * 3072, cv + 2 * 3072, cv + 3 * 3072, ck + 4 * 3072, ck + 5 * 3072, ch + 4 * 3072);
        STEP2(SBn, SA, cv + 3 * 3072, cv + 4 * 3072, cv + 5 * 3072, ck + 6 * 3072, ck + 7 * 3072, ch + 6 * 3072);
        BAR();
        SB();
        STEP2(SA, SBn, cv + 5 * 3072, cv + 6 * 3072, cv + 7 * 3072, nk, nk + 3072, nh);
        STEP2(SBn, SA, cv + 7 * 3072, nv, nv + 3072, nk + 2 * 3072, nk + 3 * 3072, nh + 2 * 3072);
        slot = nslot;
    }
    o = MFMA32(vb1, BF8(pb1), o);
    __builtin_amdgcn_s_setprio(0);
    STAMP(1, 2);
    STAMPT(1, 6, 448);
    float l;
    {
        auto rr = __builtin_amdgcn_permlane32_swap(__float_as_uint(o[8]), __float_as_uint(o[8]), false, false);
        l = __uint_as_float(rr[1]);
    }
    float big = fmaxf(fmaxf(fabsf(o[0]), fabsf(o[1])), fabsf(o[2]));
#pragma unroll
    for (int r = 3; r < 9; r += 2) big = fmaxf(fmaxf(big, fabsf(o[r])), fabsf(o[r + 1]));
    float sum = ((o[0] + o[1]) + (o[2] + o[3])) + ((o[4] + o[5]) + (o[6] + o[7])) + o[8];
    if (!hh) { big = fmaxf(fmaxf(big, fabsf(o[9])), fmaxf(fabsf(o[10]), fabsf(o[11]))); sum += (o[9] + o[10]) + o[11]; }
    const bool bad = !(l < 1.8446744073709552e19f) || !(big < 1.0e37f) || !(sum == sum);
    if (__builtin_expect(__any(bad), 0)) {
        float m = -INFINITY;
        o = zero;
        const unsigned gk = (c0 + odd) * 512 + lane * 16, gh = hh * KV_REC + qhv;
        const unsigned gv0 = r32 < 20 ? KV_V + hh * 10240 + (head * 20 + r32) * 16 : KV_ONES, gv1 = r32 < 20 ? gv0 + 20480 : KV_ONES;
#pragma unroll 1
        for (int t = 0; t < 64; t += 2) {
            const char* tp = KVb + (size_t)t * KV_REC;
            bf16x8 fk[2], fv[4];
            s16x4 fh;
            asm volatile("global_load_dwordx4 %0, %7, %11 sc1\n\tglobal_load_dwordx4 %1, %7, %12 sc1\n\tglobal_load_dwordx2 %2, %8, %11 sc1\n\t"
                         "global_load_dwordx4 %3, %9, %11 sc1\n\tglobal_load_dwordx4 %4, %10, %11 sc1\n\t"
                         "global_load_dwordx4 %5, %9, %12 sc1\n\tglobal_load_dwordx4 %6, %10, %12 sc1\n\ts_waitcnt vmcnt(0)"
                         : "=&v"(fk[0]), "=&v"(fk[1]), "=&v"(fh), "=&v"(fv[0]), "=&v"(fv[1]), "=&v"(fv[2]), "=&v"(fv[3])
                         : "v"(gk), "v"(gh), "v"(gv0), "v"(gv1), "s"(tp), "s"(tp + KV_REC) : "memory");
            f32x16 n0 = MFMA32(fk[0], q0, zero), n1 = MFMA32(fk[1], q0, zero);
            f32x32 sc = MFMA2B(fh, q4, CAT16(n0, n1));
            const float mn = fmaxf(m, rowmax32(sc));
            const float alpha = __builtin_amdgcn_exp2f(m - mn);
            m = mn;
#pragma unroll
            for (int r = 0; r < 16; ++r) o[r] *= alpha;
#pragma unroll
            for (int q = 0; q < 4; ++q) {
                u32x4 pp;
#pragma unroll
                for (int e = 0; e < 4; ++e) pp[e] = cvtpk(__builtin_amdgcn_exp2f(sc[q * 8 + 2 * e] - mn), __builtin_amdgcn_exp2f(sc[q * 8 + 2 * e + 1] - mn));
                o = MFMA32(fv[q], BF8(pp), o);
            }
        }
        auto rr = __builtin_amdgcn_permlane32_swap(__float_as_uint(o[8]), __float_as_uint(o[8]), false, false);
        l = __uint_as_float(rr[1]);
    }
    const float inv = __builtin_amdgcn_rcpf(l);
    char* ob = O + (size_t)qtile * 40 * 1024;
#pragma unroll
    for (int g = 0; g < 3; ++g) {
        if (g < 2 || !hh) {
            const int col = head * 20 + g * 8 + hh * 4;
            gstore2<true>(ob, (col >> 3) * 512 + (col & 4) * 2 + r32 * 16,
                           u32x2{cvtpk(o[g * 4] * inv, o[g * 4 + 1] * inv), cvtpk(o[g * 4 + 2] * inv, o[g * 4 + 3] * inv)});
        }
    }
    STAMP(1, 4);
    signal_flag(sig + (size_t)(qtile >> 1) * FL_GROUP + ((qtile & 1) * 32 + head) * 64, tag);
    STAMP(1, 3);
    BAR_L();
}
#undef EX
#undef G4
#undef STEP2

constexpr int GU_NST = 20;
__device__ __forceinline__ void gu_tile(int& tm, int& tn) {
    const int b = BID(), xcd = b & 7, j0 = b >> 3;
    tm = (xcd >> 2) * 8 + (j0 & 7); tn = (xcd & 3) * 4 + (j0 >> 3);
}
struct GuW {
    const char* wb; unsigned lv;
    __device__ __forceinline__ GuW(const char* GT) { wb = GT; lv = (TID() & 63) * 16; }
    __device__ __forceinline__ void operator()(int s, unsigned d) const {
#pragma unroll
        for (int i = 0; i < 3; ++i) glds4<true>(wb + s * 12288 + i * 4096, lv, lv, lv, lv, d + i * 4096);
    }
};
struct GuNat {
    const char *gb, *ub; unsigned gv[3];
    __device__ __forceinline__ GuNat(const char* Wg, const char* Wu, int tn) {
        const int lane = TID() & 63;
#pragma unroll
        for (int i = 0; i < 3; ++i) {
            const int idx = i * 64 + lane, kq = idx / 48, rem = idx - kq * 48, sub = rem >> 4, kr = (rem >> 2) & 3, c = rem & 3;
            gv[i] = (kq * 4 + kr) * 3072 + sub * 64 + c * 16 - i * 1024;
        }
        gb = Wg + tn * 192; ub = Wu + tn * 192;
    }
    __device__ __forceinline__ void operator()(int s, unsigned d) const {
        const size_t o = (size_t)s * (32 * 3072);
        glds3(gb + o, gv[0], gv[1], gv[2], d);
        glds3(ub + o, gv[0], gv[1], gv[2], d + 6144);
        glds3(gb + o + 16 * 3072, gv[0], gv[1], gv[2], d + 3072);
        glds3(ub + o + 16 * 3072, gv[0], gv[1], gv[2], d + 9216);
    }
};

template <int WS0>
__device__ __forceinline__ void gu_phase(char* smem, const char* X, const char* stats, char* Hh, const char* dep, char* sig, u64 tag,
                                         int L) {
    const int lane = TID() & 63;
    const int w = __builtin_amdgcn_readfirstlane(TID() >> 6);
    int tm, tn;
    gu_tile(tm, tn);
    const unsigned lbase = (unsigned)(uintptr_t)(lds_t)smem;
    STAMP(3, 0);
    if (w >= 6) { idle_barriers<GU_NST + 2>(); return; }
    if (w >= 4) {
        const int lw = w - 4;
        const char* xs = X + (size_t)(tm * 4 + lw * 2) * 40 * 1024;
        const unsigned xv = lane * 16, xv2 = xv + 40 * 1024 - 2048;
        wait_flags(dep + (size_t)tm * FL_GROUP + lw * 2048, 32, tag);
        STAMPT(3, 5, 256);
        ring_loader<GU_NST, X_NS, PDX, 4>([&](int s, int slot) { glds4<true>(xs + s * 2048, xv, xv, xv2, xv2, lbase + slot * X_SLOT + lw * 4096); });
        BAR(); BAR();
        return;
    }
    const int wm = w & 1, wn = w >> 1;
    const int hh = lane >> 5;
    const int xoff = wm * 4096 + lane * 16;
    int woff = WR_OFF + wn * 3072 + lane * 16;
    f32x16 acc[3][2];
#pragma unroll
    for (int i = 0; i < 3; ++i)
#pragma unroll
        for (int j = 0; j < 2; ++j)
#pragma unroll
            for (int r = 0; r < 16; ++r) acc[i][j][r] = 0.f;
    bf16x8 xf[2][2], wf[2][3];
    u32x2 part[8];
    asm volatile("" : "+v"(acc[0][0]), "+v"(acc[0][1]), "+v"(acc[1][0]), "+v"(acc[1][1]), "+v"(acc[2][0]), "+v"(acc[2][1]), "+v"(woff));
    BAR();
    STAMP(3, 1);
#pragma unroll
    for (int ks = 0; ks < 2; ++ks) {
#pragma unroll
        for (int j = 0; j < 2; ++j) xf[ks][j] = ldsr128(smem + xoff + ks * 1024 + j * 2048);
#pragma unroll
        for (int i = 0; i < 3; ++i) wf[ks][i] = ldsr128(smem + WS0 * W_SLOT + woff + ks * 6144 + i * 1024);
    }
    ln_issue(part, stats, tm * 128 + wm * 64);
    LGKM0();
    int xs = X_SLOT, ws = ((WS0 + 1) & (W_NS - 1)) * W_SLOT;
    int xa = xoff, wa = WS0 * W_SLOT + woff;
#define GM(ks, i, j) acc[i][j] = MFMA32(wf[ks][i], xf[ks][j], acc[i][j]); SB()
#define GW(ks, i) wf[ks][i] = ldsr128(smem + wa + ks * 6144 + i * 1024)
#define GX(ks, j) xf[ks][j] = ldsr128(smem + xa + ks * 1024 + j * 2048)
#pragma unroll 1
    for (int s = 0; s < GU_NST; ++s) {
        GM(0, 0, 0); GW(1, 2); GX(1, 1); SB();
        xa = xs + xoff;
        wa = ws + woff;
        asm volatile("" : "+v"(xa), "+v"(wa));
        SB();
        GM(0, 0, 1); BAR(); SB();
        GM(0, 1, 0); GW(0, 0); SB();
        GM(0, 1, 1);
        xs = xs + X_SLOT == X_NS * X_SLOT ? 0 : xs + X_SLOT;
        ws = ws + W_SLOT == W_NS * W_SLOT ? 0 : ws + W_SLOT;
        asm volatile("" : "+s"(xs), "+s"(ws));
        SB();
        GM(0, 2, 0); GW(0, 1); SB();
        GM(0, 2, 1); GX(0, 0); SB();
        GM(1, 0, 0); GW(0, 2); GX(0, 1); SB();
        GM(1, 0, 1);
        GM(1, 1, 0); GW(1, 0); SB();
        GM(1, 1, 1);
        GM(1, 2, 0); GW(1, 1); SB();
        GM(1, 2, 1); GX(1, 0); SB();
    }
#undef GW
#undef GX
#undef GM
    STAMP(3, 2);
    const float* csl = (const float*)(smem + CS_OFF);
    f32x2 st[2];
    {
        const f32x2 t = ln_collect<true>(part);
        split_halves(t, st[0], st[1]);
    }
    char* hb = Hh + (size_t)((tm * 4 + wm * 2) * 96 + tn * 6 + wn * 3) * 1024;
#pragma unroll
    for (int i = 0; i < 3; ++i) {
        const int t = wn * 3 + i;
        f32x4 c4[4];
#pragma unroll
        for (int g = 0; g < 4; ++g) c4[g] = *(const f32x4*)(csl + t * 32 + g * 8 + hh * 4);
#pragma unroll
        for (int j = 0; j < 2; ++j) {
            u32x4 o;
#pragma unroll
            for (int e = 0; e < 4; ++e) {
                const f32x2 cg = {c4[e >> 1][(e & 1) * 2], c4[e >> 1][(e & 1) * 2 + 1]}, cu = {c4[2 + (e >> 1)][(e & 1) * 2], c4[2 + (e >> 1)][(e & 1) * 2 + 1]};
                f32x2 ga = cg * st[j][1] + f32x2{acc[i][j][2 * e], acc[i][j][2 * e + 1]};
                const f32x2 up = (cu * st[j][1] + f32x2{acc[i][j][8 + 2 * e], acc[i][j][9 + 2 * e]}) * st[j][0];
                ga[0] = fmaxf(ga[0], 0.f); ga[1] = fmaxf(ga[1], 0.f);
                const f32x2 hv = ga * up;
                o[e] = cvtpk(hv[0], hv[1]);
            }
            gstore4<true>(hb + (j * 96 + i) * 1024, lane * 16, o);
        }
    }
    STAMP(3, 4);
    signal_flag(sig + (size_t)(tm * 2 + wm) * FL_GROUP + (tn * 2 + wn) * 64, tag);
    STAMP(3, 3);
    BAR_L();
}

__device__ __forceinline__ void proj_tile(int& tm, int& tn) {
    const int b = BID(), xcd = b & 7, j0 = b >> 3;
    tm = xcd * 4 + (j0 & 3); tn = j0 >> 2;
}
struct ProjW {
    const char* wb; unsigned lv;
    __device__ __forceinline__ ProjW(const char* WT) { wb = WT; lv = (TID() & 63) * 16; }
    __device__ __forceinline__ void operator()(int s, unsigned d) const {
        glds4<true>(wb + s * 10240, lv, lv, lv, lv, d);
        glds4<true>(wb + s * 10240 + 4096, lv, lv, lv, lv, d + 4096);
        glds2<true>(wb + s * 10240 + 8192, lv, lv, d + 8192);
    }
};

template <int KTOT, int WS0>
__device__ __forceinline__ void proj_phase(char* smem, const char* A, const char* Xold, char* Xnew, char* stats, char* out,
                                           const char* dep, char* sig, u64 tag, int L) {
    constexpr int NST = KTOT / 64, KS16 = KTOT / 16, KID = KTOT == 640 ? 2 : 4;
    const int lane = TID() & 63;
    const int w = __builtin_amdgcn_readfirstlane(TID() >> 6);
    int tm, tn;
    proj_tile(tm, tn);
    const unsigned lbase = (unsigned)(uintptr_t)(lds_t)smem;
    STAMP(KID, 0);
    if (w >= 6) { idle_barriers<NST + 2>(); return; }
    if (w >= 4) {
        const int lw = w - 4;
        const char* as = A + (size_t)((tm * 2 + lw) * KS16) * 1024;
        const unsigned xv = lane * 16;
        wait_flags(dep + (size_t)tm * FL_GROUP + (KTOT == 640 ? lw * 2048 : 0), 32, tag);
        STAMPT(KID, 5, 256);
        ring_loader<NST, X_NS, PDXP, 4>([&](int s, int slot) { glds4<true>(as + s * 4096, xv, xv, xv, xv, lbase + slot * X_SLOT + lw * 4096); });
        BAR(); BAR();
        return;
    }
    const int g = lane >> 4, c16 = lane & 15;
    const int r32 = (w & 1) * 16 + c16;
    const unsigned tilv = (g >> 1) * 512 + r32 * 16 + (g & 1) * 8;
    const size_t tilb = (size_t)((tm * 2 + (w >> 1)) * 40 + tn * 5) * 1024;
    u32x2 res[5];
    {
        const bool rowm = KTOT == 640 && L == 0;
        const char* rb = rowm ? Xold + ((size_t)(tm * 64 + w * 16) * 640 + tn * 80) * 2 : Xold + tilb;
        const unsigned rv = rowm ? c16 * 1280 + g * 8 : tilv;
        const int rs = rowm ? 32 : 1024;
#pragma unroll
        for (int i = 0; i < 5; ++i) res[i] = gload2<true>(rb + i * rs, rv);
    }
    const int aoff = (w >> 1) * 4096 + g * 512 + r32 * 16;
    const int woff = WR_OFF + lane * 16;
    char* sigp = sig + (size_t)(tm >> 1) * FL_GROUP + ((tm & 1) * 32 + tn * 4 + w) * 64;
    char* stp = stats + ((size_t)tn * S_ + tm * 64 + w * 16) * 8;
    char* xnp = Xnew + tilb;
    asm volatile("s_waitcnt vmcnt(0)" : "+v"(res[0]), "+v"(res[1]), "+v"(res[2]), "+v"(res[3]), "+v"(res[4]) :: "memory");
    asm volatile("" : "+s"(sigp), "+s"(stp), "+s"(xnp));
    f32x4 acc[5];
#pragma unroll
    for (int i = 0; i < 5; ++i) acc[i] = f32x4{bflo(res[i][0]), bfhi(res[i][0]), bflo(res[i][1]), bfhi(res[i][1])};
    bf16x8 af[2], wf[2][5];
    asm volatile("" : "+v"(acc[0]), "+v"(acc[1]), "+v"(acc[2]), "+v"(acc[3]), "+v"(acc[4]));
    BAR();
    STAMP(KID, 1);
#pragma unroll
    for (int kk = 0; kk < 2; ++kk) {
        af[kk] = ldsr128(smem + aoff + kk * 2048);
#pragma unroll
        for (int i = 0; i < 5; ++i) wf[kk][i] = ldsr128(smem + WS0 * W_SLOT + woff + (kk * 5 + i) * 1024);
    }
    LGKM0();
    int xs = X_SLOT, ws = ((WS0 + 1) & (W_NS - 1)) * W_SLOT;
    int xa = aoff, wa = WS0 * W_SLOT + woff;
#define PM(kk, i) acc[i] = MFMA16(wf[kk][i], af[kk], acc[i]); SB()
#define PW(kk, i) wf[kk][i] = ldsr128(smem + wa + (kk * 5 + i) * 1024)
#pragma unroll 1
    for (int s = 0; s < NST; ++s) {
        PM(0, 0); PW(1, 4); af[1] = ldsr128(smem + xa + 2048); SB();
        xa = xs + aoff; wa = ws + woff;
        asm volatile("" : "+v"(xa), "+v"(wa));
        SB();
        PM(0, 1); BAR(); SB();
        PM(0, 2); PW(0, 0); PW(0, 1); SB();
        PM(0, 3); PW(0, 2); SB();
        PM(0, 4); PW(0, 3); SB();
        PM(1, 0); PW(0, 4); af[0] = ldsr128(smem + xa); SB();
        PM(1, 1); PW(1, 0); SB();
        xs = xs + X_SLOT == X_NS * X_SLOT ? 0 : xs + X_SLOT;
        ws = ws + W_SLOT == W_NS * W_SLOT ? 0 : ws + W_SLOT;
        asm volatile("" : "+s"(xs), "+s"(ws));
        PM(1, 2); PW(1, 1); SB();
        PM(1, 3); PW(1, 2); SB();
        PM(1, 4); PW(1, 3); SB();
    }
#undef PM
#undef PW
    STAMP(KID, 2);
    u32x2 o[5];
#pragma unroll
    for (int i = 0; i < 5; ++i) o[i] = u32x2{cvtpk(acc[i][0], acc[i][1]), cvtpk(acc[i][2], acc[i][3])};
    if (KTOT == 1536 && out != nullptr) {
        char* ob = out + ((size_t)(tm * 64 + w * 16) * 640 + tn * 80) * 2;
#pragma unroll
        for (int i = 0; i < 5; ++i) gstore2<false>(ob + i * 32, c16 * 1280 + g * 8, o[i]);
    } else {
#pragma unroll
        for (int i = 0; i < 5; ++i) gstore2<true>(xnp + i * 1024, tilv, o[i]);
        SB();
        f32x4 s1 = acc[0], s2 = acc[0] * acc[0];
#pragma unroll
        for (int i = 1; i < 5; ++i) { s1 += acc[i]; s2 += acc[i] * acc[i]; }
        const float t1 = xsum16_32((s1[0] + s1[1]) + (s1[2] + s1[3])), t2 = xsum16_32((s2[0] + s2[1]) + (s2[2] + s2[3]));
        gstore2<true>(stp, c16 * 8, u32x2{__float_as_uint(t1), __float_as_uint(t2)});
        STAMP(KID, 4);
        signal_flag(sigp, tag);
    }
    STAMP(KID, 3);
    BAR_L();
}

constexpr int WS_WO = 2, WS_GU = (WS_WO + 10) & 7, WS_WD = (WS_GU + 20) & 7, WS_QKV = (WS_WD + 24) & 7;

template <int NST, int WS0, class Cur, class Next>
__device__ __forceinline__ void w_phase(unsigned ring, const Cur& cur, const Next& next) {
    int slot = (WS0 + W_PD) & (W_NS - 1);
#pragma unroll 1
    for (int s = 0; s < NST - W_PD; ++s) {
        WAIT_V(40);
        BAR();
        cur(s + W_PD, ring + slot * W_SLOT);
        slot = (slot + 1) & (W_NS - 1);
    }
#pragma unroll 1
    for (int s = 0; s < W_PD; ++s) {
        WAIT_V(40);
        BAR();
        next(s, ring + slot * W_SLOT);
        slot = (slot + 1) & (W_NS - 1);
    }
    BAR(); BAR();
}
template <int NST, int WS0, int D, class Cur>
__device__ __forceinline__ void w_phase_last(unsigned ring, const Cur& cur) {
    int slot = (WS0 + W_PD) & (W_NS - 1);
#pragma unroll 1
    for (int s = 0; s < NST - W_PD; ++s) {
        WAIT_V(40);
        BAR();
        cur(s + W_PD, ring + slot * W_SLOT);
        slot = (slot + 1) & (W_NS - 1);
    }
#pragma unroll
    for (int r = W_PD - 1; r >= 0; --r) {
        WAIT_V(r * D < 40 ? r * D : 40);
        BAR();
    }
    BAR(); BAR();
}

__device__ __forceinline__ void loader_wave(char* smem, const Params& p, u64 tag) {
    const int lane = TID() & 63;
    const unsigned lbase = (unsigned)(uintptr_t)(lds_t)smem, ring = lbase + WR_OFF;
    const unsigned lv = lane * 16;
    __builtin_amdgcn_s_setprio(LPRIO);
    int ptm, ptn, gtm, gtn, qtm, qtn, head, qblk;
    const bool hasq = qkv_tile(qtm, qtn);
    const QkvW qk(p.wqkv, qtn);
    if (hasq) {
        ring_loader<QKV_NST, W_NS, 3, 8, false>([&](int s, int slot) { qk(s, ring + slot * W_SLOT); });
        BAR(); BAR(); BAR();
    }
    static_assert(WS_QKV == 0, "first QKV phase starts at slot 0");
    proj_tile(ptm, ptn);
    gu_tile(gtm, gtn);
    attn_tile(head, qblk);
    char* wt = p.ws + WS_WT + (size_t)ptn * 68 * 5120;
    const ProjW wo(wt), wd(wt + 20 * 5120);
    char* gt = p.ws + WS_GT + (size_t)gtn * 20 * 12288;
    const GuW gu(gt);
    unsigned pv[3];
    kv_piece_offs(head, pv);
    const unsigned fr = ((head * 20 + ((lane >> 2) & 1) * 19) >> 6) * 256 + (lane & 3) * 64;
    const unsigned fo = (((lane >> 3) & 1) + 1) * 2560 + fr;
    const unsigned fo0 = lane < 32 ? (lane >> 4) * 8192 + fo : qblk * 8192 + fr;
#pragma unroll 1
    for (int L = 0; L < 2; ++L) {
        char* fl = p.ws + WS_FLAGS + (size_t)L * FL_BYTES;
        const char* kv = p.ws + WS_L0 + (size_t)L * L_BYTES + L_KV;
        __builtin_amdgcn_s_sleep(POLL_PRE);
        wait_flags_at(fl + FL_Q, fo0, lane < 40, tag);
        BAR();
        int slot = 1;
#pragma unroll 1
        for (int c = 1; c < AT_NCH; ++c) {
            BAR();
#pragma unroll
            for (int i = 0; i < 8; ++i) glds3<true>(kv + (size_t)(c * 8 + i) * KV_REC, pv[0], pv[1], pv[2], lbase + slot * AT_SLOT + i * 3072);
            if (c + 1 < AT_NCH) wait_flags_at(fl + FL_Q + (c + 1) * 8192, fo, lane < 16, tag);
            WAIT_V(0);
            slot = slot + 1 == AT_NS ? 0 : slot + 1;
            if (L == 0) {
                constexpr int SCR = WR_OFF + 7 * W_SLOT;
                const int u = ptm + (c - 2) * 32;
                if (c >= 2 && c <= 4 && u < 68) {
                    const bool isd = u >= 20;
                    const int g = lane >> 4;
                    const int q0 = isd ? (g >> 1) * 4 + (g & 1) : g * 2, qd = isd ? 1536 : 768;
                    const char* rp = smem + SCR + (c & 1) * 6144 + q0 * 768 + ((lane >> 2) & 3) * 64 + (lane & 3) * 8;
#pragma unroll
                    for (int i = 0; i < 5; ++i) {
                        const int c2 = (ptn & 1) + i;
                        const char* r2 = rp + (c2 >> 1) * 256 + (c2 & 1) * 32;
                        const bf16x8 f = __builtin_shufflevector(ldstr(r2), ldstr(r2 + qd), 0, 1, 2, 3, 4, 5, 6, 7);
                        gstore4<true>(wt + u * 5120 + i * 1024, lv, __builtin_bit_cast(u32x4, f));
                    }
                }
                const int un = u + 32;
                if (c <= 3 && un < 68) {
                    const char* src = (un >= 20 ? p.wd + (size_t)(un - 20) * 32 * 1280 : p.wo + (size_t)un * 32 * 1280) + ((ptn * 80) & ~31) * 2;
                    unsigned wv[3];
#pragma unroll
                    for (int i = 0; i < 3; ++i) {
                        const int idx = i * 64 + lane, kq = idx / 48, rem = idx - kq * 48;
                        wv[i] = (kq * 4 + ((rem >> 2) & 3)) * 1280 + (rem >> 4) * 64 + (rem & 3) * 16 - i * 1024;
                    }
                    const unsigned d = lbase + SCR + ((c + 1) & 1) * 6144;
                    glds3(src, wv[0], wv[1], wv[2], d);
                    glds3(src + 16 * 1280, wv[0], wv[1], wv[2], d + 3072);
                }
                const int gs = gtm + (c - 2) * 16;
                if ((c == 2 || c == 3) && gs < 20) {
                    const char* rp = smem + WR_OFF + (8 - c) * W_SLOT + ((lane >> 4) & 1) * 6144 + (lane >> 5) * 1536 + ((lane >> 2) & 3) * 64 + (lane & 3) * 8;
#pragma unroll
                    for (int i = 0; i < 12; ++i) {
                        const int ks = i / 6, t = i % 6;
                        const bf16x8 f = ldstr2(rp + ks * 3072 + (t >> 1) * 256 + (t & 1) * 32, 768);
                        gstore4<true>(gt + gs * 12288 + i * 1024, lv, __builtin_bit_cast(u32x4, f));
                    }
                }
                if (c <= 2 && gs + 16 < 20) {
                    const GuNat gn(p.wg, p.wu, gtn);
                    gn(gs + 16, ring + (7 - c) * W_SLOT);
                }
                if (c == 5) {
                    signal_flag(fl + FL_P + (size_t)ptn * FL_GROUP + ptm * 64, tag);
                    signal_flag(fl + FL_T + (size_t)(gtn * 16 + gtm) * 64, tag);
                }
                if (c == 6) wait_flags_at(fl, lane < 32 ? FL_P + ptn * FL_GROUP + lane * 64 : (lane < 48 ? FL_T + (gtn * 16 + (lane & 15)) * 64 :
                                              FL_U + ((lane & 2) * 12 + ((gtn * 96 + (lane & 1) * 95) >> 6)) * 8), lane < 52, tag);
            }
            if (c == 7) {
#pragma unroll 1
                for (int s = 0; s < 3; ++s) wo(s, ring + (WS_WO + s) * W_SLOT);
            }
        }
        BAR();
#pragma unroll 1
        for (int s = 3; s < W_PD; ++s) wo(s, ring + (WS_WO + s) * W_SLOT);
        BAR();
        BAR();
        w_phase<10, WS_WO>(ring, wo, gu);
        glds1<true>(p.ws + WS_CSG + gtn * 768, lv, lbase + CS_OFF);
        w_phase<GU_NST, WS_GU>(ring, gu, wd);
        if (L == 0 && hasq) {
            w_phase<24, WS_WD>(ring, wd, qk);
            w_phase_last<QKV_NST, WS_QKV, 8>(ring, qk);
        } else {
            w_phase_last<24, WS_WD, 10>(ring, wd);
        }
    }
}

__global__ __launch_bounds__(576, 3) void k_main(Params p) {
    __shared__ __attribute__((aligned(1024))) char smem[LDS_BYTES];
    startup(smem, p);
    u64 tag;
    asm volatile("s_load_dwordx2 %0, %1, 0x0\n\ts_waitcnt lgkmcnt(0)" : "=s"(tag) : "s"(p.ws + WS_TAG) : "memory");
    tag = tag * 0x9E3779B97F4A7C15ull + 0x632BE59BD9B4E019ull;
    if (__builtin_amdgcn_readfirstlane(threadIdx.x >> 6) == 8) {
        loader_wave(smem, p, tag);
        asm volatile(".Lk_code_hi:");
        if (blockIdx.x == 0 && (threadIdx.x & 63) == 0) *(u64*)(p.ws + WS_TAG) = tag;
        return;
    }
    qkv_phase<0, WS_QKV>(smem, p, nullptr, nullptr, p.ws + WS_L0 + L_Q, p.ws + WS_L0 + L_KV, nullptr, p.ws + WS_FLAGS + FL_Q, tag);
#pragma unroll 1
    for (int L = 0; L < 2; ++L) {
        char* lw = p.ws + WS_L0 + (size_t)L * L_BYTES;
        char* fl = p.ws + WS_FLAGS + (size_t)L * FL_BYTES;
#define OPQ() asm volatile("" : "+s"(lw), "+s"(fl), "+s"(p.ws), "+s"(p.x0))
        OPQ();
        attn_phase(smem, lw + L_Q, lw + L_KV, lw + L_O, fl + FL_A, tag, L);
        OPQ();
        proj_phase<640, WS_WO>(smem, lw + L_O, L ? lw + L_X0 : p.x0, lw + L_X1, lw + L_SB, nullptr, fl + FL_A, fl + FL_W, tag, L);
        OPQ();
        gu_phase<WS_GU>(smem, lw + L_X1, lw + L_SB, lw + L_H, fl + FL_W, fl + FL_G, tag, L);
        OPQ();
        proj_phase<1536, WS_WD>(smem, lw + L_H, lw + L_X1, lw + L_BYTES + L_X0, lw + L_BYTES + L_SA, L ? p.out : nullptr, fl + FL_G,
                                fl + FL_BYTES + FL_D, tag, L);
        if (L) break;
        lw += L_BYTES; fl += FL_BYTES;
        OPQ();
        qkv_phase<1, WS_QKV>(smem, p, lw + L_X0, lw + L_SA, lw + L_Q, lw + L_KV, fl + FL_D, fl + FL_Q, tag);
    }
}

extern "C" void kernel_launch(void* const* d_in, const int* in_sizes, int n_in, void* d_out, int out_size, void* d_ws, size_t ws_size,
                              hipStream_t stream) {
    Params p;
    p.x0 = (const char*)d_in[0];
    p.wqkv = (const char*)d_in[1];
    p.wo = (const char*)d_in[2];
    p.wg = (const char*)d_in[4];
    p.wu = (const char*)d_in[5];
    p.wd = (const char*)d_in[6];
    p.ws = (char*)d_ws;
    p.out = (char*)d_out;
    k_main<<<256, 576, 0, stream>>>(p);
}
```
